```python
import torch

def _softmax(x, axis=-1):
    return torch.softmax(x, dim=axis)
def _gelu(x):
    return torch.nn.functional.gelu(x, approximate="tanh")
def _silu(x):
    return torch.nn.functional.silu(x)
def _sigmoid(x):
    return torch.sigmoid(x)
def _layernorm(x, axis=-1, eps=1e-5):
    m = x.mean(dim=axis, keepdim=True)
    v = x.var(dim=axis, keepdim=True, unbiased=False)
    return (x - m) / torch.sqrt(v + eps)
def _scatter_add(tgt, idx, src, axis=0):
    return tgt.clone().index_add_(axis, idx, src)
def _segment_sum(src, seg, n):
    out = torch.zeros((n,) + src.shape[1:], dtype=src.dtype)
    return out.index_add_(0, seg, src)
def _pad(x, pw):
    flat = tuple(v for pair in reversed(pw) for v in pair)
    return torch.nn.functional.pad(x, flat)

kernel_name = 'sequential_preLN__linear_attn_block__dense_activation_relu__prompt'

def setup_inputs():

    _w = (lambda a: (lambda t: t.to(torch.bfloat16) if t.is_floating_point() else t)(torch.as_tensor(a)))
    def _A_setup():
        B, S, D = 1, 2048, 640
        return {
            "x": _w((lambda *s: torch.randn(*s, dtype=torch.bfloat16) * 0.05)(B, S, D)),
            "wqkv": _w((lambda *s: torch.randn(*s, dtype=torch.bfloat16) * 0.05)(D, 3 * D)),
            "wo": _w((lambda *s: torch.randn(*s, dtype=torch.bfloat16) * 0.05)(D, D)),
        }
    _A_in = _A_setup()

    def _B_setup():
        B, S, D, Dff = 1, 2048, 640, 1728
        return {
            "x": _w((lambda *s: torch.randn(*s, dtype=torch.bfloat16) * 0.05)(B, S, D)),
            "w1": _w((lambda *s: torch.randn(*s, dtype=torch.bfloat16) * 0.05)(D, Dff)),
            "w2": _w((lambda *s: torch.randn(*s, dtype=torch.bfloat16) * 0.05)(Dff, D)),
        }
    _B_in = _B_setup()

    return {
        "A_x": _A_in["x"],
        "A_wqkv": _A_in["wqkv"],
        "A_wo": _A_in["wo"],
        "B_x": _B_in["x"],
        "B_w1": _B_in["w1"],
        "B_w2": _B_in["w2"],
    }

def reference(A_x, A_wqkv, A_wo, B_x, B_w1, B_w2):
    def _A_ref(x, wqkv, wo):
        B, S, D = x.shape
        H = 32; Dh = D // H
        qkv = (lambda x, *d: x.permute(*d))((x @ wqkv).reshape(B, S, 3, H, Dh), 2, 0, 3, 1, 4)
        q, k, v = qkv[0], qkv[1], qkv[2]
        phi = lambda t: torch.where(t > 0, t + 1, torch.exp(t)) + 1e-6
        pq, pk = phi(q), phi(k)
        kv = torch.einsum("bhsd,bhse->bhde", pk, v)
        z = torch.einsum("bhsd->bhd", pk)
        num = torch.einsum("bhqd,bhde->bhqe", pq, kv)
        den = torch.einsum("bhqd,bhd->bhq", pq, z)[..., None] + 1e-6
        o = num / den
        return (lambda x, *d: x.permute(*d))(o, 0, 2, 1, 3).reshape(B, S, D) @ wo

    def _B_ref(x, w1, w2):
        h = torch.einsum("bsd,df->bsf", x, w1)
        zero = torch.zeros_like(h)
        h = torch.maximum(h, zero)
        return torch.einsum("bsf,fd->bsd", h, w2)

    x = A_x
    h = x + _A_ref(_layernorm(x, axis=-1), A_wqkv, A_wo)
    h2 = h + _B_ref(_layernorm(h, axis=-1), B_w1, B_w2)
    return h2
```

```cpp
#include <hip/hip_runtime.h>
#include <hip/hip_bf16.h>
#include <stdint.h>

typedef __attribute__((ext_vector_type(8))) short bf16x8;
typedef __attribute__((ext_vector_type(4))) float f32x4;
typedef unsigned short u16;

static constexpr int S = 2048, D = 640, H = 32, DH = 20, DFF = 1728;

__device__ __forceinline__ float bf2f(u16 v) { return __uint_as_float(((unsigned)v) << 16); }
__device__ __forceinline__ u16 f2bf(float f) { __hip_bfloat16 b = __float2bfloat16(f); return *reinterpret_cast<u16*>(&b); }
__device__ __forceinline__ float rbf(float f) { return bf2f(f2bf(f)); }

__global__ void ln_kernel(const u16* __restrict__ x, u16* __restrict__ y) {
    int row = blockIdx.x * 4 + (threadIdx.x >> 6);
    int lane = threadIdx.x & 63;
    float v[10];
    float s = 0.f;
    for (int i = 0; i < 10; ++i) { v[i] = bf2f(x[row * D + lane + 64 * i]); s += v[i]; }
    for (int m = 32; m >= 1; m >>= 1) s += __shfl_xor(s, m);
    float mean = s / D;
    float q = 0.f;
    for (int i = 0; i < 10; ++i) { float d = v[i] - mean; q += d * d; }
    for (int m = 32; m >= 1; m >>= 1) q += __shfl_xor(q, m);
    float var = rbf(q / D);
    float mb = rbf(mean);
    float den = rbf(sqrtf(rbf(var + 1e-5f)));
    for (int i = 0; i < 10; ++i) y[row * D + lane + 64 * i] = f2bf(rbf(v[i] - mb) / den);
}

template <int EPI>
__global__ void gemm_kernel(const u16* __restrict__ A, const u16* __restrict__ B, u16* __restrict__ C,
                            const u16* __restrict__ res, int M, int N, int K) {
    __shared__ u16 sA[64][40];
    __shared__ u16 sB[64][40];
    int bm = blockIdx.y * 64, bn = blockIdx.x * 64;
    int tid = threadIdx.x, lane = tid & 63, wid = tid >> 6;
    int wr = wid >> 1, wc = wid & 1;
    f32x4 acc[2][2] = {};
    for (int k0 = 0; k0 < K; k0 += 32) {
        for (int e = tid; e < 64 * 32; e += 256) {
            int r = e / 32, c = e % 32;
            sA[r][c] = A[(size_t)(bm + r) * K + k0 + c];
        }
        for (int e = tid; e < 64 * 32; e += 256) {
            int kk = e / 64, n = e % 64;
            sB[n][kk] = B[(size_t)(k0 + kk) * N + bn + n];
        }
        __syncthreads();
        for (int m = 0; m < 2; ++m)
            for (int n = 0; n < 2; ++n) {
                bf16x8 a, b;
                for (int j = 0; j < 8; ++j) {
                    a[j] = (short)sA[wr * 32 + m * 16 + (lane & 15)][(lane >> 4) * 8 + j];
                    b[j] = (short)sB[wc * 32 + n * 16 + (lane & 15)][(lane >> 4) * 8 + j];
                }
                acc[m][n] = __builtin_amdgcn_mfma_f32_16x16x32_bf16(a, b, acc[m][n], 0, 0, 0);
            }
        __syncthreads();
    }
    for (int m = 0; m < 2; ++m)
        for (int n = 0; n < 2; ++n)
            for (int j = 0; j < 4; ++j) {
                int r = bm + wr * 32 + m * 16 + (lane >> 4) * 4 + j;
                int c = bn + wc * 32 + n * 16 + (lane & 15);
                float v = acc[m][n][j];
                if (EPI == 1) v = fmaxf(v, 0.f);
                if (EPI == 2) v = rbf(v) + bf2f(res[(size_t)r * N + c]);
                C[(size_t)r * N + c] = f2bf(v);
            }
}

__device__ __forceinline__ float phi(float t) {
    float r = t > 0.f ? rbf(t + 1.f) : rbf(expf(t));
    return rbf(r + 1e-6f);
}

__global__ void kv_kernel(const u16* __restrict__ qkv, float* __restrict__ kv, float* __restrict__ z) {
    int h = blockIdx.x;
    int tid = threadIdx.x;
    float acc = 0.f;
    if (tid < 400) {
        int d = tid / 20, e = tid % 20;
        for (int s = 0; s < S; ++s) {
            float pk = phi(bf2f(qkv[(size_t)s * 1920 + 640 + h * 20 + d]));
            float v = bf2f(qkv[(size_t)s * 1920 + 1280 + h * 20 + e]);
            acc += pk * v;
        }
        kv[h * 400 + tid] = rbf(acc);
    } else if (tid < 420) {
        int d = tid - 400;
        for (int s = 0; s < S; ++s) acc += phi(bf2f(qkv[(size_t)s * 1920 + 640 + h * 20 + d]));
        z[h * 20 + d] = rbf(acc);
    }
}

__global__ void o_kernel(const u16* __restrict__ qkv, const float* __restrict__ kv, const float* __restrict__ z,
                         u16* __restrict__ o) {
    int s = blockIdx.x;
    int c = threadIdx.x;
    int h = c / 20, e = c % 20;
    float num = 0.f, den = 0.f;
    for (int d = 0; d < 20; ++d) {
        float pq = phi(bf2f(qkv[(size_t)s * 1920 + h * 20 + d]));
        num += pq * kv[h * 400 + d * 20 + e];
        den += pq * z[h * 20 + d];
    }
    num = rbf(num);
    den = rbf(rbf(den) + 1e-6f);
    o[(size_t)s * D + c] = f2bf(num / den);
}

extern "C" void kernel_launch(void* const* d_in, const int* in_sizes, int n_in, void* d_out, int out_size,
                              void* d_ws, size_t ws_size, hipStream_t stream) {
    const u16* x = (const u16*)d_in[0];
    const u16* wqkv = (const u16*)d_in[1];
    const u16* wo = (const u16*)d_in[2];
    const u16* w1 = (const u16*)d_in[4];
    const u16* w2 = (const u16*)d_in[5];
    u16* out = (u16*)d_out;
    char* ws = (char*)d_ws;
    u16* ln1 = (u16*)(ws);
    u16* qkv = (u16*)(ws + (4 << 20));
    float* kv = (float*)(ws + (16 << 20));
    float* z = (float*)(ws + (17 << 20));
    u16* o = (u16*)(ws + (20 << 20));
    u16* h = (u16*)(ws + (24 << 20));
    u16* ln2 = (u16*)(ws + (28 << 20));
    u16* h1 = (u16*)(ws + (32 << 20));
    ln_kernel<<<S / 4, 256, 0, stream>>>(x, ln1);
    gemm_kernel<0><<<dim3(1920 / 64, S / 64), 256, 0, stream>>>(ln1, wqkv, qkv, nullptr, S, 1920, D);
    kv_kernel<<<H, 448, 0, stream>>>(qkv, kv, z);
    o_kernel<<<S, 640, 0, stream>>>(qkv, kv, z, o);
    gemm_kernel<2><<<dim3(D / 64, S / 64), 256, 0, stream>>>(o, wo, h, x, S, D, D);
    ln_kernel<<<S / 4, 256, 0, stream>>>(h, ln2);
    gemm_kernel<1><<<dim3(DFF / 64, S / 64), 256, 0, stream>>>(ln2, w1, h1, nullptr, S, DFF, D);
    gemm_kernel<2><<<dim3(D / 64, S / 64), 256, 0, stream>>>(h1, w2, out, h, S, D, DFF);
}
```

```cpp
#include <hip/hip_runtime.h>
#include <stdint.h>
#pragma clang diagnostic ignored "-Wint-to-pointer-cast"

typedef short s16x4 __attribute__((ext_vector_type(4)));
typedef short s16x8 __attribute__((ext_vector_type(8)));
typedef float f32x4 __attribute__((ext_vector_type(4)));
typedef unsigned u32x4 __attribute__((ext_vector_type(4)));
typedef unsigned u32x2 __attribute__((ext_vector_type(2)));
typedef unsigned long long u64;
#define LAS __attribute__((address_space(3)))
#define GAS __attribute__((address_space(1)))
#define DI __device__ __forceinline__
#define RLX_AGENT __ATOMIC_RELAXED, __HIP_MEMORY_SCOPE_AGENT
#define SB() __builtin_amdgcn_sched_barrier(0)

constexpr size_t WS_FLAG = 4096;
constexpr size_t WS_KVP = 262144;
constexpr size_t WS_STAT2 = 0x120000;
constexpr size_t WS_O = 2u << 20;
constexpr size_t WS_H = 5u << 20;
constexpr size_t WS_H1 = 8u << 20;
constexpr size_t WS_W1R = 16u << 20;
constexpr size_t WS_W2R = 19u << 20;
constexpr size_t WS_STAMP = 24u << 20;

constexpr int L_A = 0;
constexpr int L_B = 102400;
constexpr int L_XRES = L_B + 40960;
constexpr int L_M = 153600;
constexpr int L_MEAN = L_M;
constexpr int L_RSTD = L_M + 512;
constexpr int L_CSKV = L_M + 1024;
constexpr int L_CS4 = L_M + 1280;
constexpr int L_KVT = L_M + 1536;
constexpr int L_CS1 = L_M + 3328;
constexpr int L_JUNK = L_M + 5632;
constexpr int LDS_BYTES = 163840;
constexpr int CODE_PF = 32768;
constexpr int CODE_BACK = 0;

enum { OP_X, OP_W, OP_R, OP_O };
struct P1Op { int kind, a, b, ks, slot; };
constexpr P1Op P1SEQ[] = {
    {OP_X, 0, 0, -1, 0}, {OP_X, 0, 1, -1, 0}, {OP_X, 0, 2, -1, 0}, {OP_X, 0, 3, -1, 0},
    {OP_W, 0, 0, -1, 0}, {OP_W, 0, 1, -1, 0}, {OP_W, 0, 2, -1, 0}, {OP_W, 0, 3, -1, 0}, {OP_W, 0, 4, -1, 0},
    {OP_X, 1, 0, -1, 0}, {OP_X, 1, 1, -1, 0}, {OP_X, 1, 2, -1, 0}, {OP_X, 1, 3, -1, 0},
    {OP_O, 0, 0, -1, 0}, {OP_O, 1, 0, -1, 0},
    {OP_W, 1, 0, -1, 0}, {OP_W, 1, 1, -1, 0}, {OP_W, 1, 2, -1, 0}, {OP_W, 1, 3, -1, 0}, {OP_W, 1, 4, -1, 0},
    {OP_X, 2, 0, -1, 0}, {OP_X, 2, 1, -1, 0}, {OP_X, 2, 2, -1, 0}, {OP_X, 2, 3, -1, 0},
    {OP_W, 2, 0, 0, 3}, {OP_W, 2, 1, 0, 8}, {OP_W, 2, 2, 0, 13},
    {OP_X, 3, 0, 1, 2}, {OP_X, 3, 1, 1, 6}, {OP_X, 3, 2, 1, 10}, {OP_X, 3, 3, 1, 14},
    {OP_W, 2, 3, 2, 3}, {OP_W, 2, 4, 2, 8}, {OP_W, 3, 0, 2, 13},
    {OP_X, 4, 0, 3, 2}, {OP_X, 4, 1, 3, 6}, {OP_X, 4, 2, 3, 10}, {OP_X, 4, 3, 3, 14},
    {OP_W, 3, 1, 4, 3}, {OP_W, 3, 2, 4, 8}, {OP_W, 3, 3, 4, 13},
    {OP_X, 5, 0, 5, 2}, {OP_X, 5, 1, 5, 6}, {OP_X, 5, 2, 5, 10}, {OP_X, 5, 3, 5, 14},
    {OP_W, 3, 4, 6, 3}, {OP_W, 4, 0, 6, 8}, {OP_W, 4, 1, 6, 13},
    {OP_X, 6, 0, 7, 2}, {OP_X, 6, 1, 7, 6}, {OP_X, 6, 2, 7, 10}, {OP_X, 6, 3, 7, 14},
    {OP_W, 4, 2, 8, 3}, {OP_W, 4, 3, 8, 8}, {OP_W, 4, 4, 8, 13},
    {OP_X, 7, 0, 9, 2}, {OP_X, 7, 1, 9, 6}, {OP_X, 7, 2, 9, 10}, {OP_X, 7, 3, 9, 14},
    {OP_R, 0, 0, 10, 3}, {OP_R, 1, 0, 10, 8}, {OP_R, 2, 0, 10, 13},
    {OP_X, 8, 0, 11, 2}, {OP_X, 8, 1, 11, 6}, {OP_X, 8, 2, 11, 10}, {OP_X, 8, 3, 11, 14},
    {OP_R, 3, 0, 12, 3}, {OP_R, 4, 0, 12, 8},
    {OP_X, 9, 0, 13, 2}, {OP_X, 9, 1, 13, 6}, {OP_X, 9, 2, 13, 10}, {OP_X, 9, 3, 13, 14},
};
constexpr int P1N = sizeof(P1SEQ) / sizeof(P1SEQ[0]);
constexpr int p1_wait(int s) {
    int issued = 0, need = 0;
    for (int i = 0; i < P1N; ++i) {
        if (P1SEQ[i].ks < 2 * s - 1 || P1SEQ[i].ks < 0) issued = i + 1;
        if ((P1SEQ[i].kind == OP_X && P1SEQ[i].a == s) || (P1SEQ[i].kind == OP_W && P1SEQ[i].a <= (s >> 1))) need = need > i + 1 ? need : i + 1;
    }
    return issued - need;
}
constexpr bool p1_ok() { for (int s = 0; s < 10; ++s) if (p1_wait(s) < 0 || p1_wait(s) > 63) return false; return true; }
static_assert(p1_ok(), "P1a schedule: an op is waited for before it is issued");

#define STAMP(i) do {} while (0)

template <int I> struct IC { static constexpr int v = I; };
template <int B, int E, class F> DI void sfor(F&& f) { if constexpr (B < E) { f(IC<B>{}); sfor<B + 1, E>(f); } }
#define OPAQUE_S(p) asm volatile("" : "+s"(p))

DI f32x4 mfma(s16x8 a, s16x8 b, f32x4 c) { return __builtin_amdgcn_mfma_f32_16x16x32_bf16(a, b, c, 0, 0, 0); }
DI s16x4 trread(unsigned addr) { return __builtin_bit_cast(s16x4, __builtin_amdgcn_ds_read_tr16_b64_v4i16((LAS s16x4*)addr)); }
DI s16x8 cat(s16x4 a, s16x4 b) { return __builtin_shufflevector(a, b, 0, 1, 2, 3, 4, 5, 6, 7); }
DI unsigned pk2(float lo, float hi) {
    typedef __bf16 bf2 __attribute__((ext_vector_type(2)));
    typedef float f2 __attribute__((ext_vector_type(2)));
    f2 v = {lo, hi};
    return __builtin_bit_cast(unsigned, __builtin_convertvector(v, bf2));
}
DI s16x8 pack8(f32x4 a, f32x4 b) {
    u32x4 r = {pk2(a[0], a[1]), pk2(a[2], a[3]), pk2(b[0], b[1]), pk2(b[2], b[3])};
    return __builtin_bit_cast(s16x8, r);
}
DI u32x2 pack4(f32x4 a) { u32x2 r = {pk2(a[0], a[1]), pk2(a[2], a[3])}; return r; }
DI float bflo(unsigned u) { return __uint_as_float(u << 16); }
DI float bfhi(unsigned u) { return __uint_as_float(u & 0xffff0000u); }
DI float phi(float t) { return (t > 0.f ? t + 1.f : __expf(t)) + 1e-6f; }

template <int LOFF> DI void glds16(unsigned voff, const void* sbase, unsigned lds) {
    asm volatile("s_add_u32 m0, %2, %3\n\ts_nop 0\n\tglobal_load_lds_dwordx4 %0, %1" ::"v"(voff), "s"(sbase), "s"(lds), "i"(LOFF) : "memory", "scc");
}
template <int LOFF, int GOFF> DI void glds16i(unsigned voff, const void* sbase, unsigned lds) {
    asm volatile("s_add_u32 m0, %2, %3\n\ts_nop 0\n\tglobal_load_lds_dwordx4 %0, %1 offset:%4" ::"v"(voff), "s"(sbase), "s"(lds), "i"(LOFF - GOFF), "i"(GOFF) : "memory", "scc");
}
template <int LOFF> DI void glds16c(unsigned voff, const void* sbase, unsigned lds) {
    asm volatile("s_add_u32 m0, %2, %3\n\ts_nop 0\n\tglobal_load_lds_dwordx4 %0, %1 sc1" ::"v"(voff), "s"(sbase), "s"(lds), "i"(LOFF) : "memory", "scc");
}
DI void glds4(unsigned voff, const void* sbase, unsigned lds) {
    asm volatile("s_mov_b32 m0, %2\n\ts_nop 0\n\tglobal_load_lds_dword %0, %1" ::"v"(voff), "s"(sbase), "s"(lds) : "memory");
}
#define WAITV(n) asm volatile("s_waitcnt vmcnt(%0)" ::"i"(n) : "memory")
#define BARX() do { __builtin_amdgcn_s_barrier(); asm volatile("" ::: "memory"); } while (0)
#define BAR() do { asm volatile("s_waitcnt lgkmcnt(0)" ::: "memory"); __builtin_amdgcn_s_barrier(); asm volatile("" ::: "memory"); } while (0)

DI void ld16_sc1(s16x8& d, unsigned voff, const void* sbase) {
    asm volatile("global_load_dwordx4 %0, %1, %2 sc1" : "=v"(d) : "v"(voff), "s"(sbase) : "memory");
}
DI void ld16a_sc1(s16x8& d, unsigned voff, const void* sbase) {
    asm volatile("global_load_dwordx4 %0, %1, %2 sc1" : "=a"(d) : "v"(voff), "s"(sbase) : "memory");
}
DI void ld8_sc1(u64& d, const void* sbase) { asm volatile("global_load_dwordx2 %0, %1, %2 sc1" : "=v"(d) : "v"(0u), "s"(sbase) : "memory"); }
DI void st16_sc1(unsigned voff, void* sbase, u32x4 v) {
    asm volatile("global_store_dwordx4 %0, %1, %2 sc1\n\ts_nop 1" ::"v"(voff), "v"(v), "s"(sbase) : "memory");
}
DI void st8_sc1(unsigned voff, void* sbase, u32x2 v) {
    asm volatile("global_store_dwordx2 %0, %1, %2 sc1" ::"v"(voff), "v"(v), "s"(sbase) : "memory");
}
DI void drain_bar() { WAITV(0); BAR(); }
DI void flags_store(char* ws, int arr, int line, int slot, u64 tag) {
    __hip_atomic_store((GAS u64*)(ws + WS_FLAG + (size_t)arr * 32768 + (size_t)line * 128) + slot, tag, RLX_AGENT);
}
template <int NV> DI void await(char* ws, int arr, int line, u64 tag, int w, int lane) {
    if (w == 0) {
        GAS u64* fl = (GAS u64*)(ws + WS_FLAG + (size_t)arr * 32768 + (size_t)line * 128) + (lane & 15);
        for (unsigned spins = 0; spins < (1u << 20); ++spins) {
            u64 f = __hip_atomic_load(fl, RLX_AGENT);
            if (__all(f == tag)) break;
        }
    }
    WAITV(NV);
    BAR();
}

__global__ void __launch_bounds__(256, 1)
fused_block(const char* __restrict__ x, const char* __restrict__ wqkv, const char* __restrict__ wo, const char* __restrict__ w1,
            const char* __restrict__ w2, char* __restrict__ out, char* __restrict__ ws) {
    __shared__ __attribute__((aligned(16))) char smem[LDS_BYTES];
    const unsigned lb = (unsigned)(uintptr_t)(LAS char*)smem;
    const int tid = threadIdx.x, lane = tid & 63;
    const int w = __builtin_amdgcn_readfirstlane(tid >> 6);
    const int b = blockIdx.x, x8 = b & 7, rr = b >> 3;
    const int i1 = 4 * (x8 >> 1) + (rr >> 3), j1 = 8 * (x8 & 1) + (rr & 7);
    const int i2 = 2 * x8 + (rr >> 4), j2 = rr & 15;
    const int g = lane >> 4, n = lane & 15, q4 = n >> 2, p4 = lane & 3;
    const s16x8 ones = {0x3F80, 0x3F80, 0x3F80, 0x3F80, 0x3F80, 0x3F80, 0x3F80, 0x3F80};
    const f32x4 z4 = {0, 0, 0, 0};
    STAMP(0);

    s16x8 X[20][2];
    f32x4 acc[5][2], sq[2], rs[2], csA, csB;
    unsigned voffW[5], voffX[4];
    const char* xb = x + (size_t)i1 * 128 * 1280;
    const unsigned ldsXw = lb + L_B + w * 4096, ldsWw = lb + L_A + w * 5120;
#pragma unroll
    for (int u = 0; u < 4; ++u)
        voffX[u] = (32 * w + 8 * u + (lane >> 3)) * 1280 + 16 * ((lane & 7) ^ ((lane >> 4) + 4 * (u & 1)));
#define ISSUE_X(s, u) glds16i<((s) % 3) * 16384 + (u) * 1024, (s) * 128>(voffX[u], xb, ldsXw)
#define ISSUE_XS(s) do { ISSUE_X(s, 0); ISSUE_X(s, 1); ISSUE_X(s, 2); ISSUE_X(s, 3); } while (0)
#pragma unroll
    for (int u = 0; u < 5; ++u) {
        int q = 64 * u + lane, row = q / 10, s = q - row * 10;
        row ^= ((row >> 3) & 1) << 2;
        voffW[u] = row * 3840 + (s < 5 ? (640 + 40 * j1) * 2 + 16 * s : (1280 + 40 * j1) * 2 + 16 * (s - 5));
    }
    const char* wkb = wqkv + (size_t)w * (32 * 3840);
    const char* wrun = wkb;
#define ISSUE_W(t, u) do { glds16<(t) * 20480 + (u) * 1024>(voffW[u], wrun, ldsWw); if ((u) == 4) { wrun += 128 * 3840; OPAQUE_S(wrun); } } while (0)
    const char* rsrc[5];
    int rpitch[5];
    unsigned rdst[5];
    u64 epoch;
    auto p1issue = [&](auto N) {
        constexpr P1Op op = P1SEQ[N.v];
        if constexpr (op.kind == OP_X) ISSUE_X(op.a, op.b);
        else if constexpr (op.kind == OP_W) ISSUE_W(op.a, op.b);
        else if constexpr (op.kind == OP_R) {
            const int rrow = 8 * w + (lane >> 3);
            glds16<op.a * 4096>((unsigned)(rrow * rpitch[op.a]) + 16 * ((lane & 7) ^ (((rrow >> 1) & 3) << 1)), rsrc[op.a], lb + L_A + w * 1024);
        } else if constexpr (op.a == 0) {
            u64 pc;
            asm volatile("s_getpc_b64 %0" : "=s"(pc));
            pc &= ~(u64)127;
            glds4((unsigned)(((rr * 4 + w) * (CODE_PF / 128 / 128) + (lane % (CODE_PF / 128 / 128))) * 128), (const void*)(pc - CODE_BACK), lb + L_JUNK);
        } else ld8_sc1(epoch, ws);
    };
    sfor<0, P1N>([&](auto N) { if constexpr (P1SEQ[N.v].ks < 0) p1issue(N); });
#pragma unroll
    for (int r = 0; r < 5; ++r) {
        const int u = (r < 4 || b < 56) ? b + 256 * r : b + 768;
        const bool is1 = u < 540;
        const int v = is1 ? u : u - 540, ksr = is1 ? v / 27 : v / 10, cl = is1 ? v - ksr * 27 : v - ksr * 10;
        rpitch[r] = is1 ? 3456 : 1280;
        rsrc[r] = (is1 ? w1 : w2) + (size_t)ksr * 32 * rpitch[r] + cl * 128;
        const int T = 4 * cl + w, c8r = T / 5;
        rdst[r] = is1 ? (unsigned)(WS_W1R + (size_t)(T * 20 + ksr) * 1024) : (unsigned)(WS_W2R + (size_t)((c8r * 54 + ksr) * 5 + (T - 5 * c8r)) * 1024);
    }
    const int ro1 = 8 * g + q4 + 4 * (g & 1), ro2 = 8 * g + q4 + 4 * (1 - (g & 1));
    {
        unsigned vW1[2], vW2[2], vM1[2], vM2[2], vC1[2], vC2[2];
        const int mo = (p4 == 0 ? 32 : p4 == 1 ? 72 : p4 == 2 ? 112 : 152);
#pragma unroll
        for (int h = 0; h < 2; ++h) {
            vW1[h] = lb + L_A + h * 51200 + ro1 * 160 + 8 * p4; vW2[h] = lb + L_A + h * 51200 + ro2 * 160 + 8 * p4;
            vM1[h] = lb + L_A + h * 51200 + ro1 * 160 + mo; vM2[h] = lb + L_A + h * 51200 + ro2 * 160 + mo;
            vC1[h] = vW1[h] + 40 * w; vC2[h] = vW2[h] + 40 * w;
            asm volatile("" : "+v"(vW1[h]), "+v"(vW2[h]), "+v"(vM1[h]), "+v"(vM2[h]), "+v"(vC1[h]), "+v"(vC2[h]));
        }
        unsigned vD1 = vM1[0] + w * 5120, vD2 = vM2[0] + w * 5120;
        unsigned vX[2];
#pragma unroll
        for (int kl = 0; kl < 2; ++kl) vX[kl] = lb + L_B + w * 4096 + n * 128 + 16 * ((4 * kl + g) ^ (n >> 1));
#pragma unroll
        for (int t = 0; t < 5; ++t) acc[t][0] = acc[t][1] = z4;
        sq[0] = sq[1] = rs[0] = rs[1] = csA = csB = z4;
        s16x4 Wl[2][7], Wh[2][7];
        auto side = [&](auto KS, auto I) {
            constexpr int ks = KS.v, i = I.v, r = ks & 1, s = ks >> 1, kl = ks & 1, h = ks >= 10;
            constexpr unsigned ko = (ks - 10 * h) * 5120;
            if constexpr (i < 2) X[ks][i] = *(volatile LAS s16x8*)(vX[kl] + (s % 3) * 16384 + i * 2048);
            else if constexpr (i < 10) { constexpr int t = (i - 2) >> 1; if constexpr (i & 1) Wh[r][t] = trread(vW2[h] + ko + 40 * t); else Wl[r][t] = trread(vW1[h] + ko + 40 * t); }
            else if constexpr (i < 12) { if constexpr (i & 1) Wh[r][4] = trread(vM2[h] + ko); else Wl[r][4] = trread(vM1[h] + ko); }
            else if constexpr (i < 14) { if constexpr (i & 1) Wh[r][5] = trread(vC2[h] + ko); else Wl[r][5] = trread(vC1[h] + ko); }
            else {
                constexpr int u = ks >> 2;
                if constexpr (i & 1) { Wh[r][6] = trread(vD2 + (u % 3) * 20480); if constexpr (u == 2) { vD2 += 61440; asm volatile("" : "+v"(vD2)); } }
                else { Wl[r][6] = trread(vD1 + (u % 3) * 20480); if constexpr (u == 2) { vD1 += 61440; asm volatile("" : "+v"(vD1)); } }
            }
        };
        WAITV(p1_wait(0));
        BARX();
        sfor<0, 14>([&](auto I) { side(IC<0>{}, I); });
        SB();
        sfor<0, 20>([&](auto KS) {
            constexpr int ks = KS.v;
            if constexpr ((ks & 1) && ks < 19) {
                constexpr int s = (ks + 1) >> 1;
                WAITV(p1_wait(s));
                if constexpr ((s & 1) == 0) BARX();
                SB();
            }
            constexpr int r = ks & 1;
            constexpr int nm = ((ks & 3) == 3) ? 16 : 15;
            constexpr int nside = ks < 19 ? (((ks + 1) & 3) == 3 ? 16 : 14) : 0;
            sfor<0, 16>([&](auto I) {
                constexpr int i = I.v;
                if constexpr (i < nm) {
                    if constexpr (i < 10) { constexpr int t = i >> 1, m = i & 1; acc[t][m] = mfma(X[ks][m], cat(Wl[r][t], Wh[r][t]), acc[t][m]); }
                    else if constexpr (i == 10) csA = mfma(ones, cat(Wl[r][5], Wh[r][5]), csA);
                    else if constexpr (i < 13) sq[i - 11] = mfma(X[ks][i - 11], X[ks][i - 11], sq[i - 11]);
                    else if constexpr (i < 15) rs[i - 13] = mfma(X[ks][i - 13], ones, rs[i - 13]);
                    else csB = mfma(ones, cat(Wl[r][6], Wh[r][6]), csB);
                }
                if constexpr (i < nside) side(IC<ks + 1>{}, I);
                sfor<0, P1N>([&](auto N) { if constexpr (P1SEQ[N.v].ks == ks && P1SEQ[N.v].slot == i) p1issue(N); });
                SB();
            });
        });
    }
    asm volatile("" : "+v"(epoch), "+v"(csA), "+v"(csB));
    const u64 tag = epoch + 1;
    STAMP(1);

    {
        LAS float* meanp = (LAS float*)(lb + L_MEAN);
        LAS float* rstdp = (LAS float*)(lb + L_RSTD);
#pragma unroll
        for (int m = 0; m < 2; ++m) {
            const int r = n & 3;
            float su = r == 0 ? rs[m][0] : r == 1 ? rs[m][1] : r == 2 ? rs[m][2] : rs[m][3];
            float s2 = r == 0 ? sq[m][0] : r == 1 ? sq[m][1] : r == 2 ? sq[m][2] : sq[m][3];
            float mean = su * (1.f / 640.f);
            float var = fmaxf(s2 * (1.f / 640.f) - mean * mean, 0.f);
            float rstd = __builtin_amdgcn_rsqf(var + 1e-5f);
            if (g == q4) { meanp[32 * w + 16 * m + n] = mean; rstdp[32 * w + 16 * m + n] = rstd; }
        }
        if (g == 0) { ((LAS float*)(lb + L_CSKV))[16 * w + n] = csA[0]; ((LAS float*)(lb + L_CS4))[16 * w + n] = csB[0]; }
    }
    unsigned voffQ[5];
#pragma unroll
    for (int u = 0; u < 5; ++u) {
        int q = 64 * u + lane, row = q / 5, s = q - row * 5;
        row ^= ((row >> 3) & 1) << 2;
        voffQ[u] = row * 3840 + 80 * j1 + 16 * s;
    }
    BAR();
    {
        const int rw = 4 * g + q4;
        const unsigned ra_ = lb + L_A + rw * 128 + 16 * ((2 * w + (p4 >> 1)) ^ (((rw >> 1) & 3) << 1)) + 8 * (p4 & 1);
        sfor<0, 5>([&](auto R) {
            constexpr int r = R.v;
            const unsigned dst = rdst[r];
            const s16x8 f = cat(trread(ra_ + r * 4096), trread(ra_ + r * 4096 + 2048));
            st16_sc1((unsigned)(lane * 16), ws + dst, __builtin_bit_cast(u32x4, f));
        });
    }
    {
        f32x4 mean4[2], rstd4[2];
#pragma unroll
        for (int m = 0; m < 2; ++m) {
            mean4[m] = *(LAS f32x4*)(lb + L_MEAN + (32 * w + 16 * m + 4 * g) * 4);
            rstd4[m] = *(LAS f32x4*)(lb + L_RSTD + (32 * w + 16 * m + 4 * g) * 4);
        }
        float cs[5];
#pragma unroll
        for (int t = 0; t < 4; ++t) cs[t] = ((LAS float*)(lb + L_CSKV))[16 * t + n];
        {
            LAS float* c4 = (LAS float*)(lb + L_CS4);
            cs[4] = (c4[n] + c4[16 + n]) + (c4[32 + n] + c4[48 + n]);
        }
        s16x8 F[4], MA, MB;
        const char* qs = wqkv + (size_t)w * (64 * 3840);
        unsigned ql = lb + L_B + w * 5120;
        sfor<0, 5>([&](auto TT) {
            constexpr int t = TT.v;
            f32x4 v[2], vb_[2];
            sfor<0, 2>([&](auto MM) {
                constexpr int m = MM.v;
                constexpr int pc0 = 3 * t + m;
                glds16<(pc0 % 5) * 1024>(voffQ[pc0 % 5], qs, ql);
                if constexpr (pc0 % 5 == 4) { const int adv = (pc0 / 5 == 0 || w < 2) ? 4 : 0; qs += (size_t)adv * (64 * 3840); ql += adv * 5120; OPAQUE_S(qs); OPAQUE_S(ql); }
#pragma unroll
                for (int r = 0; r < 4; ++r) {
                    float y = rstd4[m][r] * (acc[t][m][r] - mean4[m][r] * cs[t]);
                    if (t < 2) v[m][r] = phi(y);
                    else if (t < 4) v[m][r] = y;
                    else { v[m][r] = n < 8 ? phi(y) : y; vb_[m][r] = n < 8 ? 1.f : y; }
                }
                SB();
            });
            {
                constexpr int pc2 = 3 * t + 2;
                glds16<(pc2 % 5) * 1024>(voffQ[pc2 % 5], qs, ql);
                if constexpr (pc2 % 5 == 4) { const int adv = (pc2 / 5 == 0 || w < 2) ? 4 : 0; qs += (size_t)adv * (64 * 3840); ql += adv * 5120; OPAQUE_S(qs); OPAQUE_S(ql); }
            }
            if constexpr (t < 4) F[t] = pack8(v[0], v[1]);
            else { MA = pack8(v[0], v[1]); MB = pack8(vb_[0], vb_[1]); }
            SB();
        });
        f32x4 P1 = mfma(F[0], F[2], z4), P2 = mfma(F[0], MB, z4), P3 = mfma(MA, F[2], z4), P4 = mfma(MA, MB, z4);
        f32x4 P5 = mfma(F[1], F[3], z4), P6 = mfma(F[1], MB, z4), P7 = mfma(MA, F[3], z4);
        LAS char* red = (LAS char*)(lb + L_A + 20480 + w * 3360);
        *(LAS f32x4*)(red + (n * 20 + 4 * g) * 4) = P1;
        *(LAS f32x4*)(red + (420 + n * 20 + 4 * g) * 4) = P5;
        if (n == 0 || (n >= 8 && n < 12)) *(LAS f32x4*)(red + ((n == 0 ? 20 : 8 + n) * 20 + 4 * g) * 4) = P2;
        if (n == 0 || n >= 12) *(LAS f32x4*)(red + (420 + (n == 0 ? 20 : 4 + n) * 20 + 4 * g) * 4) = P6;
        if (g < 2) *(LAS f32x4*)(red + (420 * g + n * 20 + 16) * 4) = (g == 0 ? P3 : P7);
        if (g < 2 && (n == 0 || (g == 0 ? (n >= 8 && n < 12) : n >= 12)))
            *(LAS f32x4*)(red + (420 * g + (n == 0 ? 20 : (g == 0 ? 8 + n : 4 + n)) * 20 + 16) * 4) = P4;
    }
    BAR();
    if (tid < 210) {
        LAS char* red = (LAS char*)(lb + L_A + 20480 + tid * 16);
        f32x4 s = (*(LAS f32x4*)(red) + *(LAS f32x4*)(red + 3360)) + (*(LAS f32x4*)(red + 6720) + *(LAS f32x4*)(red + 10080));
        st16_sc1((unsigned)(tid * 16), ws + WS_KVP + (size_t)(j1 * 16 + i1) * 3360, __builtin_bit_cast(u32x4, s));
    }
    drain_bar();
    if (tid < 16) flags_store(ws, 0, tid * 16 + j1, i1, tag);
    STAMP(2);

    const int half = j2 >> 3, c8 = j2 & 7;
    unsigned voff160[5];
    {
        int lane2 = lane; asm volatile("" : "+v"(lane2));
#pragma unroll
        for (int u = 0; u < 5; ++u) { int q = 64 * u + lane2, row = q / 10, s = q - row * 10; voff160[u] = row * 1280 + 160 * c8 + 16 * s; }
    }
    s16x8 Fa[2], Fb[2];
    {
        const char* worun = wo + (size_t)w * (32 * 1280);
        const unsigned vQ1 = lb + L_B + ro1 * 80 + 8 * p4, vQ2 = lb + L_B + ro2 * 80 + 8 * p4;
        const int to = (p4 == 0 ? 32 : p4 == 1 ? 72 : p4 == 2 ? 32 : 72);
        const unsigned vT1 = lb + L_B + ro1 * 80 + to, vT2 = lb + L_B + ro2 * 80 + to;
        f32x4 aq[3][2], cq[3];
#pragma unroll
        for (int t = 0; t < 3; ++t) aq[t][0] = aq[t][1] = cq[t] = z4;
        s16x4 Wl[2][3], Wh[2][3];
        auto side = [&](auto KS, auto I) {
            constexpr int ks = KS.v, i = I.v, r = ks & 1, t = i >> 1;
            constexpr unsigned ko = ks * 2560;
            if constexpr (t < 2) { if constexpr (i & 1) Wh[r][t] = trread(vQ2 + ko + 40 * t); else Wl[r][t] = trread(vQ1 + ko + 40 * t); }
            else { if constexpr (i & 1) Wh[r][2] = trread(vT2 + ko); else Wl[r][2] = trread(vT1 + ko); }
        };
        sfor<0, 6>([&](auto I) { side(IC<0>{}, I); });
        SB();
        sfor<0, 20>([&](auto KS) {
            constexpr int ks = KS.v, r = ks & 1;
            sfor<0, 9>([&](auto I) {
                constexpr int i = I.v, t = i / 3, m = i % 3;
                const s16x8 wf = cat(Wl[r][t], Wh[r][t]);
                if constexpr (m < 2) aq[t][m] = mfma(wf, X[ks][m], aq[t][m]);
                else cq[t] = mfma(wf, ones, cq[t]);
                if constexpr (i < 6 && ks < 19) side(IC<ks + 1>{}, I);
                if constexpr ((i == 3 || i == 8) && (2 * ks + (i == 8)) < 25) {
                    constexpr int pc_ = 2 * ks + (i == 8), tt = pc_ / 5, u = pc_ % 5;
                    glds16<tt * 20480 + u * 1024>(voff160[u], worun, ldsWw);
                    if constexpr (u == 4) { worun += 128 * 1280; OPAQUE_S(worun); }
                }
                SB();
            });
        });
#pragma unroll
        for (int m = 0; m < 2; ++m) {
            const float mean = ((LAS float*)(lb + L_MEAN))[32 * w + 16 * m + n], rstd = ((LAS float*)(lb + L_RSTD))[32 * w + 16 * m + n];
            f32x4 v[3];
#pragma unroll
            for (int t = 0; t < 3; ++t)
#pragma unroll
                for (int r = 0; r < 4; ++r) {
                    float y = phi(rstd * (aq[t][m][r] - mean * cq[t][r]));
                    v[t][r] = (t == 2 && g >= 2) ? 0.f : y;
                }
            Fa[m] = pack8(v[0], v[2]);
            Fb[m] = pack8(v[1], v[2]);
        }
    }
    STAMP(3);

    await<63>(ws, 0, i1 * 16 + j1, tag, w, lane);
    STAMP(4);
    if (tid < 210) {
        s16x8 p[16];
        const char* kb = ws + WS_KVP + (size_t)j1 * 16 * 3360;
#pragma unroll
        for (int i = 0; i < 16; ++i) ld16_sc1(p[i], (unsigned)(tid * 16 + i * 3360), kb);
        asm volatile("s_waitcnt vmcnt(0)" : "+v"(p[0]), "+v"(p[1]), "+v"(p[2]), "+v"(p[3]), "+v"(p[4]), "+v"(p[5]), "+v"(p[6]), "+v"(p[7]),
                     "+v"(p[8]), "+v"(p[9]), "+v"(p[10]), "+v"(p[11]), "+v"(p[12]), "+v"(p[13]), "+v"(p[14]), "+v"(p[15])::"memory");
        f32x4 s = __builtin_bit_cast(f32x4, p[0]);
#pragma unroll
        for (int i = 1; i < 16; ++i) s += __builtin_bit_cast(f32x4, p[i]);
        *(LAS u32x2*)(lb + L_KVT + tid * 8) = pack4(s);
    }
    BAR();
    {
        const u32x2 zz = {0, 0};
        auto kvchunk = [&](int head, int e, int c) { return *(LAS u32x2*)(lb + L_KVT + (head * 420 + e * 20 + 4 * c) * 2); };
        auto mk = [&](u32x2 lo, u32x2 hi) { u32x4 r = {lo[0], lo[1], hi[0], hi[1]}; return __builtin_bit_cast(s16x8, r); };
        s16x8 Ama = mk(kvchunk(0, n, g), g == 0 ? kvchunk(0, n, 4) : zz);
        s16x8 Amb = mk(kvchunk(1, n, g), g == 1 ? kvchunk(1, n, 4) : zz);
        s16x8 Aza = mk(kvchunk(0, 20, g), g == 0 ? kvchunk(0, 20, 4) : zz);
        s16x8 Azb = mk(kvchunk(1, 20, g), g == 1 ? kvchunk(1, 20, 4) : zz);
        u32x2 ta_lo = kvchunk(0, 16 + (n & 3), g), ta_hi = kvchunk(0, 16 + (n & 3), 4);
        u32x2 tb_lo = kvchunk(1, 16 + (n & 3), g), tb_hi = kvchunk(1, 16 + (n & 3), 4);
        s16x8 Ata = mk(n < 4 ? ta_lo : zz, (n < 4 && g == 0) ? ta_hi : zz);
        s16x8 Atb = mk((n >= 4 && n < 8) ? tb_lo : zz, (n >= 4 && n < 8 && g == 1) ? tb_hi : zz);
#pragma unroll
        for (int m = 0; m < 2; ++m) {
            f32x4 Da = mfma(Ama, Fa[m], z4), Db = mfma(Amb, Fb[m], z4);
            f32x4 Dt = mfma(Atb, Fb[m], mfma(Ata, Fa[m], z4));
            f32x4 Dza = mfma(Aza, Fa[m], z4), Dzb = mfma(Azb, Fb[m], z4);
            const float ra = __builtin_amdgcn_rcpf(Dza[0] + 1e-6f), rb = __builtin_amdgcn_rcpf(Dzb[0] + 1e-6f);
            const int mt = 8 * i1 + 2 * w + m;
            s16x8 of = pack8(Da * ra, Db * rb);
            st16_sc1((unsigned)((mt * 20 + j1) * 1024 + lane * 16), ws + WS_O, __builtin_bit_cast(u32x4, of));
            u32x2 tl = pack4(Dt * (g == 0 ? ra : rb));
            const int t = j1 >> 1;
            if (g < 2) st8_sc1((unsigned)((mt * 20 + 16 + (t >> 1)) * 1024 + (n + 16 * (2 * (j1 & 1) + g)) * 16 + (t & 1) * 8), ws + WS_O, tl);
        }
    }
    drain_bar();
    if (tid < 16) flags_store(ws, 1, i1 * 16 + tid, j1, tag);
    STAMP(5);

    const int T0 = j2 < 12 ? 7 * j2 : 84 + 6 * (j2 - 12);
    const int odd = (j2 < 12) ? (j2 & 1) : 0;
    const int Tp0 = T0 + odd;
    const int Tsg = j2 < 12 ? (odd ? T0 : T0 + 6) : Tp0;
    const unsigned vlan16 = lane * 16;
    const int mtW = 8 * i2 + 4 * half + w;
    {
        const char* xs_ = x + (size_t)(8 * i2 + 4 * half) * 16 * 1280;
        if (w < 2) sfor<0, 5>([&](auto U) { glds16<U.v * 1024>(voff160[U.v], xs_ + (size_t)w * (32 * 1280), lb + L_XRES + w * 5120); });
    }
    await<63>(ws, 1, i2 * 16 + j2, tag, w, lane);
    STAMP(6);
    {
        const int mh = w >> 1, kh = w & 1;
        s16x8 O[10][2];
        unsigned orun = (unsigned)(((8 * i2 + 4 * half + 2 * mh) * 20 + 8 * kh) * 1024 + lane * 16);
#define ISSUE_O(i) do { ld16_sc1(O[i][0], orun, ws + WS_O); ld16_sc1(O[i][1], orun + 20 * 1024, ws + WS_O); \
            orun += ((i) == 7 ? (unsigned)((9 - 6 * kh) * 1024) : 1024u); asm volatile("" : "+v"(orun)); } while (0)
        sfor<0, 5>([&](auto I) { ISSUE_O(I.v); });
        const unsigned vA = lb + L_A + (4 * g + q4) * 160 + 8 * p4 + kh * (8 * 6400);
        const unsigned vB = lb + L_A + (20 * g + 16 + q4) * 160 + 8 * p4 + kh * (2 * 25600);
        f32x4 a[2][5];
#pragma unroll
        for (int t = 0; t < 5; ++t) a[0][t] = a[1][t] = z4;
        s16x4 Wl[2][5], Wh[2][5];
        auto side = [&](auto I, auto J) {
            constexpr int i = I.v, j = J.v, r = i & 1, t = j >> 1;
            if constexpr (i < 8) { if constexpr (j & 1) Wh[r][t] = trread(vA + i * 6400 + 3200 + 32 * t); else Wl[r][t] = trread(vA + i * 6400 + 32 * t); }
            else { if constexpr (j & 1) Wh[r][t] = trread(vB + (i - 8) * 25600 + 12800 + 32 * t); else Wl[r][t] = trread(vB + (i - 8) * 25600 + 32 * t); }
        };
        sfor<0, 10>([&](auto J) { side(IC<0>{}, J); });
        SB();
        sfor<0, 10>([&](auto I) {
            constexpr int i = I.v;
            constexpr int nyo = 2 * ((i + 4 < 9 ? i + 4 : 9) - i) + (i < 5 ? i : 5);
            asm volatile("s_waitcnt vmcnt(%2)" : "+v"(O[i][0]), "+v"(O[i][1]) : "i"(nyo) : "memory");
            SB();
            sfor<0, 10>([&](auto J) {
                constexpr int j = J.v, t = j >> 1, m = j & 1;
                a[m][t] = mfma(cat(Wl[i & 1][t], Wh[i & 1][t]), O[i][m], a[m][t]);
                if constexpr (i < 9) side(IC<i + 1>{}, J);
                if constexpr (j == 4 && i + 5 < 10) ISSUE_O(i + 5);
                if constexpr (j == 8) {
                    const int Tg = i < 5 ? Tp0 + 5 : Tsg;
                    glds16c<(5 + i / 5) * 20480 + (i % 5) * 4096>(vlan16, ws + WS_W1R + (size_t)(Tg * 20 + (i % 5) * 4 + w) * 1024, lb + w * 1024);
                }
                SB();
            });
        });
        STAMP(28);
        LAS char* ex = (LAS char*)(lb + L_A);
        BAR();
#pragma unroll
        for (int t = 0; t < 5; ++t) *(LAS f32x4*)(ex + ((w ^ 1) * 5 + t) * 1024 + lane * 16) = kh ? a[0][t] : a[1][t];
        BAR();
        float su = 0.f, s2 = 0.f;
        u32x2 hpk[5];
#pragma unroll
        for (int t = 0; t < 5; ++t) {
            f32x4 at = (kh ? a[1][t] : a[0][t]) + *(LAS f32x4*)(ex + (w * 5 + t) * 1024 + lane * 16);
            u32x2 ap = pack4(at);
            const u32x2 xr = *(LAS u32x2*)(lb + L_XRES + (16 * w + n) * 160 + 32 * t + 8 * g);
            f32x4 hv = {bflo(xr[0]) + bflo(ap[0]), bfhi(xr[0]) + bfhi(ap[0]), bflo(xr[1]) + bflo(ap[1]), bfhi(xr[1]) + bfhi(ap[1])};
            hpk[t] = pack4(hv);
            *(LAS u32x2*)(lb + L_XRES + (16 * w + n) * 160 + 32 * t + 8 * g) = hpk[t];
            const f32x4 hr = {bflo(hpk[t][0]), bfhi(hpk[t][0]), bflo(hpk[t][1]), bfhi(hpk[t][1])};
#pragma unroll
            for (int r = 0; r < 4; ++r) { su += hr[r]; s2 += hr[r] * hr[r]; }
        }
        const unsigned hb = (unsigned)((mtW * 20 + ((5 * c8) >> 1)) * 1024 + lane * 16);
        const int od = c8 & 1;
        st8_sc1(hb + (od ? 8 : 2048), ws + WS_H, od ? hpk[0] : hpk[4]);
        { const u32x2 p0 = od ? hpk[1] : hpk[0], p1 = od ? hpk[2] : hpk[1]; st16_sc1(hb + (od ? 1024 : 0), ws + WS_H, u32x4{p0[0], p0[1], p1[0], p1[1]}); }
        { const u32x2 p0 = od ? hpk[3] : hpk[2], p1 = od ? hpk[4] : hpk[3]; st16_sc1(hb + (od ? 2048 : 1024), ws + WS_H, u32x4{p0[0], p0[1], p1[0], p1[1]}); }
        su += __shfl_xor(su, 16); s2 += __shfl_xor(s2, 16);
        su += __shfl_xor(su, 32); s2 += __shfl_xor(s2, 32);
        if (g == 0) { u32x2 st = {__float_as_uint(su), __float_as_uint(s2)}; st8_sc1((unsigned)((mtW * 16 + n) * 64 + c8 * 8), ws + WS_STAT2, st); }
    }
    drain_bar();
    if (tid < 16) flags_store(ws, 2, i2 * 16 + tid, j2, tag);
    if (b == 0 && tid == 0) __hip_atomic_store((GAS u64*)ws, tag, RLX_AGENT);
    STAMP(7);

    {
        const char* s1 = ws + WS_W1R + (size_t)(Tp0 * 20 + w) * 1024;
        const unsigned l1 = lb + L_A + w * 1024;
        sfor<0, 25>([&](auto U) { glds16c<U.v * 4096>(vlan16, s1, l1); s1 += 4096; OPAQUE_S(s1); });
    }
    await<0>(ws, 2, i2 * 16 + j2, tag, w, lane);
    STAMP(8);
    {
        s16x8 H[20][2];
        const int mt0 = 8 * i2 + 2 * w;
        s16x8 stv[2];
#pragma unroll
        for (int m = 0; m < 2; ++m) ld16_sc1(stv[m], (unsigned)(((mt0 + m) * 16 + n) * 64 + g * 16), ws + WS_STAT2);
        unsigned hrun = (unsigned)(mt0 * 20 * 1024 + lane * 16);
#define ISSUE_H(ks) do { ld16_sc1(H[ks][0], hrun, ws + WS_H); ld16_sc1(H[ks][1], hrun + 20 * 1024, ws + WS_H); hrun += 1024; asm volatile("" : "+v"(hrun)); } while (0)
        unsigned vF0 = lb + vlan16, vF1 = lb + 61440 + vlan16, vF2 = lb + 122880 + vlan16;
        asm volatile("" : "+v"(vF0), "+v"(vF1), "+v"(vF2));
        auto wread = [&](auto TT, auto KS) -> s16x8 {
            constexpr int t = TT.v, ks = KS.v;
            return *(volatile LAS s16x8*)((t < 3 ? vF0 : t < 6 ? vF1 : vF2) + (t % 3) * 20480 + ks * 1024);
        };
        {
            f32x4 c[7];
            s16x8 Cf[7];
            const unsigned o1 = w * 5 * 1024;
            sfor<0, 7>([&](auto TT) { c[TT.v] = z4; });
            sfor<0, 5>([&](auto K) {
                constexpr int k = K.v;
                sfor<0, 7>([&](auto TT) { constexpr int t = TT.v; Cf[t] = *(volatile LAS s16x8*)((t < 3 ? vF0 : t < 6 ? vF1 : vF2) + o1 + (t % 3) * 20480 + k * 1024); });
                if constexpr (k < 4) { ISSUE_H(2 * k); ISSUE_H(2 * k + 1); }
                SB();
                sfor<0, 7>([&](auto TT) { c[TT.v] = mfma(Cf[TT.v], ones, c[TT.v]); });
                SB();
            });
            if (n == 0) sfor<0, 7>([&](auto TT) { constexpr int t = TT.v; *(LAS f32x4*)(lb + L_CS1 + (t * 4 + w) * 64 + g * 16) = c[t]; });
        }
        STAMP(19);
        asm volatile("s_waitcnt vmcnt(16)" : "+v"(stv[0]), "+v"(stv[1])::"memory");
        float mean[2], rstd[2];
#pragma unroll
        for (int m = 0; m < 2; ++m) {
            f32x4 s = __builtin_bit_cast(f32x4, stv[m]);
            float su = s[0] + s[2], s2 = s[1] + s[3];
            su += __shfl_xor(su, 16); s2 += __shfl_xor(s2, 16);
            su += __shfl_xor(su, 32); s2 += __shfl_xor(s2, 32);
            mean[m] = su * (1.f / 640.f);
            rstd[m] = __builtin_amdgcn_rsqf(fmaxf(s2 * (1.f / 640.f) - mean[m] * mean[m], 0.f) + 1e-5f);
        }
        BAR();
        f32x4 cs1[7];
        sfor<0, 7>([&](auto TT) {
            const LAS char* p = (const LAS char*)(lb + L_CS1 + TT.v * 256 + g * 16);
            cs1[TT.v] = (*(const LAS f32x4*)p + *(const LAS f32x4*)(p + 64)) + (*(const LAS f32x4*)(p + 128) + *(const LAS f32x4*)(p + 192));
            asm volatile("" : "+a"(cs1[TT.v]));
        });
        const int ksb = w < 2 ? 14 * w : 28 + 13 * (w - 2);
        const int nks = w < 2 ? 14 : 13;
        s16x8 V[14][5];
        const char* w2run = ws + WS_W2R + (size_t)(c8 * 54 + ksb) * 5120;
        const int step13 = nks == 14 ? 5120 : 0;
#define ISSUE_V(it, t) do { if constexpr ((it) < 8) ld16a_sc1(V[it][t], vlan16 + (t) * 1024, w2run); else ld16_sc1(V[it][t], vlan16 + (t) * 1024, w2run); \
            if ((t) == 4) { w2run += ((it) == 12 ? step13 : 5120); OPAQUE_S(w2run); } } while (0)
        auto fin1 = [&](f32x4 a_, f32x4 c_, int m) {
            f32x4 v;
#pragma unroll
            for (int r = 0; r < 4; ++r) v[r] = fmaxf(a_[r] - mean[m] * c_[r], 0.f) * rstd[m];
            return v;
        };
        f32x4 a1[4][2];
        sfor<0, 4>([&](auto TT) { a1[TT.v][0] = a1[TT.v][1] = z4; });
        {
            s16x8 Wf[2][4];
            sfor<0, 4>([&](auto TT) { Wf[0][TT.v] = wread(TT, IC<0>{}); });
            SB();
            sfor<0, 20>([&](auto KS) {
                constexpr int ks = KS.v;
                constexpr int last = (ks + 7 < 19 ? ks + 7 : 19);
                asm volatile("s_waitcnt vmcnt(%2)" : "+v"(H[ks][0]), "+v"(H[ks][1]) : "i"(2 * (last - ks)) : "memory");
                SB();
                sfor<0, 8>([&](auto I) {
                    constexpr int i = I.v, t = i >> 1, m = i & 1;
                    a1[t][m] = mfma(Wf[ks & 1][t], H[ks][m], a1[t][m]);
                    if constexpr (ks < 19 && (i & 1) == 0) Wf[(ks + 1) & 1][t] = wread(IC<t>{}, IC<ks + 1>{});
                    if constexpr (i == 3 && ks + 8 < 20) ISSUE_H(ks + 8);
                    SB();
                });
            });
        }
        STAMP(20);
        f32x4 a2[3][2];
        sfor<0, 3>([&](auto TT) { a2[TT.v][0] = a2[TT.v][1] = z4; });
        {
            s16x8 Wf[2][3];
            sfor<0, 3>([&](auto TT) { Wf[0][TT.v] = wread(IC<4 + TT.v>{}, IC<0>{}); });
            f32x4 e0, e1;
            sfor<0, 20>([&](auto KS) {
                constexpr int ks = KS.v;
                sfor<0, 6>([&](auto I) {
                    constexpr int i = I.v, t = i >> 1, m = i & 1;
                    a2[t][m] = mfma(Wf[ks & 1][t], H[ks][m], a2[t][m]);
                    if constexpr (ks < 19 && (i & 1) == 0) Wf[(ks + 1) & 1][t] = wread(IC<4 + t>{}, IC<ks + 1>{});
                    if constexpr (i == 3 || (i == 5 && (ks & 1) == 0)) { constexpr int pv = (ks >> 1) * 3 + ((ks & 1) ? 2 : (i == 5 ? 1 : 0)); ISSUE_V(pv / 5, pv % 5); }
                    if constexpr (ks >= 1 && ks < 17 && ((ks - 1) & 3) < 3) {
                        constexpr int u = (ks - 1) >> 2, ph = (ks - 1) & 3, P = u >> 1, mm = u & 1;
                        if constexpr (ph == 0 && i == 1) e0 = fin1(a1[2 * P][mm], cs1[2 * P], mm);
                        if constexpr (ph == 1 && i == 1) e1 = fin1(a1[2 * P + 1][mm], cs1[2 * P + 1], mm);
                        if constexpr (ph == 2 && i == 1)
                            st16_sc1((unsigned)(((mt0 + mm) * 54 + (Tp0 >> 1) + P) * 1024 + lane * 16), ws + WS_H1, __builtin_bit_cast(u32x4, pack8(e0, e1)));
                    }
                    SB();
                });
            });
        }
        STAMP(22);
#pragma unroll
        for (int m = 0; m < 2; ++m) {
            st16_sc1((unsigned)(((mt0 + m) * 54 + (Tp0 >> 1) + 2) * 1024 + lane * 16), ws + WS_H1,
                     __builtin_bit_cast(u32x4, pack8(fin1(a2[0][m], cs1[4], m), fin1(a2[1][m], cs1[5], m))));
            if (j2 < 12) st8_sc1((unsigned)(((mt0 + m) * 54 + (Tsg >> 1)) * 1024 + lane * 16 + (Tsg & 1) * 8), ws + WS_H1, pack4(fin1(a2[2][m], cs1[6], m)));
        }
        STAMP(24);
        drain_bar();
        if (tid < 16) flags_store(ws, 3, i2 * 16 + tid, j2, tag);
        STAMP(9);

        sfor<30, 70>([&](auto U) { ISSUE_V(U.v / 5, U.v % 5); });
        await<63>(ws, 3, i2 * 16 + j2, tag, w, lane);
        STAMP(10);
        unsigned arun = (unsigned)(((8 * i2 + 4 * half) * 54 + ksb) * 1024 + lane * 16);
        const unsigned astep13 = nks == 14 ? 1024u : 0u;
        s16x8 A[4][4];
#define ISSUE_A(it, m) do { ld16_sc1(A[(it) & 3][m], arun + (m) * 54 * 1024, ws + WS_H1); \
            if ((m) == 3) { arun += ((it) == 12 ? astep13 : 1024u); asm volatile("" : "+v"(arun)); } } while (0)
        sfor<0, 3>([&](auto IT) { sfor<0, 4>([&](auto M) { ISSUE_A(IT.v, M.v); }); });
        f32x4 a[4][5];
#pragma unroll
        for (int m = 0; m < 4; ++m)
#pragma unroll
            for (int t = 0; t < 5; ++t) a[m][t] = z4;
        sfor<0, 14>([&](auto IT) {
            constexpr int it = IT.v;
            constexpr int ny = 4 * ((it + 2 < 13 ? it + 2 : 13) - it);
            if constexpr (it == 1) STAMP(25);
            if constexpr (it == 5) STAMP(26);
            if constexpr (it == 9) STAMP(27);
            if constexpr (it < 8)
                asm volatile("s_waitcnt vmcnt(%9)" : "+v"(A[it & 3][0]), "+v"(A[it & 3][1]), "+v"(A[it & 3][2]), "+v"(A[it & 3][3]),
                             "+a"(V[it][0]), "+a"(V[it][1]), "+a"(V[it][2]), "+a"(V[it][3]), "+a"(V[it][4]) : "i"(ny) : "memory");
            else
                asm volatile("s_waitcnt vmcnt(%9)" : "+v"(A[it & 3][0]), "+v"(A[it & 3][1]), "+v"(A[it & 3][2]), "+v"(A[it & 3][3]),
                             "+v"(V[it][0]), "+v"(V[it][1]), "+v"(V[it][2]), "+v"(V[it][3]), "+v"(V[it][4]) : "i"(ny) : "memory");
            SB();
            if (it < 13 || nks == 14) {
                sfor<0, 20>([&](auto I) {
                    constexpr int i = I.v, t = i >> 2, m = i & 3;
                    a[m][t] = mfma(V[it][t], A[it & 3][m], a[m][t]);
                    if constexpr ((i & 3) == 1 && i < 16 && it + 3 < 14) ISSUE_A(it + 3, i >> 2);
                    SB();
                });
            }
        });
        STAMP(11);
        BAR();
#pragma unroll
        for (int m = 0; m < 4; ++m)
#pragma unroll
            for (int t = 0; t < 5; ++t)
                if (m != w) *(LAS f32x4*)(lb + ((m * 4 + w) * 5 + t) * 1024 + lane * 16) = a[m][t];
        BAR();
        f32x4 fin[5];
#pragma unroll
        for (int t = 0; t < 5; ++t) fin[t] = w == 0 ? a[0][t] : w == 1 ? a[1][t] : w == 2 ? a[2][t] : a[3][t];
#pragma unroll
        for (int s = 0; s < 4; ++s)
            if (s != w) {
#pragma unroll
                for (int t = 0; t < 5; ++t) fin[t] += *(LAS f32x4*)(lb + ((w * 4 + s) * 5 + t) * 1024 + lane * 16);
            }
        LAS char* ot = (LAS char*)(lb + L_B + w * 2560);
#pragma unroll
        for (int t = 0; t < 5; ++t) {
            u32x2 fp = pack4(fin[t]);
            const u32x2 hp = *(LAS u32x2*)(lb + L_XRES + (16 * w + n) * 160 + 32 * t + 8 * g);
            f32x4 ov = {bflo(hp[0]) + bflo(fp[0]), bfhi(hp[0]) + bfhi(fp[0]), bflo(hp[1]) + bflo(fp[1]), bfhi(hp[1]) + bfhi(fp[1])};
            *(LAS u32x2*)(ot + n * 160 + 32 * t + 8 * g) = pack4(ov);
        }
        asm volatile("s_waitcnt lgkmcnt(0)" ::: "memory");
        int lane3 = lane; asm volatile("" : "+v"(lane3));
#pragma unroll
        for (int u = 0; u < 3; ++u) {
            const int q = 64 * u + lane3, row = q / 10, sl = q - row * 10;
            if (q < 160) *(GAS u32x4*)(out + (size_t)(mtW * 16 + row) * 1280 + 160 * c8 + 16 * sl) = *(LAS u32x4*)(ot + q * 16);
        }
    }
    STAMP(12);
}

extern "C" void kernel_launch(void* const* d_in, const int* in_sizes, int n_in, void* d_out, int out_size, void* d_ws, size_t ws_size,
                              hipStream_t stream) {
    (void)in_sizes; (void)n_in; (void)out_size; (void)ws_size;
    fused_block<<<256, 256, 0, stream>>>((const char*)d_in[0], (const char*)d_in[1], (const char*)d_in[2], (const char*)d_in[4],
                                         (const char*)d_in[5], (char*)d_out, (char*)d_ws);
}
```

```cpp
#include <hip/hip_runtime.h>
#include <stdint.h>
#pragma clang diagnostic ignored "-Wint-to-pointer-cast"

typedef short s16x4 __attribute__((ext_vector_type(4)));
typedef short s16x8 __attribute__((ext_vector_type(8)));
typedef float f32x4 __attribute__((ext_vector_type(4)));
typedef unsigned u32x4 __attribute__((ext_vector_type(4)));
typedef unsigned u32x2 __attribute__((ext_vector_type(2)));
typedef unsigned long long u64;
#define LAS __attribute__((address_space(3)))
#define GAS __attribute__((address_space(1)))
#define DI __device__ __forceinline__
#define RLX_AGENT __ATOMIC_RELAXED, __HIP_MEMORY_SCOPE_AGENT
#define SB() __builtin_amdgcn_sched_barrier(0)

constexpr size_t WS_FLAG = 4096;
constexpr size_t WS_KVP = 262144;
constexpr size_t WS_STAT2 = 0x120000;
constexpr size_t WS_O = 2u << 20;
constexpr size_t WS_H = 5u << 20;
constexpr size_t WS_H1 = 8u << 20;
constexpr size_t WS_W1R = 16u << 20;
constexpr size_t WS_W2R = 19u << 20;
constexpr size_t WS_STAMP = 24u << 20;

constexpr int L_A = 0;
constexpr int L_B = 102400;
constexpr int L_XRES = L_B + 40960;
constexpr int L_M = 155648;
constexpr int L_MEAN = L_M;
constexpr int L_RSTD = L_M + 512;
constexpr int L_CSKV = L_M + 1024;
constexpr int L_CS4 = L_M + 1280;
constexpr int L_KVT = L_M + 1536;
constexpr int L_CS1 = L_M + 3328;
constexpr int L_JUNK = L_M + 5632;
constexpr int LDS_BYTES = 163840;
constexpr int CODE_PF = 32768;
constexpr int CODE_BACK = 0;

enum { OP_X, OP_W, OP_R, OP_O, OP_Q, OP_T, OP_S, OP_U };
struct P1Op { int kind, a, b, ks, slot; };
constexpr P1Op P1SEQ[] = {
    {OP_U, 0, 0, -1, 0}, {OP_U, 1, 0, -1, 0},
    {OP_X, 0, 0, -1, 0}, {OP_X, 0, 1, -1, 0}, {OP_X, 0, 2, -1, 0}, {OP_X, 0, 3, -1, 0},
    {OP_W, 0, 0, -1, 0}, {OP_W, 0, 1, -1, 0}, {OP_W, 0, 2, -1, 0}, {OP_W, 0, 3, -1, 0}, {OP_W, 0, 4, -1, 0},
    {OP_U, 2, 0, -1, 0}, {OP_U, 3, 0, -1, 0},
    {OP_X, 1, 0, -1, 0}, {OP_X, 1, 1, -1, 0}, {OP_X, 1, 2, -1, 0}, {OP_X, 1, 3, -1, 0},
    {OP_O, 0, 0, -1, 0}, {OP_O, 1, 0, -1, 0},
    {OP_W, 1, 0, -1, 0}, {OP_W, 1, 1, -1, 0}, {OP_W, 1, 2, -1, 0}, {OP_W, 1, 3, -1, 0}, {OP_W, 1, 4, -1, 0},
    {OP_X, 2, 0, -1, 0}, {OP_X, 2, 1, -1, 0}, {OP_X, 2, 2, -1, 0}, {OP_X, 2, 3, -1, 0},
    {OP_W, 2, 0, 0, 3}, {OP_W, 2, 1, 0, 8}, {OP_W, 2, 2, 0, 13},
    {OP_X, 3, 0, 1, 2}, {OP_X, 3, 1, 1, 6}, {OP_X, 3, 2, 1, 10}, {OP_X, 3, 3, 1, 14},
    {OP_W, 2, 3, 2, 3}, {OP_W, 2, 4, 2, 8}, {OP_W, 3, 0, 2, 13},
    {OP_X, 4, 0, 3, 2}, {OP_X, 4, 1, 3, 6}, {OP_X, 4, 2, 3, 10}, {OP_X, 4, 3, 3, 14},
    {OP_W, 3, 1, 4, 3}, {OP_U, 4, 0, 4, 5}, {OP_W, 3, 2, 4, 8}, {OP_W, 3, 3, 4, 13},
    {OP_X, 5, 0, 5, 2}, {OP_X, 5, 1, 5, 6}, {OP_X, 5, 2, 5, 10}, {OP_X, 5, 3, 5, 14},
    {OP_W, 3, 4, 6, 3}, {OP_W, 4, 0, 6, 8}, {OP_W, 4, 1, 6, 13},
    {OP_X, 6, 0, 7, 2}, {OP_X, 6, 1, 7, 6}, {OP_X, 6, 2, 7, 10}, {OP_X, 6, 3, 7, 14},
    {OP_W, 4, 2, 8, 3}, {OP_W, 4, 3, 8, 8}, {OP_W, 4, 4, 8, 13},
    {OP_X, 7, 0, 9, 2}, {OP_X, 7, 1, 9, 6}, {OP_X, 7, 2, 9, 10}, {OP_X, 7, 3, 9, 14},
    {OP_R, 0, 0, 10, 3}, {OP_R, 1, 0, 10, 8}, {OP_R, 2, 0, 10, 13},
    {OP_X, 8, 0, 11, 2}, {OP_X, 8, 1, 11, 6}, {OP_X, 8, 2, 11, 10}, {OP_X, 8, 3, 11, 14},
    {OP_R, 3, 0, 12, 3}, {OP_R, 4, 0, 12, 8}, {OP_U, 5, 0, 12, 13},
    {OP_X, 9, 0, 13, 2}, {OP_X, 9, 1, 13, 6}, {OP_X, 9, 2, 13, 10}, {OP_X, 9, 3, 13, 14},
    {OP_Q, 0, 0, 15, 3}, {OP_Q, 0, 1, 15, 8}, {OP_Q, 0, 2, 15, 13}, {OP_Q, 0, 3, 16, 3}, {OP_Q, 0, 4, 16, 8},
    {OP_T, 0, 0, 17, 1}, {OP_T, 1, 0, 17, 2}, {OP_Q, 1, 0, 17, 3}, {OP_T, 2, 0, 17, 5}, {OP_T, 3, 0, 17, 6}, {OP_T, 4, 0, 17, 7},
    {OP_Q, 1, 1, 17, 8}, {OP_Q, 1, 2, 17, 13},
    {OP_S, 0, 0, 18, 1}, {OP_Q, 1, 3, 18, 3}, {OP_S, 1, 0, 18, 6}, {OP_Q, 1, 4, 18, 8}, {OP_S, 2, 0, 18, 11}, {OP_S, 3, 0, 19, 2}, {OP_S, 4, 0, 19, 7},
};
constexpr int P1N = sizeof(P1SEQ) / sizeof(P1SEQ[0]);
constexpr int p1_wait(int s) {
    int issued = 0, need = 0;
    for (int i = 0; i < P1N; ++i) {
        if (P1SEQ[i].ks < 2 * s - 1 || P1SEQ[i].ks < 0) issued = i + 1;
        if ((P1SEQ[i].kind == OP_X && P1SEQ[i].a == s) || (P1SEQ[i].kind == OP_W && P1SEQ[i].a <= (s >> 1))) need = need > i + 1 ? need : i + 1;
    }
    return issued - need;
}
constexpr bool p1_ok() { for (int s = 0; s < 10; ++s) if (p1_wait(s) < 0 || p1_wait(s) > 63) return false; return true; }
static_assert(p1_ok(), "P1a schedule: an op is waited for before it is issued");

#define STAMP(i) do {} while (0)
#define STAMPRT(i) do {} while (0)

template <int I> struct IC { static constexpr int v = I; };
template <int B, int E, class F> DI void sfor(F&& f) { if constexpr (B < E) { f(IC<B>{}); sfor<B + 1, E>(f); } }
#define OPAQUE_S(p) asm volatile("" : "+s"(p))

DI f32x4 mfma(s16x8 a, s16x8 b, f32x4 c) { return __builtin_amdgcn_mfma_f32_16x16x32_bf16(a, b, c, 0, 0, 0); }
DI s16x4 trread(unsigned addr) { return __builtin_bit_cast(s16x4, __builtin_amdgcn_ds_read_tr16_b64_v4i16((LAS s16x4*)addr)); }
DI s16x8 cat(s16x4 a, s16x4 b) { return __builtin_shufflevector(a, b, 0, 1, 2, 3, 4, 5, 6, 7); }
DI unsigned pk2(float lo, float hi) {
    typedef __bf16 bf2 __attribute__((ext_vector_type(2)));
    typedef float f2 __attribute__((ext_vector_type(2)));
    f2 v = {lo, hi};
    return __builtin_bit_cast(unsigned, __builtin_convertvector(v, bf2));
}
DI s16x8 pack8(f32x4 a, f32x4 b) {
    u32x4 r = {pk2(a[0], a[1]), pk2(a[2], a[3]), pk2(b[0], b[1]), pk2(b[2], b[3])};
    return __builtin_bit_cast(s16x8, r);
}
DI u32x2 pack4(f32x4 a) { u32x2 r = {pk2(a[0], a[1]), pk2(a[2], a[3])}; return r; }
DI float bflo(unsigned u) { return __uint_as_float(u << 16); }
DI float bfhi(unsigned u) { return __uint_as_float(u & 0xffff0000u); }
DI float phi(float t) { return (fmaxf(t, 0.f) + 1e-6f) + __builtin_amdgcn_fmed3f(__builtin_amdgcn_exp2f(t * 1.44269504088896f), 0.f, 1.f); }
DI float add_xor16(float v) { const unsigned u = __float_as_uint(v); auto r = __builtin_amdgcn_permlane16_swap(u, u, false, false); return __uint_as_float(r[0]) + __uint_as_float(r[1]); }
DI float add_xor32(float v) { const unsigned u = __float_as_uint(v); auto r = __builtin_amdgcn_permlane32_swap(u, u, false, false); return __uint_as_float(r[0]) + __uint_as_float(r[1]); }
constexpr int wq_pos(int grp) { return grp < 3 ? 16384 + 5120 * grp : grp < 7 ? 32768 + 5120 * (grp - 3) : 5120 * (grp - 7); }

template <int LOFF> DI void glds16(unsigned voff, const void* sbase, unsigned lds) {
    asm volatile("s_add_u32 m0, %2, %3\n\ts_nop 0\n\tglobal_load_lds_dwordx4 %0, %1" ::"v"(voff), "s"(sbase), "s"(lds), "i"(LOFF) : "memory", "scc");
}
template <int LOFF, int GOFF> DI void glds16i(unsigned voff, const void* sbase, unsigned lds) {
    asm volatile("s_add_u32 m0, %2, %3\n\ts_nop 0\n\tglobal_load_lds_dwordx4 %0, %1 offset:%4" ::"v"(voff), "s"(sbase), "s"(lds), "i"(LOFF - GOFF), "i"(GOFF) : "memory", "scc");
}
template <int LOFF> DI void glds16c(unsigned voff, const void* sbase, unsigned lds) {
    asm volatile("s_add_u32 m0, %2, %3\n\ts_nop 0\n\tglobal_load_lds_dwordx4 %0, %1 sc1" ::"v"(voff), "s"(sbase), "s"(lds), "i"(LOFF) : "memory", "scc");
}
DI void glds4(unsigned voff, const void* sbase, unsigned lds) {
    asm volatile("s_mov_b32 m0, %2\n\ts_nop 0\n\tglobal_load_lds_dword %0, %1" ::"v"(voff), "s"(sbase), "s"(lds) : "memory");
}
#define WAITV(n) asm volatile("s_waitcnt vmcnt(%0)" ::"i"(n) : "memory")
#define BARX() do { __builtin_amdgcn_s_barrier(); asm volatile("" ::: "memory"); } while (0)
#define BAR() do { asm volatile("s_waitcnt lgkmcnt(0)" ::: "memory"); __builtin_amdgcn_s_barrier(); asm volatile("" ::: "memory"); } while (0)

DI void ld16_sc1(s16x8& d, unsigned voff, const void* sbase) {
    asm volatile("global_load_dwordx4 %0, %1, %2 sc1" : "=v"(d) : "v"(voff), "s"(sbase) : "memory");
}
DI void ld16a_sc1(s16x8& d, unsigned voff, const void* sbase) {
    asm volatile("global_load_dwordx4 %0, %1, %2 sc1" : "=a"(d) : "v"(voff), "s"(sbase) : "memory");
}
DI void ld8_sc1(u64& d, const void* sbase) { asm volatile("global_load_dwordx2 %0, %1, %2 sc1" : "=v"(d) : "v"(0u), "s"(sbase) : "memory"); }
DI void st16_sc1(unsigned voff, void* sbase, u32x4 v) {
    asm volatile("global_store_dwordx4 %0, %1, %2 sc1\n\ts_nop 1" ::"v"(voff), "v"(v), "s"(sbase) : "memory");
}
DI void st8_sc1(unsigned voff, void* sbase, u32x2 v) {
    asm volatile("global_store_dwordx2 %0, %1, %2 sc1" ::"v"(voff), "v"(v), "s"(sbase) : "memory");
}
DI void drain_bar() { WAITV(0); BAR(); }
DI void flags_store(char* ws, int arr, int line, int slot, u64 tag) {
    __hip_atomic_store((GAS u64*)(ws + WS_FLAG + (size_t)arr * 32768 + (size_t)line * 128) + slot, tag, RLX_AGENT);
}
template <int NV> DI void await(char* ws, int arr, int line, u64 tag, int w, int lane) {
    if (w == 0) {
        GAS u64* fl = (GAS u64*)(ws + WS_FLAG + (size_t)arr * 32768 + (size_t)line * 128) + (lane & 15);
        for (unsigned spins = 0; spins < (1u << 20); ++spins) {
            u64 f = __hip_atomic_load(fl, RLX_AGENT);
            if (__all(f == tag)) break;
        }
    }
    WAITV(NV);
    BAR();
}

__global__ void __launch_bounds__(256, 1)
fused_block(const char* __restrict__ x, const char* __restrict__ wqkv, const char* __restrict__ wo, const char* __restrict__ w1,
            const char* __restrict__ w2, char* __restrict__ out, char* __restrict__ ws) {
    __shared__ __attribute__((aligned(16))) char smem[LDS_BYTES];
    const unsigned lb = (unsigned)(uintptr_t)(LAS char*)smem;
    const int tid = threadIdx.x, lane = tid & 63;
    const int w = __builtin_amdgcn_readfirstlane(tid >> 6);
    const int b = blockIdx.x, x8 = b & 7, rr = b >> 3;
    const int i1 = 4 * (x8 >> 1) + (rr >> 3), j1 = 8 * (x8 & 1) + (rr & 7);
    const int i2 = i1, j2 = j1;
    const int half = (rr >> 2) & 1, c8 = 4 * (x8 & 1) + (rr & 3), jw = 8 * half + c8;
    const int g = lane >> 4, n = lane & 15, q4 = n >> 2, p4 = lane & 3;
    const s16x8 ones = {0x3F80, 0x3F80, 0x3F80, 0x3F80, 0x3F80, 0x3F80, 0x3F80, 0x3F80};
    const f32x4 z4 = {0, 0, 0, 0};
    STAMP(0); STAMPRT(62);

    s16x8 X[20][2];
    f32x4 acc[5][2], sq[2], rs[2], csA, csB;
    unsigned voffW[5], voffX[4];
    const char* xb = x + (size_t)i1 * 128 * 1280;
    const unsigned ldsXw = lb + L_B + w * 4096, ldsWw = lb + L_A + w * 5120;
#pragma unroll
    for (int u = 0; u < 4; ++u)
        voffX[u] = (32 * w + 8 * u + (lane >> 3)) * 1280 + 16 * ((lane & 7) ^ ((lane >> 4) + 4 * (u & 1)));
#define ISSUE_X(s, u) glds16i<((s) % 3) * 16384 + (u) * 1024, (s) * 128>(voffX[u], xb, ldsXw)
#define ISSUE_XS(s) do { ISSUE_X(s, 0); ISSUE_X(s, 1); ISSUE_X(s, 2); ISSUE_X(s, 3); } while (0)
#pragma unroll
    for (int u = 0; u < 5; ++u) {
        int q = 64 * u + lane, row = q / 10, s = q - row * 10;
        row ^= ((row >> 3) & 1) << 2;
        voffW[u] = row * 3840 + (s < 5 ? (640 + 40 * j1) * 2 + 16 * s : (1280 + 40 * j1) * 2 + 16 * (s - 5));
    }
    const char* wkb = wqkv + (size_t)w * (32 * 3840);
    const char* wrun = wkb;
#define ISSUE_W(t, u) do { glds16<(t) * 20480 + (u) * 1024>(voffW[u], wrun, ldsWw); if ((u) == 4) { wrun += 128 * 3840; OPAQUE_S(wrun); } } while (0)
    unsigned voffQ[5];
#pragma unroll
    for (int u = 0; u < 5; ++u) {
        int q = 64 * u + lane, row = q / 5, s = q - row * 5;
        row ^= ((row >> 3) & 1) << 2;
        voffQ[u] = row * 3840 + 80 * j1 + 16 * s;
    }
    auto issueQ = [&](auto A, auto U) {
        constexpr int a = A.v, u = U.v;
        const int grp = a == 0 ? (w < 2 ? w : 2) : a == 1 ? 3 + w : 7 + (w < 2 ? w : 2);
        const int pos = a == 0 ? 16384 + 5120 * grp : a == 1 ? 32768 + 5120 * w : 5120 * (grp - 7);
        glds16<u * 1024>(voffQ[u], wqkv + (size_t)grp * (64 * 3840), lb + L_B + pos);
    };
    const unsigned raR = lb + L_A + (4 * g + q4) * 128 + 16 * ((2 * w + (p4 >> 1)) ^ ((((4 * g + q4) >> 1) & 3) << 1)) + 8 * (p4 & 1);
    s16x8 Rf[5];
    auto runit = [&](auto R, unsigned& isw1, unsigned& ksr, unsigned& cl) {
        constexpr int r = R.v;
        const unsigned ub = (unsigned)b;
        unsigned v;
        if constexpr (r < 2) { isw1 = 1; v = ub + 256 * r; }
        else if constexpr (r == 2) { isw1 = ub < 28; v = isw1 ? ub + 512 : ub - 28; }
        else if constexpr (r == 3) { isw1 = 0; v = ub + 228; }
        else { isw1 = 0; v = ub < 56 ? ub + 484 : ub + 228; }
        ksr = isw1 ? (v * 2428u) >> 16 : (v * 6554u) >> 16;
        cl = v - ksr * (isw1 ? 27u : 10u);
    };
    u64 epoch;
    auto p1issue = [&](auto N) {
        constexpr P1Op op = P1SEQ[N.v];
        if constexpr (op.kind == OP_X) ISSUE_X(op.a, op.b);
        else if constexpr (op.kind == OP_W) ISSUE_W(op.a, op.b);
        else if constexpr (op.kind == OP_R) {
            unsigned isw1, ksr, cl;
            runit(IC<op.a>{}, isw1, ksr, cl);
            const unsigned pitch = isw1 ? 3456u : 1280u;
            const unsigned rrow = 8 * w + (lane >> 3);
            glds16<op.a * 4096>(rrow * pitch + 16 * ((lane & 7) ^ (((rrow >> 1) & 3) << 1)), (isw1 ? w1 : w2) + (ksr * 32 * pitch + cl * 128), lb + L_A + w * 1024);
        } else if constexpr (op.kind == OP_U) {
            const unsigned wv = rr * 4 + w;
            if constexpr (op.a == 0 || op.a == 2) {
                const unsigned l = lane < 20 ? lane : 0, rw = (l * 52u) >> 8;
                glds4((4 * wv + rw) * 1280 + (l - 5 * rw + (op.a == 2 ? 5 : 0)) * 128, x + (size_t)(x8 >> 1) * 512 * 1280, lb + L_JUNK);
            } else if constexpr (op.a == 1 || op.a == 3) {
                const unsigned li = wv * 25 + (lane < 25 ? lane : 0), rw = (li * 6554u) >> 16, rem = li - rw * 10;
                glds4((rw + (op.a == 3 ? 320 : 0)) * 3840 + 1280 + (rem >= 5 ? 640 : 0) + (x8 & 1) * 640 + rem * 128, wqkv, lb + L_JUNK);
            } else if constexpr (op.a == 4) {
                const unsigned li = wv * 25 + (lane < 25 ? lane : 0), rw = (li * 13108u) >> 16, rem = li - rw * 5;
                glds4(rw * 3840 + (x8 & 1) * 640 + rem * 128, wqkv, lb + L_JUNK);
            } else {
                const unsigned li = wv * 25 + (lane < 25 ? lane : 0), rw = (li * 13108u) >> 16, rem = li - rw * 5;
                glds4(rw * 1280 + (x8 & 1) * 640 + rem * 128, wo, lb + L_JUNK);
            }
        } else if constexpr (op.kind == OP_Q) issueQ(IC<op.a>{}, IC<op.b>{});
        else if constexpr (op.kind == OP_T) {
            Rf[op.a] = cat(trread(raR + op.a * 4096), trread(raR + op.a * 4096 + 2048));
        } else if constexpr (op.kind == OP_S) {
            unsigned isw1, ksr, cl;
            runit(IC<op.a>{}, isw1, ksr, cl);
            const unsigned T = 4 * cl + w, c8r = (T * 52u) >> 8;
            const unsigned dst = isw1 ? (unsigned)WS_W1R + (T * 20 + ksr) * 1024 : (unsigned)WS_W2R + ((c8r * 54 + ksr) * 5 + (T - 5 * c8r)) * 1024;
            st16_sc1((unsigned)(lane * 16), ws + dst, __builtin_bit_cast(u32x4, Rf[op.a]));
        } else if constexpr (op.a == 0) {
            u64 pc;
            asm volatile("s_getpc_b64 %0" : "=s"(pc));
            pc &= ~(u64)127;
            glds4((unsigned)(((rr * 4 + w) * (CODE_PF / 128 / 128) + (lane % (CODE_PF / 128 / 128))) * 128), (const void*)(pc - CODE_BACK), lb + L_JUNK);
        } else ld8_sc1(epoch, ws);
    };
    sfor<0, P1N>([&](auto N) { if constexpr (P1SEQ[N.v].ks < 0) p1issue(N); });
    const int ro1 = 8 * g + q4 + 4 * (g & 1), ro2 = 8 * g + q4 + 4 * (1 - (g & 1));
    {
        unsigned vW1[2], vW2[2], vM1[2], vM2[2], vC1[2], vC2[2];
        const int mo = (p4 == 0 ? 32 : p4 == 1 ? 72 : p4 == 2 ? 112 : 152);
#pragma unroll
        for (int h = 0; h < 2; ++h) {
            vW1[h] = lb + L_A + h * 51200 + ro1 * 160 + 8 * p4; vW2[h] = lb + L_A + h * 51200 + ro2 * 160 + 8 * p4;
            vM1[h] = lb + L_A + h * 51200 + ro1 * 160 + mo; vM2[h] = lb + L_A + h * 51200 + ro2 * 160 + mo;
            vC1[h] = vW1[h] + 40 * w; vC2[h] = vW2[h] + 40 * w;
            asm volatile("" : "+v"(vW1[h]), "+v"(vW2[h]), "+v"(vM1[h]), "+v"(vM2[h]), "+v"(vC1[h]), "+v"(vC2[h]));
        }
        unsigned vD1 = vM1[0] + w * 5120, vD2 = vM2[0] + w * 5120;
        unsigned vX[2];
#pragma unroll
        for (int kl = 0; kl < 2; ++kl) vX[kl] = lb + L_B + w * 4096 + n * 128 + 16 * ((4 * kl + g) ^ (n >> 1));
#pragma unroll
        for (int t = 0; t < 5; ++t) acc[t][0] = acc[t][1] = z4;
        sq[0] = sq[1] = rs[0] = rs[1] = csA = csB = z4;
        s16x4 Wl[2][7], Wh[2][7];
        auto side = [&](auto KS, auto I) {
            constexpr int ks = KS.v, i = I.v, r = ks & 1, s = ks >> 1, kl = ks & 1, h = ks >= 10;
            constexpr unsigned ko = (ks - 10 * h) * 5120;
            if constexpr (i < 2) X[ks][i] = *(volatile LAS s16x8*)(vX[kl] + (s % 3) * 16384 + i * 2048);
            else if constexpr (i < 10) { constexpr int t = (i - 2) >> 1; if constexpr (i & 1) Wh[r][t] = trread(vW2[h] + ko + 40 * t); else Wl[r][t] = trread(vW1[h] + ko + 40 * t); }
            else if constexpr (i < 12) { if constexpr (i & 1) Wh[r][4] = trread(vM2[h] + ko); else Wl[r][4] = trread(vM1[h] + ko); }
            else if constexpr (i < 14) { if constexpr (i & 1) Wh[r][5] = trread(vC2[h] + ko); else Wl[r][5] = trread(vC1[h] + ko); }
            else {
                constexpr int u = ks >> 2;
                if constexpr (i & 1) { Wh[r][6] = trread(vD2 + (u % 3) * 20480); if constexpr (u == 2) { vD2 += 61440; asm volatile("" : "+v"(vD2)); } }
                else { Wl[r][6] = trread(vD1 + (u % 3) * 20480); if constexpr (u == 2) { vD1 += 61440; asm volatile("" : "+v"(vD1)); } }
            }
        };
        STAMP(48);
        WAITV(p1_wait(0));
        BARX();
        STAMP(49);
        sfor<0, 14>([&](auto I) { side(IC<0>{}, I); });
        SB();
        sfor<0, 20>([&](auto KS) {
            constexpr int ks = KS.v;
            if constexpr ((ks & 1) && ks < 19) {
                constexpr int s = (ks + 1) >> 1;
                WAITV(p1_wait(s));
                if constexpr (s >= 8) BAR();
                else if constexpr ((s & 1) == 0) BARX();
                SB();
            }
            if constexpr (ks == 4) STAMP(50);
            if constexpr (ks == 8) STAMP(51);
            if constexpr (ks == 12) STAMP(52);
            if constexpr (ks == 16) STAMP(53);
            constexpr int r = ks & 1;
            constexpr int nm = ((ks & 3) == 3) ? 16 : 15;
            constexpr int nside = ks < 19 ? (((ks + 1) & 3) == 3 ? 16 : 14) : 0;
            sfor<0, 16>([&](auto I) {
                constexpr int i = I.v;
                if constexpr (i < nm) {
                    if constexpr (i < 10) { constexpr int t = i >> 1, m = i & 1; acc[t][m] = mfma(X[ks][m], cat(Wl[r][t], Wh[r][t]), acc[t][m]); }
                    else if constexpr (i == 10) csA = mfma(ones, cat(Wl[r][5], Wh[r][5]), csA);
                    else if constexpr (i < 13) sq[i - 11] = mfma(X[ks][i - 11], X[ks][i - 11], sq[i - 11]);
                    else if constexpr (i < 15) rs[i - 13] = mfma(X[ks][i - 13], ones, rs[i - 13]);
                    else csB = mfma(ones, cat(Wl[r][6], Wh[r][6]), csB);
                }
                if constexpr (i < nside) side(IC<ks + 1>{}, I);
                sfor<0, P1N>([&](auto N) { if constexpr (P1SEQ[N.v].ks == ks && P1SEQ[N.v].slot == i) p1issue(N); });
                SB();
            });
        });
    }
    asm volatile("" : "+v"(epoch), "+v"(csA), "+v"(csB));
    const u64 tag = epoch + 1;
    STAMP(1);

    if (g == 0) { ((LAS float*)(lb + L_CSKV))[16 * w + n] = csA[0]; ((LAS float*)(lb + L_CS4))[16 * w + n] = csB[0]; }
    f32x4 lnA[2], lnB[2];
#pragma unroll
    for (int m = 0; m < 2; ++m) {
        f32x4 mean4, rstd4;
#pragma unroll
        for (int r = 0; r < 4; ++r) {
            const float s2 = __uint_as_float(__builtin_amdgcn_ds_bpermute(4 * (20 * g + r), __float_as_uint(sq[m][r])));
            const float mean = rs[m][r] * (1.f / 640.f);
            const float rstd = __builtin_amdgcn_rsqf(fmaxf(s2 * (1.f / 640.f) - mean * mean, 0.f) + 1e-5f);
            mean4[r] = mean; rstd4[r] = rstd;
            lnA[m][r] = rstd; lnB[m][r] = -mean * rstd;
        }
        *(LAS f32x4*)(lb + L_MEAN + (32 * w + 16 * m + 4 * g) * 4) = mean4;
        *(LAS f32x4*)(lb + L_RSTD + (32 * w + 16 * m + 4 * g) * 4) = rstd4;
    }
    BAR();
    STAMP(31);
    {
        float cs[5];
#pragma unroll
        for (int t = 0; t < 4; ++t) cs[t] = ((LAS float*)(lb + L_CSKV))[16 * t + n];
        {
            LAS float* c4 = (LAS float*)(lb + L_CS4);
            cs[4] = (c4[n] + c4[16 + n]) + (c4[32 + n] + c4[48 + n]);
        }
        s16x8 F[4], MA, MB;
        sfor<0, 5>([&](auto TT) {
            constexpr int t = TT.v;
            issueQ(IC<2>{}, TT);
            f32x4 v[2], vb_[2];
#pragma unroll
            for (int m = 0; m < 2; ++m)
#pragma unroll
                for (int r = 0; r < 4; ++r) {
                    const float y = lnA[m][r] * acc[t][m][r] + lnB[m][r] * cs[t];
                    if (t < 2) v[m][r] = phi(y);
                    else if (t < 4) v[m][r] = y;
                    else { v[m][r] = n < 8 ? phi(y) : y; vb_[m][r] = n < 8 ? 1.f : y; }
                }
            if constexpr (t < 4) F[t] = pack8(v[0], v[1]);
            else { MA = pack8(v[0], v[1]); MB = pack8(vb_[0], vb_[1]); }
            SB();
        });
        STAMP(33);
        f32x4 P1 = mfma(F[0], F[2], z4), P2 = mfma(F[0], MB, z4), P3 = mfma(MA, F[2], z4), P4 = mfma(MA, MB, z4);
        f32x4 P5 = mfma(F[1], F[3], z4), P6 = mfma(F[1], MB, z4), P7 = mfma(MA, F[3], z4);
        LAS char* red = (LAS char*)(lb + L_A + 20480 + w * 3360);
        *(LAS f32x4*)(red + (n * 20 + 4 * g) * 4) = P1;
        *(LAS f32x4*)(red + (420 + n * 20 + 4 * g) * 4) = P5;
        if (n == 0 || (n >= 8 && n < 12)) *(LAS f32x4*)(red + ((n == 0 ? 20 : 8 + n) * 20 + 4 * g) * 4) = P2;
        if (n == 0 || n >= 12) *(LAS f32x4*)(red + (420 + (n == 0 ? 20 : 4 + n) * 20 + 4 * g) * 4) = P6;
        if (g < 2) *(LAS f32x4*)(red + (420 * g + n * 20 + 16) * 4) = (g == 0 ? P3 : P7);
        if (g < 2 && (n == 0 || (g == 0 ? (n >= 8 && n < 12) : n >= 12)))
            *(LAS f32x4*)(red + (420 * g + (n == 0 ? 20 : (g == 0 ? 8 + n : 4 + n)) * 20 + 16) * 4) = P4;
    }
    STAMP(34);
    BAR();
    STAMP(35);
    if (tid < 210) {
        LAS char* red = (LAS char*)(lb + L_A + 20480 + tid * 16);
        f32x4 s = (*(LAS f32x4*)(red) + *(LAS f32x4*)(red + 3360)) + (*(LAS f32x4*)(red + 6720) + *(LAS f32x4*)(red + 10080));
        st16_sc1((unsigned)(tid * 16), ws + WS_KVP + (size_t)(j1 * 16 + i1) * 3360, __builtin_bit_cast(u32x4, s));
    }
    STAMP(36);
    drain_bar();
    if (tid < 16) flags_store(ws, 0, tid * 16 + j1, i1, tag);
    STAMP(2);

    unsigned voff160[5];
    {
        int lane2 = lane; asm volatile("" : "+v"(lane2));
#pragma unroll
        for (int u = 0; u < 5; ++u) { int q = 64 * u + lane2, row = q / 10, s = q - row * 10; voff160[u] = row * 1280 + 160 * c8 + 16 * s; }
    }
    s16x8 Fa[2], Fb[2], kp[16];
    const char* worun = wo + (size_t)w * (32 * 1280);
    {
        const unsigned vQ1 = lb + L_B + ro1 * 80 + 8 * p4, vQ2 = lb + L_B + ro2 * 80 + 8 * p4;
        const int to = (p4 == 0 ? 32 : p4 == 1 ? 72 : p4 == 2 ? 32 : 72);
        const unsigned vT1 = lb + L_B + ro1 * 80 + to, vT2 = lb + L_B + ro2 * 80 + to;
        f32x4 aq[3][2], cq[3];
#pragma unroll
        for (int t = 0; t < 3; ++t) aq[t][0] = aq[t][1] = cq[t] = z4;
        s16x4 Wl[2][3], Wh[2][3];
        auto side = [&](auto KS, auto I) {
            constexpr int ks = KS.v, i = I.v, r = ks & 1, t = i >> 1;
            constexpr unsigned ko = wq_pos(ks >> 1) + (ks & 1) * 2560;
            if constexpr (t < 2) { if constexpr (i & 1) Wh[r][t] = trread(vQ2 + ko + 40 * t); else Wl[r][t] = trread(vQ1 + ko + 40 * t); }
            else { if constexpr (i & 1) Wh[r][2] = trread(vT2 + ko); else Wl[r][2] = trread(vT1 + ko); }
        };
        sfor<0, 6>([&](auto I) { side(IC<0>{}, I); });
        SB();
        const char* kb = ws + WS_KVP + (size_t)j1 * 16 * 3360;
        const unsigned kvo = (tid < 210 ? tid : 209) * 16;
        sfor<0, 20>([&](auto KS) {
            constexpr int ks = KS.v, r = ks & 1;
            if constexpr (ks == 13) { STAMP(3); await<63>(ws, 0, i1 * 16 + j1, tag, w, lane); STAMP(4); SB(); }
            sfor<0, 9>([&](auto I) {
                constexpr int i = I.v, t = i / 3, m = i % 3;
                const s16x8 wf = cat(Wl[r][t], Wh[r][t]);
                if constexpr (m < 2) aq[t][m] = mfma(wf, X[ks][m], aq[t][m]);
                else cq[t] = mfma(wf, ones, cq[t]);
                if constexpr (i < 6 && ks < 19) side(IC<ks + 1>{}, I);
                if constexpr ((i == 3 || i == 8) && ks < 6) {
                    constexpr int pc_ = 2 * ks + (i == 8), tt = pc_ / 5, u = pc_ % 5;
                    glds16<tt * 20480 + u * 1024>(voff160[u], worun, ldsWw);
                    if constexpr (u == 4) { worun += 128 * 1280; OPAQUE_S(worun); }
                }
                if constexpr (ks >= 13 && ks < 19 && (i == 2 || i == 5 || i == 8) && 3 * (ks - 13) + i / 3 < 16) {
                    constexpr int pi = 3 * (ks - 13) + i / 3;
                    ld16_sc1(kp[pi], kvo + pi * 3360, kb);
                }
                SB();
            });
        });
        STAMP(37);
#pragma unroll
        for (int m = 0; m < 2; ++m) {
            const float mean = ((LAS float*)(lb + L_MEAN))[32 * w + 16 * m + n], rstd = ((LAS float*)(lb + L_RSTD))[32 * w + 16 * m + n];
            f32x4 v[3];
#pragma unroll
            for (int t = 0; t < 3; ++t)
#pragma unroll
                for (int r = 0; r < 4; ++r) {
                    float y = phi(rstd * aq[t][m][r] - (mean * rstd) * cq[t][r]);
                    v[t][r] = (t == 2 && g >= 2) ? 0.f : y;
                }
            Fa[m] = pack8(v[0], v[2]);
            Fb[m] = pack8(v[1], v[2]);
        }
        asm volatile("" : "+v"(Fa[0]), "+v"(Fa[1]), "+v"(Fb[0]), "+v"(Fb[1]));
    }

    {
        asm volatile("s_waitcnt vmcnt(0)" : "+v"(kp[0]), "+v"(kp[1]), "+v"(kp[2]), "+v"(kp[3]), "+v"(kp[4]), "+v"(kp[5]), "+v"(kp[6]), "+v"(kp[7]),
                     "+v"(kp[8]), "+v"(kp[9]), "+v"(kp[10]), "+v"(kp[11]), "+v"(kp[12]), "+v"(kp[13]), "+v"(kp[14]), "+v"(kp[15])::"memory");
        f32x4 s0 = __builtin_bit_cast(f32x4, kp[0]), s1 = __builtin_bit_cast(f32x4, kp[1]);
#pragma unroll
        for (int i = 2; i < 16; i += 2) { s0 += __builtin_bit_cast(f32x4, kp[i]); s1 += __builtin_bit_cast(f32x4, kp[i + 1]); }
        if (tid < 210) *(LAS u32x2*)(lb + L_KVT + tid * 8) = pack4(s0 + s1);
    }
    STAMP(38);
    BAR();
    STAMP(39);
    {
        const u32x2 zz = {0, 0};
        auto kvchunk = [&](int head, int e, int c) { return *(LAS u32x2*)(lb + L_KVT + (head * 420 + e * 20 + 4 * c) * 2); };
        auto mk = [&](u32x2 lo, u32x2 hi) { u32x4 r = {lo[0], lo[1], hi[0], hi[1]}; return __builtin_bit_cast(s16x8, r); };
        s16x8 Ama = mk(kvchunk(0, n, g), g == 0 ? kvchunk(0, n, 4) : zz);
        s16x8 Amb = mk(kvchunk(1, n, g), g == 1 ? kvchunk(1, n, 4) : zz);
        s16x8 Aza = mk(kvchunk(0, 20, g), g == 0 ? kvchunk(0, 20, 4) : zz);
        s16x8 Azb = mk(kvchunk(1, 20, g), g == 1 ? kvchunk(1, 20, 4) : zz);
        u32x2 ta_lo = kvchunk(0, 16 + (n & 3), g), ta_hi = kvchunk(0, 16 + (n & 3), 4);
        u32x2 tb_lo = kvchunk(1, 16 + (n & 3), g), tb_hi = kvchunk(1, 16 + (n & 3), 4);
        s16x8 Ata = mk(n < 4 ? ta_lo : zz, (n < 4 && g == 0) ? ta_hi : zz);
        s16x8 Atb = mk((n >= 4 && n < 8) ? tb_lo : zz, (n >= 4 && n < 8 && g == 1) ? tb_hi : zz);
#pragma unroll
        for (int m = 0; m < 2; ++m) {
            f32x4 Da = mfma(Ama, Fa[m], z4), Db = mfma(Amb, Fb[m], z4);
            f32x4 Dt = mfma(Atb, Fb[m], mfma(Ata, Fa[m], z4));
            f32x4 Dza = mfma(Aza, Fa[m], z4), Dzb = mfma(Azb, Fb[m], z4);
            const float ra = __builtin_amdgcn_rcpf(Dza[0] + 1e-6f), rb = __builtin_amdgcn_rcpf(Dzb[0] + 1e-6f);
            const int mt = 8 * i1 + 2 * w + m;
            s16x8 of = pack8(Da * ra, Db * rb);
            st16_sc1((unsigned)((mt * 20 + j1) * 1024 + lane * 16), ws + WS_O, __builtin_bit_cast(u32x4, of));
            u32x2 tl = pack4(Dt * (g == 0 ? ra : rb));
            const int t = j1 >> 1;
            if (g < 2) st8_sc1((unsigned)((mt * 20 + 16 + (t >> 1)) * 1024 + (n + 16 * (2 * (j1 & 1) + g)) * 16 + (t & 1) * 8), ws + WS_O, tl);
        }
    }
    STAMP(40);
    drain_bar();
    if (tid < 16) flags_store(ws, 1, i1 * 16 + tid, j1, tag);
    STAMP(5);

    const int T0 = j2 < 12 ? 7 * j2 : 84 + 6 * (j2 - 12);
    const int odd = (j2 < 12) ? (j2 & 1) : 0;
    const int Tp0 = T0 + odd;
    const int Tsg = j2 < 12 ? (odd ? T0 : T0 + 6) : Tp0;
    const unsigned vlan16 = lane * 16;
    sfor<12, 25>([&](auto P) {
        constexpr int tt = P.v / 5, u = P.v % 5;
        glds16<tt * 20480 + u * 1024>(voff160[u], worun, ldsWw);
        if constexpr (u == 4) { worun += 128 * 1280; OPAQUE_S(worun); }
    });
    const int mtW = 8 * i2 + 4 * half + w;
    {
        const char* xs_ = x + (size_t)(8 * i2 + 4 * half) * 16 * 1280;
        if (w < 2) sfor<0, 5>([&](auto U) { glds16<U.v * 1024>(voff160[U.v], xs_ + (size_t)w * (32 * 1280), lb + L_XRES + w * 5120); });
    }
    STAMP(45);
    await<0>(ws, 1, i2 * 16 + jw, tag, w, lane);
    STAMP(6);
    {
        s16x8 O[20];
        unsigned orun = (unsigned)(mtW * 20 * 1024 + lane * 16);
#define ISSUE_O(i) do { ld16_sc1(O[i], orun, ws + WS_O); orun += 1024u; asm volatile("" : "+v"(orun)); } while (0)
        sfor<0, 8>([&](auto I) { ISSUE_O(I.v); });
        unsigned vA = lb + L_A + (4 * g + q4) * 160 + 8 * p4, vA2 = vA + 8 * 6400;
        const unsigned vB = lb + L_A + (20 * g + 16 + q4) * 160 + 8 * p4;
        unsigned vB2 = vB + 2 * 25600;
        asm volatile("" : "+v"(vA), "+v"(vA2), "+v"(vB2));
        f32x4 a[5];
#pragma unroll
        for (int t = 0; t < 5; ++t) a[t] = z4;
        s16x4 Wl[3][5], Wh[3][5];
        auto side = [&](auto I, auto J) {
            constexpr int i = I.v, j = J.v, r = i % 3, t = j >> 1;
            if constexpr (i < 16) {
                const unsigned a_ = (i < 8 ? vA : vA2) + (i & 7) * 6400 + 32 * t;
                if constexpr (j & 1) Wh[r][t] = trread(a_ + 3200); else Wl[r][t] = trread(a_);
            } else {
                const unsigned a_ = (i < 18 ? vB : vB2) + (i & 1) * 25600 + 32 * t;
                if constexpr (j & 1) Wh[r][t] = trread(a_ + 12800); else Wl[r][t] = trread(a_);
            }
        };
        sfor<0, 2>([&](auto I) { sfor<0, 10>([&](auto J) { side(I, J); }); });
        SB();
        sfor<0, 20>([&](auto I) {
            constexpr int i = I.v;
            constexpr int lastO = i + 7 < 19 ? i + 7 : 19;
            constexpr int firstk = i < 8 ? 0 : i - 8;
            constexpr int nW1 = (i + 1) / 2 - (firstk + 1) / 2;
            constexpr int nT = (i == 13 ? 1 : i > 13 ? 2 : 0);
            asm volatile("s_waitcnt vmcnt(%1)" : "+v"(O[i]) : "i"((lastO - i) + nW1 + nT) : "memory");
            SB();
            sfor<0, 5>([&](auto T) {
                constexpr int t = T.v;
                if constexpr (t == 0 && (i == 12 || i == 13)) {
                    int li = rr * 280 + w * 70 + (i == 12 ? lane : 64 + (lane < 6 ? lane : 0)); li = li < ((x8 & 1) ? 8320 : 8960) ? li : 8319;
                    glds4((unsigned)(li * 128), ws + WS_W1R + (x8 & 1) * 56 * 20480, lb + L_JUNK);
                }
                a[t] = mfma(cat(Wl[i % 3][t], Wh[i % 3][t]), O[i], a[t]);
                if constexpr (i + 2 < 20) { side(IC<i + 2>{}, IC<2 * t>{}); side(IC<i + 2>{}, IC<2 * t + 1>{}); }
                if constexpr (t == 1 && i + 8 < 20) ISSUE_O(i + 8);
                if constexpr (t == 3 && (i & 1) == 0) {
                    constexpr int pc_ = i / 2;
                    const int Tg = pc_ < 5 ? Tp0 + 5 : Tsg;
                    glds16c<(5 + pc_ / 5) * 20480 + (pc_ % 5) * 4096>(vlan16, ws + WS_W1R + (size_t)(Tg * 20 + (pc_ % 5) * 4 + w) * 1024, lb + w * 1024);
                }
                SB();
            });
        });
        STAMP(28);
        float su = 0.f, s2 = 0.f;
        u32x2 hpk[5];
#pragma unroll
        for (int t = 0; t < 5; ++t) {
            const u32x2 xr = *(LAS u32x2*)(lb + L_XRES + (16 * w + n) * 160 + 32 * t + 8 * g);
            const f32x4 hv = f32x4{bflo(xr[0]), bfhi(xr[0]), bflo(xr[1]), bfhi(xr[1])} + a[t];
            hpk[t] = pack4(hv);
            *(LAS u32x2*)(lb + L_XRES + (16 * w + n) * 160 + 32 * t + 8 * g) = hpk[t];
#pragma unroll
            for (int r = 0; r < 4; ++r) { su += hv[r]; s2 += hv[r] * hv[r]; }
        }
        const unsigned hb = (unsigned)((mtW * 20 + ((5 * c8) >> 1)) * 1024 + lane * 16);
        const int od = c8 & 1;
        st8_sc1(hb + (od ? 8 : 2048), ws + WS_H, od ? hpk[0] : hpk[4]);
        { const u32x2 p0 = od ? hpk[1] : hpk[0], p1 = od ? hpk[2] : hpk[1]; st16_sc1(hb + (od ? 1024 : 0), ws + WS_H, u32x4{p0[0], p0[1], p1[0], p1[1]}); }
        { const u32x2 p0 = od ? hpk[3] : hpk[2], p1 = od ? hpk[4] : hpk[3]; st16_sc1(hb + (od ? 2048 : 1024), ws + WS_H, u32x4{p0[0], p0[1], p1[0], p1[1]}); }
        su = add_xor32(add_xor16(su)); s2 = add_xor32(add_xor16(s2));
        if (g == 0) { u32x2 st = {__float_as_uint(su), __float_as_uint(s2)}; st8_sc1((unsigned)((mtW * 16 + n) * 64 + c8 * 8), ws + WS_STAT2, st); }
    }
    STAMP(42);
    drain_bar();
    if (tid < 16) flags_store(ws, 2, i2 * 16 + tid, jw, tag);
    if (b == 0 && tid == 0) __hip_atomic_store((GAS u64*)ws, tag, RLX_AGENT);
    STAMP(7);

    {
        const char* s1 = ws + WS_W1R + (size_t)(Tp0 * 20 + w) * 1024;
        const unsigned l1 = lb + L_A + w * 1024;
        sfor<0, 25>([&](auto U) { glds16c<U.v * 4096>(vlan16, s1, l1); s1 += 4096; OPAQUE_S(s1); });
    }
    STAMP(43);
    await<0>(ws, 2, i2 * 16 + j2, tag, w, lane);
    STAMP(8);
    {
        s16x8 H[20][2];
        const int mt0 = 8 * i2 + 2 * w;
        s16x8 stv[2];
#pragma unroll
        for (int m = 0; m < 2; ++m) ld16_sc1(stv[m], (unsigned)(((mt0 + m) * 16 + n) * 64 + g * 16), ws + WS_STAT2);
        unsigned hrun = (unsigned)(mt0 * 20 * 1024 + lane * 16);
#define ISSUE_H(ks) do { ld16_sc1(H[ks][0], hrun, ws + WS_H); ld16_sc1(H[ks][1], hrun + 20 * 1024, ws + WS_H); hrun += 1024; asm volatile("" : "+v"(hrun)); } while (0)
        unsigned vF0 = lb + vlan16, vF1 = lb + 61440 + vlan16, vF2 = lb + 122880 + vlan16;
        asm volatile("" : "+v"(vF0), "+v"(vF1), "+v"(vF2));
        auto wread = [&](auto TT, auto KS) -> s16x8 {
            constexpr int t = TT.v, ks = KS.v;
            return *(volatile LAS s16x8*)((t < 3 ? vF0 : t < 6 ? vF1 : vF2) + (t % 3) * 20480 + ks * 1024);
        };
        {
            f32x4 c0 = z4, c1 = z4;
            const unsigned ta = lb + vlan16 + w * 20480, tb = lb + vlan16 + (w < 3 ? w + 4 : w) * 20480;
            s16x8 Ca[4], Cb[4];
            sfor<0, 3>([&](auto K) { Ca[K.v] = *(volatile LAS s16x8*)(ta + K.v * 1024); Cb[K.v] = *(volatile LAS s16x8*)(tb + K.v * 1024); });
            SB();
            sfor<0, 20>([&](auto K) {
                constexpr int k = K.v;
                c0 = mfma(ones, Ca[k & 3], c0);
                if constexpr (k + 3 < 20) Ca[(k + 3) & 3] = *(volatile LAS s16x8*)(ta + (k + 3) * 1024);
                SB();
                c1 = mfma(ones, Cb[k & 3], c1);
                if constexpr (k + 3 < 20) Cb[(k + 3) & 3] = *(volatile LAS s16x8*)(tb + (k + 3) * 1024);
                if constexpr ((k & 1) == 0 && k < 16) ISSUE_H(k >> 1);
                SB();
            });
            auto split = [&](float c) { const unsigned hi = pk2(c, c); const float lo = c - bflo(hi); u32x2 r = {hi, pk2(lo, lo)}; return r; };
            if (g == 0) {
                *(LAS u32x2*)(lb + L_CS1 + w * 128 + n * 8) = split(c0[0]);
                if (w < 3) *(LAS u32x2*)(lb + L_CS1 + (w + 4) * 128 + n * 8) = split(c1[0]);
            }
            if (tid < 2) *(LAS u64*)(lb + L_CS1 + 896 + tid * 8) = 0;
        }
        STAMP(19);
        asm volatile("s_waitcnt vmcnt(16)" : "+v"(stv[0]), "+v"(stv[1])::"memory");
        s16x8 Hm[2];
#pragma unroll
        for (int m = 0; m < 2; ++m) {
            f32x4 s = __builtin_bit_cast(f32x4, stv[m]);
            const float nm = add_xor32(add_xor16(s[0] + s[2])) * (-1.f / 640.f);
            const unsigned hi = pk2(nm, nm);
            const unsigned pr = g == 0 ? ((hi & 0xffffu) | (pk2(nm - bflo(hi), 0.f) << 16)) : 0u;
            Hm[m] = __builtin_bit_cast(s16x8, u32x4{pr, pr, 0u, 0u});
        }
        BAR();
        const unsigned csa = lb + L_CS1 + (g == 0 ? n * 8 : 896);
        auto csfrag = [&](int t) { const u32x2 c = *(LAS u32x2*)(csa + (g == 0 ? t * 128 : 0)); return __builtin_bit_cast(s16x8, u32x4{c[0], c[1], 0u, 0u}); };
        const int ksb = w < 2 ? 14 * w : 28 + 13 * (w - 2);
        const int nks = w < 2 ? 14 : 13;
        s16x8 V[14][5];
        const char* w2run = ws + WS_W2R + (size_t)(c8 * 54 + ksb) * 5120;
        const int step13 = nks == 14 ? 5120 : 0;
#define ISSUE_V(it, t) do { if constexpr ((it) < 8) ld16a_sc1(V[it][t], vlan16 + (t) * 1024, w2run); else ld16_sc1(V[it][t], vlan16 + (t) * 1024, w2run); \
            if ((t) == 4) { w2run += ((it) == 12 ? step13 : 5120); OPAQUE_S(w2run); } } while (0)
        auto relu2 = [&](unsigned p) { typedef short s2_ __attribute__((ext_vector_type(2))); const s2_ z = {0, 0};
                                       return __builtin_bit_cast(unsigned, __builtin_elementwise_max(__builtin_bit_cast(s2_, p), z)); };
        auto fin8 = [&](f32x4 x0, f32x4 x1) { return u32x4{relu2(pk2(x0[0], x0[1])), relu2(pk2(x0[2], x0[3])), relu2(pk2(x1[0], x1[1])), relu2(pk2(x1[2], x1[3]))}; };
        f32x4 a1[4][2];
        sfor<0, 4>([&](auto TT) { const s16x8 cf = csfrag(TT.v); a1[TT.v][0] = mfma(cf, Hm[0], z4); a1[TT.v][1] = mfma(cf, Hm[1], z4); });
        {
            s16x8 Wf[3][4];
            sfor<0, 2>([&](auto KS) { sfor<0, 4>([&](auto TT) { Wf[KS.v][TT.v] = wread(TT, KS); }); });
            SB();
            sfor<0, 20>([&](auto KS) {
                constexpr int ks = KS.v;
                constexpr int last = (ks + 7 < 19 ? ks + 7 : 19);
                asm volatile("s_waitcnt vmcnt(%2)" : "+v"(H[ks][0]), "+v"(H[ks][1]) : "i"(2 * (last - ks) + (ks == 13 ? 1 : ks > 13 ? 2 : 0)) : "memory");
                SB();
                sfor<0, 8>([&](auto I) {
                    constexpr int i = I.v, t = i >> 1, m = i & 1;
                    a1[t][m] = mfma(Wf[ks % 3][t], H[ks][m], a1[t][m]);
                    if constexpr (ks + 2 < 20 && (i & 1) == 0) Wf[(ks + 2) % 3][t] = wread(IC<t>{}, IC<ks + 2>{});
                    if constexpr (i == 3 && ks + 8 < 20) ISSUE_H(ks + 8);
                    if constexpr (i == 3 && (ks == 12 || ks == 13)) {
                        int li = w * 68 + (ks == 12 ? lane : 64 + (lane < 4 ? lane : 0)); li = li < 270 ? li : 269;
                        glds4((unsigned)((rr * 270 + li) * 128), ws + WS_W2R + (size_t)(x8 & 1) * 4 * 54 * 5120, lb + L_JUNK);
                    }
                    SB();
                });
            });
        }
        STAMP(20);
        f32x4 a2[3][2];
        sfor<0, 3>([&](auto TT) { const s16x8 cf = csfrag(4 + TT.v); a2[TT.v][0] = mfma(cf, Hm[0], z4); a2[TT.v][1] = mfma(cf, Hm[1], z4); });
        {
            s16x8 Wf[3][3];
            sfor<0, 2>([&](auto KS) { sfor<0, 3>([&](auto TT) { Wf[KS.v][TT.v] = wread(IC<4 + TT.v>{}, KS); }); });
            u32x2 e0, e1;
            sfor<0, 20>([&](auto KS) {
                constexpr int ks = KS.v;
                sfor<0, 6>([&](auto I) {
                    constexpr int i = I.v, t = i >> 1, m = i & 1;
                    a2[t][m] = mfma(Wf[ks % 3][t], H[ks][m], a2[t][m]);
                    if constexpr (ks + 2 < 20 && (i & 1) == 0) Wf[(ks + 2) % 3][t] = wread(IC<4 + t>{}, IC<ks + 2>{});
                    if constexpr (i == 3 || (i == 5 && (ks & 1) == 0)) { constexpr int pv = (ks >> 1) * 3 + ((ks & 1) ? 2 : (i == 5 ? 1 : 0)); ISSUE_V(pv / 5, pv % 5); }
                    if constexpr (ks >= 1 && ks < 17 && ((ks - 1) & 3) < 3) {
                        constexpr int u = (ks - 1) >> 2, ph = (ks - 1) & 3, P = u >> 1, mm = u & 1;
                        if constexpr (ph == 0 && i == 1) e0 = u32x2{relu2(pk2(a1[2 * P][mm][0], a1[2 * P][mm][1])), relu2(pk2(a1[2 * P][mm][2], a1[2 * P][mm][3]))};
                        if constexpr (ph == 1 && i == 1) e1 = u32x2{relu2(pk2(a1[2 * P + 1][mm][0], a1[2 * P + 1][mm][1])), relu2(pk2(a1[2 * P + 1][mm][2], a1[2 * P + 1][mm][3]))};
                        if constexpr (ph == 2 && i == 1)
                            st16_sc1((unsigned)(((mt0 + mm) * 54 + (Tp0 >> 1) + P) * 1024 + lane * 16), ws + WS_H1, u32x4{e0[0], e0[1], e1[0], e1[1]});
                    }
                    SB();
                });
            });
        }
        STAMP(22);
#pragma unroll
        for (int m = 0; m < 2; ++m) {
            st16_sc1((unsigned)(((mt0 + m) * 54 + (Tp0 >> 1) + 2) * 1024 + lane * 16), ws + WS_H1, fin8(a2[0][m], a2[1][m]));
            if (j2 < 12) st8_sc1((unsigned)(((mt0 + m) * 54 + (Tsg >> 1)) * 1024 + lane * 16 + (Tsg & 1) * 8), ws + WS_H1,
                                 u32x2{relu2(pk2(a2[2][m][0], a2[2][m][1])), relu2(pk2(a2[2][m][2], a2[2][m][3]))});
        }
        STAMP(24);
        drain_bar();
        if (tid < 16) flags_store(ws, 3, i2 * 16 + tid, j2, tag);
        STAMP(9);

        sfor<30, 70>([&](auto U) { ISSUE_V(U.v / 5, U.v % 5); });
        STAMP(44);
        await<63>(ws, 3, i2 * 16 + jw, tag, w, lane);
        STAMP(10);
        unsigned arun = (unsigned)(((8 * i2 + 4 * half) * 54 + ksb) * 1024 + lane * 16);
        const unsigned astep13 = nks == 14 ? 1024u : 0u;
        s16x8 A[4][4];
        unsigned aoff[4];
#pragma unroll
        for (int m = 0; m < 4; ++m) aoff[m] = ((w + m) & 3) * 54 * 1024;
        s16x8 st2v;
        ld16_sc1(st2v, (unsigned)((mtW * 16 + n) * 64 + g * 16), ws + WS_STAT2);
#define ISSUE_A(it, m) do { ld16_sc1(A[(it) & 3][m], arun + aoff[m], ws + WS_H1); \
            if ((m) == 3) { arun += ((it) == 12 ? astep13 : 1024u); asm volatile("" : "+v"(arun)); } } while (0)
        sfor<0, 3>([&](auto IT) { sfor<0, 4>([&](auto M) { ISSUE_A(IT.v, M.v); }); });
        f32x4 a[4][5];
#pragma unroll
        for (int m = 0; m < 4; ++m)
#pragma unroll
            for (int t = 0; t < 5; ++t) a[m][t] = z4;
        sfor<0, 14>([&](auto IT) {
            constexpr int it = IT.v;
            constexpr int ny = 4 * ((it + 2 < 13 ? it + 2 : 13) - it);
            if constexpr (it == 1) STAMP(25);
            if constexpr (it == 5) STAMP(26);
            if constexpr (it == 9) STAMP(27);
            if constexpr (it < 8)
                asm volatile("s_waitcnt vmcnt(%9)" : "+v"(A[it & 3][0]), "+v"(A[it & 3][1]), "+v"(A[it & 3][2]), "+v"(A[it & 3][3]),
                             "+a"(V[it][0]), "+a"(V[it][1]), "+a"(V[it][2]), "+a"(V[it][3]), "+a"(V[it][4]) : "i"(ny) : "memory");
            else
                asm volatile("s_waitcnt vmcnt(%9)" : "+v"(A[it & 3][0]), "+v"(A[it & 3][1]), "+v"(A[it & 3][2]), "+v"(A[it & 3][3]),
                             "+v"(V[it][0]), "+v"(V[it][1]), "+v"(V[it][2]), "+v"(V[it][3]), "+v"(V[it][4]) : "i"(ny) : "memory");
            SB();
            if (it < 13 || nks == 14) {
                sfor<0, 20>([&](auto I) {
                    constexpr int i = I.v, t = i >> 2, m = i & 3;
                    a[m][t] = mfma(V[it][t], A[it & 3][m], a[m][t]);
                    if constexpr ((i & 3) == 1 && i < 16 && it + 3 < 14) ISSUE_A(it + 3, i >> 2);
                    SB();
                });
            }
        });
        STAMP(11);
        asm volatile("" : "+v"(st2v));
        const f32x4 sv = __builtin_bit_cast(f32x4, st2v);
        const float smean = add_xor32(add_xor16(sv[0] + sv[2])) * (1.f / 640.f);
        const float rstd2 = __builtin_amdgcn_rsqf(fmaxf(add_xor32(add_xor16(sv[1] + sv[3])) * (1.f / 640.f) - smean * smean, 0.f) + 1e-5f);
        BAR();
#pragma unroll
        for (int m = 1; m < 4; ++m)
#pragma unroll
            for (int t = 0; t < 5; ++t) *(LAS f32x4*)(lb + ((((w + m) & 3) * 3 + (m - 1)) * 5 + t) * 1024 + lane * 16) = a[m][t];
        BAR();
        STAMP(46);
        f32x4 fin[5];
#pragma unroll
        for (int t = 0; t < 5; ++t) {
            const LAS char* p = (const LAS char*)(lb + (w * 15 + t) * 1024 + lane * 16);
            fin[t] = ((a[0][t] + *(const LAS f32x4*)p) + (*(const LAS f32x4*)(p + 5120) + *(const LAS f32x4*)(p + 10240))) * rstd2;
        }
        STAMP(47);
        LAS char* ot = (LAS char*)(lb + L_B + w * 2560);
#pragma unroll
        for (int t = 0; t < 5; ++t) {
            u32x2 fp = pack4(fin[t]);
            const u32x2 hp = *(LAS u32x2*)(lb + L_XRES + (16 * w + n) * 160 + 32 * t + 8 * g);
            f32x4 ov = {bflo(hp[0]) + bflo(fp[0]), bfhi(hp[0]) + bfhi(fp[0]), bflo(hp[1]) + bflo(fp[1]), bfhi(hp[1]) + bfhi(fp[1])};
            *(LAS u32x2*)(ot + n * 160 + 32 * t + 8 * g) = pack4(ov);
        }
        asm volatile("s_waitcnt lgkmcnt(0)" ::: "memory");
        int lane3 = lane; asm volatile("" : "+v"(lane3));
#pragma unroll
        for (int u = 0; u < 3; ++u) {
            const int q = 64 * u + lane3, row = q / 10, sl = q - row * 10;
            if (q < 160) *(GAS u32x4*)(out + (size_t)(mtW * 16 + row) * 1280 + 160 * c8 + 16 * sl) = *(LAS u32x4*)(ot + q * 16);
        }
    }
    STAMP(12); STAMPRT(63);
}

extern "C" void kernel_launch(void* const* d_in, const int* in_sizes, int n_in, void* d_out, int out_size, void* d_ws, size_t ws_size,
                              hipStream_t stream) {
    (void)in_sizes; (void)n_in; (void)out_size; (void)ws_size;
    fused_block<<<256, 256, 0, stream>>>((const char*)d_in[0], (const char*)d_in[1], (const char*)d_in[2], (const char*)d_in[4],
                                         (const char*)d_in[5], (char*)d_out, (char*)d_ws);
}
```

```cpp
#include <hip/hip_runtime.h>
#include <stdint.h>
#pragma clang diagnostic ignored "-Wint-to-pointer-cast"

typedef short s16x4 __attribute__((ext_vector_type(4)));
typedef short s16x8 __attribute__((ext_vector_type(8)));
typedef float f32x4 __attribute__((ext_vector_type(4)));
typedef unsigned u32x4 __attribute__((ext_vector_type(4)));
typedef unsigned u32x2 __attribute__((ext_vector_type(2)));
typedef unsigned long long u64;
#define LAS __attribute__((address_space(3)))
#define GAS __attribute__((address_space(1)))
#define DI __device__ __forceinline__
#define RLX_AGENT __ATOMIC_RELAXED, __HIP_MEMORY_SCOPE_AGENT
#define SB() __builtin_amdgcn_sched_barrier(0)

constexpr size_t WS_FLAG = 4096;
constexpr size_t WS_KVP = 262144;
constexpr size_t WS_STAT2 = 0x120000;
constexpr size_t WS_O = 2u << 20;
constexpr size_t WS_H = 5u << 20;
constexpr size_t WS_H1 = 8u << 20;
constexpr size_t WS_W1R = 16u << 20;
constexpr size_t WS_W2R = 19u << 20;
constexpr size_t WS_STAMP = 24u << 20;

constexpr int L_A = 0;
constexpr int L_B = 102400;
constexpr int L_XRES = L_B + 40960;
constexpr int L_M = 155648;
constexpr int L_MEAN = L_M;
constexpr int L_RSTD = L_M + 512;
constexpr int L_CSKV = L_M + 1024;
constexpr int L_CS4 = L_M + 1280;
constexpr int L_KVT = L_M + 1536;
constexpr int L_CS1 = L_M + 3328;
constexpr int L_JUNK = L_M + 5632;
constexpr int LDS_BYTES = 163840;
constexpr int CODE_LINES = 368;

enum { OP_X, OP_W, OP_R, OP_O, OP_Q, OP_T, OP_S, OP_U };
struct P1Op { int kind, a, b, ks, slot; };
constexpr P1Op P1SEQ[] = {
    {OP_O, 0, 0, -1, 0},
    {OP_U, 0, 0, -1, 0}, {OP_U, 1, 0, -1, 0},
    {OP_X, 0, 0, -1, 0}, {OP_X, 0, 1, -1, 0}, {OP_X, 0, 2, -1, 0}, {OP_X, 0, 3, -1, 0},
    {OP_W, 0, 0, -1, 0}, {OP_W, 0, 1, -1, 0}, {OP_W, 0, 2, -1, 0}, {OP_W, 0, 3, -1, 0}, {OP_W, 0, 4, -1, 0},
    {OP_U, 2, 0, -1, 0}, {OP_U, 3, 0, -1, 0},
    {OP_X, 1, 0, -1, 0}, {OP_X, 1, 1, -1, 0}, {OP_X, 1, 2, -1, 0}, {OP_X, 1, 3, -1, 0},
    {OP_O, 1, 0, -1, 0},
    {OP_W, 1, 0, -1, 0}, {OP_W, 1, 1, -1, 0}, {OP_W, 1, 2, -1, 0}, {OP_W, 1, 3, -1, 0}, {OP_W, 1, 4, -1, 0},
    {OP_X, 2, 0, -1, 0}, {OP_X, 2, 1, -1, 0}, {OP_X, 2, 2, -1, 0}, {OP_X, 2, 3, -1, 0},
    {OP_W, 2, 0, 0, 3}, {OP_W, 2, 1, 0, 8}, {OP_W, 2, 2, 0, 13},
    {OP_X, 3, 0, 1, 2}, {OP_X, 3, 1, 1, 6}, {OP_X, 3, 2, 1, 10}, {OP_X, 3, 3, 1, 14},
    {OP_W, 2, 3, 2, 3}, {OP_O, 2, 0, 2, 5}, {OP_W, 2, 4, 2, 8}, {OP_W, 3, 0, 2, 13},
    {OP_X, 4, 0, 3, 2}, {OP_X, 4, 1, 3, 6}, {OP_X, 4, 2, 3, 10}, {OP_X, 4, 3, 3, 14},
    {OP_W, 3, 1, 4, 3}, {OP_U, 4, 0, 4, 5}, {OP_W, 3, 2, 4, 8}, {OP_W, 3, 3, 4, 13},
    {OP_X, 5, 0, 5, 2}, {OP_X, 5, 1, 5, 6}, {OP_X, 5, 2, 5, 10}, {OP_X, 5, 3, 5, 14},
    {OP_W, 3, 4, 6, 3}, {OP_W, 4, 0, 6, 8}, {OP_W, 4, 1, 6, 13},
    {OP_X, 6, 0, 7, 2}, {OP_X, 6, 1, 7, 6}, {OP_X, 6, 2, 7, 10}, {OP_X, 6, 3, 7, 14},
    {OP_W, 4, 2, 8, 3}, {OP_W, 4, 3, 8, 8}, {OP_W, 4, 4, 8, 13},
    {OP_X, 7, 0, 9, 2}, {OP_X, 7, 1, 9, 6}, {OP_X, 7, 2, 9, 10}, {OP_X, 7, 3, 9, 14},
    {OP_R, 0, 0, 10, 3}, {OP_R, 1, 0, 10, 8}, {OP_R, 2, 0, 10, 13},
    {OP_X, 8, 0, 11, 2}, {OP_X, 8, 1, 11, 6}, {OP_X, 8, 2, 11, 10}, {OP_X, 8, 3, 11, 14},
    {OP_R, 3, 0, 12, 3}, {OP_R, 4, 0, 12, 8}, {OP_U, 5, 0, 12, 13},
    {OP_X, 9, 0, 13, 2}, {OP_X, 9, 1, 13, 6}, {OP_X, 9, 2, 13, 10}, {OP_X, 9, 3, 13, 14},
    {OP_Q, 0, 0, 15, 3}, {OP_Q, 0, 1, 15, 8}, {OP_Q, 0, 2, 15, 13}, {OP_Q, 0, 3, 16, 3}, {OP_Q, 0, 4, 16, 8},
    {OP_T, 0, 0, 17, 1}, {OP_T, 1, 0, 17, 2}, {OP_Q, 1, 0, 17, 3}, {OP_T, 2, 0, 17, 5}, {OP_T, 3, 0, 17, 6}, {OP_T, 4, 0, 17, 7},
    {OP_Q, 1, 1, 17, 8}, {OP_Q, 1, 2, 17, 13},
    {OP_S, 0, 0, 18, 1}, {OP_Q, 1, 3, 18, 3}, {OP_S, 1, 0, 18, 6}, {OP_Q, 1, 4, 18, 8}, {OP_S, 2, 0, 18, 11}, {OP_S, 3, 0, 19, 2}, {OP_S, 4, 0, 19, 7},
};
constexpr int P1N = sizeof(P1SEQ) / sizeof(P1SEQ[0]);
constexpr int p1_wait(int s) {
    int issued = 0, need = 0;
    for (int i = 0; i < P1N; ++i) {
        if (P1SEQ[i].ks < 2 * s - 1 || P1SEQ[i].ks < 0) issued = i + 1;
        if ((P1SEQ[i].kind == OP_X && P1SEQ[i].a == s) || (P1SEQ[i].kind == OP_W && P1SEQ[i].a <= (s >> 1))) need = need > i + 1 ? need : i + 1;
    }
    return issued - need;
}
constexpr bool p1_ok() { for (int s = 0; s < 10; ++s) if (p1_wait(s) < 0 || p1_wait(s) > 63) return false; return true; }
static_assert(p1_ok(), "P1a schedule: an op is waited for before it is issued");

#define STAMP(i) do {} while (0)
#define STAMPRT(i) do {} while (0)

template <int I> struct IC { static constexpr int v = I; };
template <int B, int E, class F> DI void sfor(F&& f) { if constexpr (B < E) { f(IC<B>{}); sfor<B + 1, E>(f); } }
#define OPAQUE_S(p) asm volatile("" : "+s"(p))

DI f32x4 mfma(s16x8 a, s16x8 b, f32x4 c) { return __builtin_amdgcn_mfma_f32_16x16x32_bf16(a, b, c, 0, 0, 0); }
DI s16x4 trread(unsigned addr) { return __builtin_bit_cast(s16x4, __builtin_amdgcn_ds_read_tr16_b64_v4i16((LAS s16x4*)addr)); }
DI s16x8 cat(s16x4 a, s16x4 b) { return __builtin_shufflevector(a, b, 0, 1, 2, 3, 4, 5, 6, 7); }
DI unsigned pk2(float lo, float hi) {
    typedef __bf16 bf2 __attribute__((ext_vector_type(2)));
    typedef float f2 __attribute__((ext_vector_type(2)));
    f2 v = {lo, hi};
    return __builtin_bit_cast(unsigned, __builtin_convertvector(v, bf2));
}
DI s16x8 pack8(f32x4 a, f32x4 b) {
    u32x4 r = {pk2(a[0], a[1]), pk2(a[2], a[3]), pk2(b[0], b[1]), pk2(b[2], b[3])};
    return __builtin_bit_cast(s16x8, r);
}
DI u32x2 pack4(f32x4 a) { u32x2 r = {pk2(a[0], a[1]), pk2(a[2], a[3])}; return r; }
DI float bflo(unsigned u) { return __uint_as_float(u << 16); }
DI float bfhi(unsigned u) { return __uint_as_float(u & 0xffff0000u); }
DI float phi(float t) { return (fmaxf(t, 0.f) + 1e-6f) + __builtin_amdgcn_fmed3f(__builtin_amdgcn_exp2f(t * 1.44269504088896f), 0.f, 1.f); }
DI float add_xor16(float v) { const unsigned u = __float_as_uint(v); auto r = __builtin_amdgcn_permlane16_swap(u, u, false, false); return __uint_as_float(r[0]) + __uint_as_float(r[1]); }
DI float add_xor32(float v) { const unsigned u = __float_as_uint(v); auto r = __builtin_amdgcn_permlane32_swap(u, u, false, false); return __uint_as_float(r[0]) + __uint_as_float(r[1]); }
constexpr int wq_pos(int grp) { return grp < 3 ? 16384 + 5120 * grp : grp < 7 ? 32768 + 5120 * (grp - 3) : 5120 * (grp - 7); }

template <int LOFF> DI void glds16(unsigned voff, const void* sbase, unsigned lds) {
    asm volatile("s_add_u32 m0, %2, %3\n\ts_nop 0\n\tglobal_load_lds_dwordx4 %0, %1" ::"v"(voff), "s"(sbase), "s"(lds), "i"(LOFF) : "memory", "scc");
}
template <int LOFF, int GOFF> DI void glds16i(unsigned voff, const void* sbase, unsigned lds) {
    asm volatile("s_add_u32 m0, %2, %3\n\ts_nop 0\n\tglobal_load_lds_dwordx4 %0, %1 offset:%4" ::"v"(voff), "s"(sbase), "s"(lds), "i"(LOFF - GOFF), "i"(GOFF) : "memory", "scc");
}
template <int LOFF> DI void glds16c(unsigned voff, const void* sbase, unsigned lds) {
    asm volatile("s_add_u32 m0, %2, %3\n\ts_nop 0\n\tglobal_load_lds_dwordx4 %0, %1 sc1" ::"v"(voff), "s"(sbase), "s"(lds), "i"(LOFF) : "memory", "scc");
}
DI void glds4(unsigned voff, const void* sbase, unsigned lds) {
    asm volatile("s_mov_b32 m0, %2\n\ts_nop 0\n\tglobal_load_lds_dword %0, %1" ::"v"(voff), "s"(sbase), "s"(lds) : "memory");
}
#define WAITV(n) asm volatile("s_waitcnt vmcnt(%0)" ::"i"(n) : "memory")
#define BARX() do { __builtin_amdgcn_s_barrier(); asm volatile("" ::: "memory"); } while (0)
#define BAR() do { asm volatile("s_waitcnt lgkmcnt(0)" ::: "memory"); __builtin_amdgcn_s_barrier(); asm volatile("" ::: "memory"); } while (0)

DI void ld16_sc1(s16x8& d, unsigned voff, const void* sbase) {
    asm volatile("global_load_dwordx4 %0, %1, %2 sc1" : "=v"(d) : "v"(voff), "s"(sbase) : "memory");
}
DI void ld16a_sc1(s16x8& d, unsigned voff, const void* sbase) {
    asm volatile("global_load_dwordx4 %0, %1, %2 sc1" : "=a"(d) : "v"(voff), "s"(sbase) : "memory");
}
DI void ld8_sc1(u64& d, const void* sbase) { asm volatile("global_load_dwordx2 %0, %1, %2 sc1" : "=v"(d) : "v"(0u), "s"(sbase) : "memory"); }
DI void st16_sc1(unsigned voff, void* sbase, u32x4 v) {
    asm volatile("global_store_dwordx4 %0, %1, %2 sc1\n\ts_nop 1" ::"v"(voff), "v"(v), "s"(sbase) : "memory");
}
DI void st8_sc1(unsigned voff, void* sbase, u32x2 v) {
    asm volatile("global_store_dwordx2 %0, %1, %2 sc1" ::"v"(voff), "v"(v), "s"(sbase) : "memory");
}
DI void drain_bar() { WAITV(0); BAR(); }
DI void flags_store(char* ws, int arr, int line, int slot, u64 tag) {
    __hip_atomic_store((GAS u64*)(ws + WS_FLAG + (size_t)arr * 32768 + (size_t)line * 128) + slot, tag, RLX_AGENT);
}
template <int NV> DI void await(char* ws, int arr, int line, u64 tag, int w, int lane) {
    if (w == 0) {
        GAS u64* fl = (GAS u64*)(ws + WS_FLAG + (size_t)arr * 32768 + (size_t)line * 128) + (lane & 15);
        for (unsigned spins = 0; spins < (1u << 20); ++spins) {
            u64 f = __hip_atomic_load(fl, RLX_AGENT);
            if (__all(f == tag)) break;
        }
    }
    WAITV(NV);
    BAR();
}

__global__ void __launch_bounds__(256, 1)
fused_block(const char* __restrict__ x, const char* __restrict__ wqkv, const char* __restrict__ wo, const char* __restrict__ w1,
            const char* __restrict__ w2, char* __restrict__ out, char* __restrict__ ws) {
    __shared__ __attribute__((aligned(16))) char smem[LDS_BYTES];
    const unsigned lb = (unsigned)(uintptr_t)(LAS char*)smem;
    const int tid = threadIdx.x, lane = tid & 63;
    const int w = __builtin_amdgcn_readfirstlane(tid >> 6);
    const int b = blockIdx.x, x8 = b & 7, rr = b >> 3;
    const int i1 = 4 * (x8 >> 1) + (rr >> 3), j1 = 8 * (x8 & 1) + (rr & 7);
    const int i2 = i1, j2 = j1;
    const int half = (rr >> 2) & 1, c8 = 4 * (x8 & 1) + (rr & 3), jw = 8 * half + c8;
    const int g = lane >> 4, n = lane & 15, q4 = n >> 2, p4 = lane & 3;
    const s16x8 ones = {0x3F80, 0x3F80, 0x3F80, 0x3F80, 0x3F80, 0x3F80, 0x3F80, 0x3F80};
    const f32x4 z4 = {0, 0, 0, 0};
    STAMP(0); STAMPRT(62);

    s16x8 X[20][2];
    f32x4 acc[5][2], sq[2], rs[2], csA, csB;
    unsigned voffW[5], voffX[4];
    const char* xb = x + (size_t)i1 * 128 * 1280;
    const unsigned ldsXw = lb + L_B + w * 4096, ldsWw = lb + L_A + w * 5120;
#pragma unroll
    for (int u = 0; u < 4; ++u)
        voffX[u] = (32 * w + 8 * u + (lane >> 3)) * 1280 + 16 * ((lane & 7) ^ ((lane >> 4) + 4 * (u & 1)));
#define ISSUE_X(s, u) glds16i<((s) % 3) * 16384 + (u) * 1024, (s) * 128>(voffX[u], xb, ldsXw)
#define ISSUE_XS(s) do { ISSUE_X(s, 0); ISSUE_X(s, 1); ISSUE_X(s, 2); ISSUE_X(s, 3); } while (0)
#pragma unroll
    for (int u = 0; u < 5; ++u) {
        int q = 64 * u + lane, row = q / 10, s = q - row * 10;
        row ^= ((row >> 3) & 1) << 2;
        voffW[u] = row * 3840 + (s < 5 ? (640 + 40 * j1) * 2 + 16 * s : (1280 + 40 * j1) * 2 + 16 * (s - 5));
    }
    const char* wkb = wqkv + (size_t)w * (32 * 3840);
    const char* wrun = wkb;
#define ISSUE_W(t, u) do { glds16<(t) * 20480 + (u) * 1024>(voffW[u], wrun, ldsWw); if ((u) == 4) { wrun += 128 * 3840; OPAQUE_S(wrun); } } while (0)
    unsigned voffQ[5];
#pragma unroll
    for (int u = 0; u < 5; ++u) {
        int q = 64 * u + lane, row = q / 5, s = q - row * 5;
        row ^= ((row >> 3) & 1) << 2;
        voffQ[u] = row * 3840 + 80 * j1 + 16 * s;
    }
    auto issueQ = [&](auto A, auto U) {
        constexpr int a = A.v, u = U.v;
        const int grp = a == 0 ? (w < 2 ? w : 2) : a == 1 ? 3 + w : 7 + (w < 2 ? w : 2);
        const int pos = a == 0 ? 16384 + 5120 * grp : a == 1 ? 32768 + 5120 * w : 5120 * (grp - 7);
        glds16<u * 1024>(voffQ[u], wqkv + (size_t)grp * (64 * 3840), lb + L_B + pos);
    };
    const unsigned raR = lb + L_A + (4 * g + q4) * 128 + 16 * ((2 * w + (p4 >> 1)) ^ ((((4 * g + q4) >> 1) & 3) << 1)) + 8 * (p4 & 1);
    s16x8 Rf[5];
    auto runit = [&](auto R, unsigned& isw1, unsigned& ksr, unsigned& cl) {
        constexpr int r = R.v;
        const unsigned ub = (unsigned)b;
        unsigned v;
        if constexpr (r < 2) { isw1 = 1; v = ub + 256 * r; }
        else if constexpr (r == 2) { isw1 = ub < 28; v = isw1 ? ub + 512 : ub - 28; }
        else if constexpr (r == 3) { isw1 = 0; v = ub + 228; }
        else { isw1 = 0; v = ub < 56 ? ub + 484 : ub + 228; }
        ksr = isw1 ? (v * 2428u) >> 16 : (v * 6554u) >> 16;
        cl = v - ksr * (isw1 ? 27u : 10u);
    };
    u64 epoch;
    auto p1issue = [&](auto N) {
        constexpr P1Op op = P1SEQ[N.v];
        if constexpr (op.kind == OP_X) ISSUE_X(op.a, op.b);
        else if constexpr (op.kind == OP_W) ISSUE_W(op.a, op.b);
        else if constexpr (op.kind == OP_R) {
            unsigned isw1, ksr, cl;
            runit(IC<op.a>{}, isw1, ksr, cl);
            const unsigned pitch = isw1 ? 3456u : 1280u;
            const unsigned rrow = 8 * w + (lane >> 3);
            glds16<op.a * 4096>(rrow * pitch + 16 * ((lane & 7) ^ (((rrow >> 1) & 3) << 1)), (isw1 ? w1 : w2) + (ksr * 32 * pitch + cl * 128), lb + L_A + w * 1024);
        } else if constexpr (op.kind == OP_U) {
            const unsigned wv = rr * 4 + w;
            if constexpr (op.a == 0 || op.a == 2) {
                const unsigned l = lane < 20 ? lane : 0, rw = (l * 52u) >> 8;
                glds4((4 * wv + rw) * 1280 + (l - 5 * rw + (op.a == 2 ? 5 : 0)) * 128, x + (size_t)(x8 >> 1) * 512 * 1280, lb + L_JUNK);
            } else if constexpr (op.a == 1 || op.a == 3) {
                const unsigned li = wv * 25 + (lane < 25 ? lane : 0), rw = (li * 6554u) >> 16, rem = li - rw * 10;
                glds4((rw + (op.a == 3 ? 320 : 0)) * 3840 + 1280 + (rem >= 5 ? 640 : 0) + (x8 & 1) * 640 + rem * 128, wqkv, lb + L_JUNK);
            } else if constexpr (op.a == 4) {
                const unsigned li = wv * 25 + (lane < 25 ? lane : 0), rw = (li * 13108u) >> 16, rem = li - rw * 5;
                glds4(rw * 3840 + (x8 & 1) * 640 + rem * 128, wqkv, lb + L_JUNK);
            } else {
                const unsigned li = wv * 25 + (lane < 25 ? lane : 0), rw = (li * 13108u) >> 16, rem = li - rw * 5;
                glds4(rw * 1280 + (x8 & 1) * 640 + rem * 128, wo, lb + L_JUNK);
            }
        } else if constexpr (op.kind == OP_Q) issueQ(IC<op.a>{}, IC<op.b>{});
        else if constexpr (op.kind == OP_T) {
            Rf[op.a] = cat(trread(raR + op.a * 4096), trread(raR + op.a * 4096 + 2048));
        } else if constexpr (op.kind == OP_S) {
            unsigned isw1, ksr, cl;
            runit(IC<op.a>{}, isw1, ksr, cl);
            const unsigned T = 4 * cl + w, c8r = (T * 52u) >> 8;
            const unsigned dst = isw1 ? (unsigned)WS_W1R + (T * 20 + ksr) * 1024 : (unsigned)WS_W2R + ((c8r * 54 + ksr) * 5 + (T - 5 * c8r)) * 1024;
            st16_sc1((unsigned)(lane * 16), ws + dst, __builtin_bit_cast(u32x4, Rf[op.a]));
        } else if constexpr (op.a == 0) {
            u64 pc;
            asm volatile("s_getpc_b64 %0" : "=s"(pc));
            pc &= ~(u64)127;
            const unsigned cl_ = (rr * 4 + w) * 3 + ((lane & 3) < 3 ? (lane & 3) : 0);
            glds4((cl_ < CODE_LINES ? cl_ : CODE_LINES - 1) * 128, (const void*)pc, lb + L_JUNK);
        } else if constexpr (op.a == 2) {
            const char* p = lane < 24 ? ws + (size_t)lane * (1 << 20) : lane < 27 ? out + (size_t)(lane - 24) * 1300000 : lane < 30 ? w1 + (size_t)(lane - 27) * 1100000
                          : lane < 33 ? w2 + (size_t)(lane - 30) * 1100000 : wo;
            asm volatile("s_mov_b32 m0, %1\n\ts_nop 0\n\tglobal_load_lds_dword %0, off" ::"v"(p), "s"(lb + L_JUNK) : "memory");
        } else ld8_sc1(epoch, ws);
    };
    sfor<0, P1N>([&](auto N) { if constexpr (P1SEQ[N.v].ks < 0) p1issue(N); });
    const int ro1 = 8 * g + q4 + 4 * (g & 1), ro2 = 8 * g + q4 + 4 * (1 - (g & 1));
    {
        unsigned vW1[2], vW2[2], vM1[2], vM2[2], vC1[2], vC2[2];
        const int mo = (p4 == 0 ? 32 : p4 == 1 ? 72 : p4 == 2 ? 112 : 152);
#pragma unroll
        for (int h = 0; h < 2; ++h) {
            vW1[h] = lb + L_A + h * 51200 + ro1 * 160 + 8 * p4; vW2[h] = lb + L_A + h * 51200 + ro2 * 160 + 8 * p4;
            vM1[h] = lb + L_A + h * 51200 + ro1 * 160 + mo; vM2[h] = lb + L_A + h * 51200 + ro2 * 160 + mo;
            vC1[h] = vW1[h] + 40 * w; vC2[h] = vW2[h] + 40 * w;
            asm volatile("" : "+v"(vW1[h]), "+v"(vW2[h]), "+v"(vM1[h]), "+v"(vM2[h]), "+v"(vC1[h]), "+v"(vC2[h]));
        }
        unsigned vD1 = vM1[0] + w * 5120, vD2 = vM2[0] + w * 5120;
        unsigned vX[2];
#pragma unroll
        for (int kl = 0; kl < 2; ++kl) vX[kl] = lb + L_B + w * 4096 + n * 128 + 16 * ((4 * kl + g) ^ (n >> 1));
#pragma unroll
        for (int t = 0; t < 5; ++t) acc[t][0] = acc[t][1] = z4;
        sq[0] = sq[1] = rs[0] = rs[1] = csA = csB = z4;
        s16x4 Wl[2][7], Wh[2][7];
        auto side = [&](auto KS, auto I) {
            constexpr int ks = KS.v, i = I.v, r = ks & 1, s = ks >> 1, kl = ks & 1, h = ks >= 10;
            constexpr unsigned ko = (ks - 10 * h) * 5120;
            if constexpr (i < 2) X[ks][i] = *(volatile LAS s16x8*)(vX[kl] + (s % 3) * 16384 + i * 2048);
            else if constexpr (i < 10) { constexpr int t = (i - 2) >> 1; if constexpr (i & 1) Wh[r][t] = trread(vW2[h] + ko + 40 * t); else Wl[r][t] = trread(vW1[h] + ko + 40 * t); }
            else if constexpr (i < 12) { if constexpr (i & 1) Wh[r][4] = trread(vM2[h] + ko); else Wl[r][4] = trread(vM1[h] + ko); }
            else if constexpr (i < 14) { if constexpr (i & 1) Wh[r][5] = trread(vC2[h] + ko); else Wl[r][5] = trread(vC1[h] + ko); }
            else {
                constexpr int u = ks >> 2;
                if constexpr (i & 1) { Wh[r][6] = trread(vD2 + (u % 3) * 20480); if constexpr (u == 2) { vD2 += 61440; asm volatile("" : "+v"(vD2)); } }
                else { Wl[r][6] = trread(vD1 + (u % 3) * 20480); if constexpr (u == 2) { vD1 += 61440; asm volatile("" : "+v"(vD1)); } }
            }
        };
        STAMP(48);
        WAITV(p1_wait(0));
        BARX();
        STAMP(49);
        sfor<0, 14>([&](auto I) { side(IC<0>{}, I); });
        SB();
        sfor<0, 20>([&](auto KS) {
            constexpr int ks = KS.v;
            if constexpr ((ks & 1) && ks < 19) {
                constexpr int s = (ks + 1) >> 1;
                WAITV(p1_wait(s));
                if constexpr (s >= 8) BAR();
                else if constexpr ((s & 1) == 0) BARX();
                SB();
            }
            if constexpr (ks == 4) STAMP(50);
            if constexpr (ks == 8) STAMP(51);
            if constexpr (ks == 12) STAMP(52);
            if constexpr (ks == 16) STAMP(53);
            constexpr int r = ks & 1;
            constexpr int nm = ((ks & 3) == 3) ? 16 : 15;
            constexpr int nside = ks < 19 ? (((ks + 1) & 3) == 3 ? 16 : 14) : 0;
            sfor<0, 16>([&](auto I) {
                constexpr int i = I.v;
                if constexpr (i < nm) {
                    if constexpr (i < 10) { constexpr int t = i >> 1, m = i & 1; acc[t][m] = mfma(X[ks][m], cat(Wl[r][t], Wh[r][t]), acc[t][m]); }
                    else if constexpr (i == 10) csA = mfma(ones, cat(Wl[r][5], Wh[r][5]), csA);
                    else if constexpr (i < 13) sq[i - 11] = mfma(X[ks][i - 11], X[ks][i - 11], sq[i - 11]);
                    else if constexpr (i < 15) rs[i - 13] = mfma(X[ks][i - 13], ones, rs[i - 13]);
                    else csB = mfma(ones, cat(Wl[r][6], Wh[r][6]), csB);
                }
                if constexpr (i < nside) side(IC<ks + 1>{}, I);
                sfor<0, P1N>([&](auto N) { if constexpr (P1SEQ[N.v].ks == ks && P1SEQ[N.v].slot == i) p1issue(N); });
                SB();
            });
        });
    }
    asm volatile("" : "+v"(epoch), "+v"(csA), "+v"(csB));
    const u64 tag = epoch + 1;
    STAMP(1);

    if (g == 0) { ((LAS float*)(lb + L_CSKV))[16 * w + n] = csA[0]; ((LAS float*)(lb + L_CS4))[16 * w + n] = csB[0]; }
    f32x4 lnA[2], lnB[2];
#pragma unroll
    for (int m = 0; m < 2; ++m) {
        f32x4 mean4, rstd4;
#pragma unroll
        for (int r = 0; r < 4; ++r) {
            const float s2 = __uint_as_float(__builtin_amdgcn_ds_bpermute(4 * (20 * g + r), __float_as_uint(sq[m][r])));
            const float mean = rs[m][r] * (1.f / 640.f);
            const float rstd = __builtin_amdgcn_rsqf(fmaxf(s2 * (1.f / 640.f) - mean * mean, 0.f) + 1e-5f);
            mean4[r] = mean; rstd4[r] = rstd;
            lnA[m][r] = rstd; lnB[m][r] = -mean * rstd;
        }
        *(LAS f32x4*)(lb + L_MEAN + (32 * w + 16 * m + 4 * g) * 4) = mean4;
        *(LAS f32x4*)(lb + L_RSTD + (32 * w + 16 * m + 4 * g) * 4) = rstd4;
    }
    BAR();
    STAMP(31);
    {
        float cs[5];
#pragma unroll
        for (int t = 0; t < 4; ++t) cs[t] = ((LAS float*)(lb + L_CSKV))[16 * t + n];
        {
            LAS float* c4 = (LAS float*)(lb + L_CS4);
            cs[4] = (c4[n] + c4[16 + n]) + (c4[32 + n] + c4[48 + n]);
        }
        s16x8 F[4], MA, MB;
        sfor<0, 5>([&](auto TT) {
            constexpr int t = TT.v;
            issueQ(IC<2>{}, TT);
            f32x4 v[2], vb_[2];
#pragma unroll
            for (int m = 0; m < 2; ++m)
#pragma unroll
                for (int r = 0; r < 4; ++r) {
                    const float y = lnA[m][r] * acc[t][m][r] + lnB[m][r] * cs[t];
                    if (t < 2) v[m][r] = phi(y);
                    else if (t < 4) v[m][r] = y;
                    else { v[m][r] = n < 8 ? phi(y) : y; vb_[m][r] = n < 8 ? 1.f : y; }
                }
            if constexpr (t < 4) F[t] = pack8(v[0], v[1]);
            else { MA = pack8(v[0], v[1]); MB = pack8(vb_[0], vb_[1]); }
            SB();
        });
        STAMP(33);
        f32x4 P1 = mfma(F[0], F[2], z4), P2 = mfma(F[0], MB, z4), P3 = mfma(MA, F[2], z4), P4 = mfma(MA, MB, z4);
        f32x4 P5 = mfma(F[1], F[3], z4), P6 = mfma(F[1], MB, z4), P7 = mfma(MA, F[3], z4);
        LAS char* red = (LAS char*)(lb + L_A + 20480 + w * 3360);
        *(LAS f32x4*)(red + (n * 20 + 4 * g) * 4) = P1;
        *(LAS f32x4*)(red + (420 + n * 20 + 4 * g) * 4) = P5;
        if (n == 0 || (n >= 8 && n < 12)) *(LAS f32x4*)(red + ((n == 0 ? 20 : 8 + n) * 20 + 4 * g) * 4) = P2;
        if (n == 0 || n >= 12) *(LAS f32x4*)(red + (420 + (n == 0 ? 20 : 4 + n) * 20 + 4 * g) * 4) = P6;
        if (g < 2) *(LAS f32x4*)(red + (420 * g + n * 20 + 16) * 4) = (g == 0 ? P3 : P7);
        if (g < 2 && (n == 0 || (g == 0 ? (n >= 8 && n < 12) : n >= 12)))
            *(LAS f32x4*)(red + (420 * g + (n == 0 ? 20 : (g == 0 ? 8 + n : 4 + n)) * 20 + 16) * 4) = P4;
    }
    STAMP(34);
    BAR();
    STAMP(35);
    if (tid < 210) {
        LAS char* red = (LAS char*)(lb + L_A + 20480 + tid * 16);
        f32x4 s = (*(LAS f32x4*)(red) + *(LAS f32x4*)(red + 3360)) + (*(LAS f32x4*)(red + 6720) + *(LAS f32x4*)(red + 10080));
        st16_sc1((unsigned)(tid * 16), ws + WS_KVP + (size_t)(j1 * 16 + i1) * 3360, __builtin_bit_cast(u32x4, s));
    }
    STAMP(36);
    drain_bar();
    if (tid < 16) flags_store(ws, 0, tid * 16 + j1, i1, tag);
    STAMP(2);

    unsigned voff160[5];
    {
        int lane2 = lane; asm volatile("" : "+v"(lane2));
#pragma unroll
        for (int u = 0; u < 5; ++u) { int q = 64 * u + lane2, row = q / 10, s = q - row * 10; voff160[u] = row * 1280 + 160 * c8 + 16 * s; }
    }
    s16x8 Fa[2], Fb[2], kp[16];
    const char* worun = wo + (size_t)w * (32 * 1280);
    {
        const unsigned vQ1 = lb + L_B + ro1 * 80 + 8 * p4, vQ2 = lb + L_B + ro2 * 80 + 8 * p4;
        const int to = (p4 == 0 ? 32 : p4 == 1 ? 72 : p4 == 2 ? 32 : 72);
        const unsigned vT1 = lb + L_B + ro1 * 80 + to, vT2 = lb + L_B + ro2 * 80 + to;
        f32x4 aq[3][2], cq[3];
#pragma unroll
        for (int t = 0; t < 3; ++t) aq[t][0] = aq[t][1] = cq[t] = z4;
        s16x4 Wl[2][3], Wh[2][3];
        auto side = [&](auto KS, auto I) {
            constexpr int ks = KS.v, i = I.v, r = ks & 1, t = i >> 1;
            constexpr unsigned ko = wq_pos(ks >> 1) + (ks & 1) * 2560;
            if constexpr (t < 2) { if constexpr (i & 1) Wh[r][t] = trread(vQ2 + ko + 40 * t); else Wl[r][t] = trread(vQ1 + ko + 40 * t); }
            else { if constexpr (i & 1) Wh[r][2] = trread(vT2 + ko); else Wl[r][2] = trread(vT1 + ko); }
        };
        sfor<0, 6>([&](auto I) { side(IC<0>{}, I); });
        SB();
        const char* kb = ws + WS_KVP + (size_t)j1 * 16 * 3360;
        const unsigned kvo = (tid < 210 ? tid : 209) * 16;
        sfor<0, 20>([&](auto KS) {
            constexpr int ks = KS.v, r = ks & 1;
            if constexpr (ks == 13) { STAMP(3); await<63>(ws, 0, i1 * 16 + j1, tag, w, lane); STAMP(4); SB(); }
            sfor<0, 9>([&](auto I) {
                constexpr int i = I.v, t = i / 3, m = i % 3;
                const s16x8 wf = cat(Wl[r][t], Wh[r][t]);
                if constexpr (m < 2) aq[t][m] = mfma(wf, X[ks][m], aq[t][m]);
                else cq[t] = mfma(wf, ones, cq[t]);
                if constexpr (i < 6 && ks < 19) side(IC<ks + 1>{}, I);
                if constexpr ((i == 3 || i == 8) && ks < 6) {
                    constexpr int pc_ = 2 * ks + (i == 8), tt = pc_ / 5, u = pc_ % 5;
                    glds16<tt * 20480 + u * 1024>(voff160[u], worun, ldsWw);
                    if constexpr (u == 4) { worun += 128 * 1280; OPAQUE_S(worun); }
                }
                if constexpr (ks >= 13 && ks < 19 && (i == 2 || i == 5 || i == 8) && 3 * (ks - 13) + i / 3 < 16) {
                    constexpr int pi = 3 * (ks - 13) + i / 3;
                    ld16_sc1(kp[pi], kvo + pi * 3360, kb);
                }
                SB();
            });
        });
        STAMP(37);
#pragma unroll
        for (int m = 0; m < 2; ++m) {
            const float mean = ((LAS float*)(lb + L_MEAN))[32 * w + 16 * m + n], rstd = ((LAS float*)(lb + L_RSTD))[32 * w + 16 * m + n];
            f32x4 v[3];
#pragma unroll
            for (int t = 0; t < 3; ++t)
#pragma unroll
                for (int r = 0; r < 4; ++r) {
                    float y = phi(rstd * aq[t][m][r] - (mean * rstd) * cq[t][r]);
                    v[t][r] = (t == 2 && g >= 2) ? 0.f : y;
                }
            Fa[m] = pack8(v[0], v[2]);
            Fb[m] = pack8(v[1], v[2]);
        }
        asm volatile("" : "+v"(Fa[0]), "+v"(Fa[1]), "+v"(Fb[0]), "+v"(Fb[1]));
    }

    {
        asm volatile("s_waitcnt vmcnt(0)" : "+v"(kp[0]), "+v"(kp[1]), "+v"(kp[2]), "+v"(kp[3]), "+v"(kp[4]), "+v"(kp[5]), "+v"(kp[6]), "+v"(kp[7]),
                     "+v"(kp[8]), "+v"(kp[9]), "+v"(kp[10]), "+v"(kp[11]), "+v"(kp[12]), "+v"(kp[13]), "+v"(kp[14]), "+v"(kp[15])::"memory");
        f32x4 s0 = __builtin_bit_cast(f32x4, kp[0]), s1 = __builtin_bit_cast(f32x4, kp[1]);
#pragma unroll
        for (int i = 2; i < 16; i += 2) { s0 += __builtin_bit_cast(f32x4, kp[i]); s1 += __builtin_bit_cast(f32x4, kp[i + 1]); }
        if (tid < 210) *(LAS u32x2*)(lb + L_KVT + tid * 8) = pack4(s0 + s1);
    }
    STAMP(38);
    BAR();
    STAMP(39);
    {
        const u32x2 zz = {0, 0};
        auto kvchunk = [&](int head, int e, int c) { return *(LAS u32x2*)(lb + L_KVT + (head * 420 + e * 20 + 4 * c) * 2); };
        auto mk = [&](u32x2 lo, u32x2 hi) { u32x4 r = {lo[0], lo[1], hi[0], hi[1]}; return __builtin_bit_cast(s16x8, r); };
        s16x8 Ama = mk(kvchunk(0, n, g), g == 0 ? kvchunk(0, n, 4) : zz);
        s16x8 Amb = mk(kvchunk(1, n, g), g == 1 ? kvchunk(1, n, 4) : zz);
        s16x8 Aza = mk(kvchunk(0, 20, g), g == 0 ? kvchunk(0, 20, 4) : zz);
        s16x8 Azb = mk(kvchunk(1, 20, g), g == 1 ? kvchunk(1, 20, 4) : zz);
        u32x2 ta_lo = kvchunk(0, 16 + (n & 3), g), ta_hi = kvchunk(0, 16 + (n & 3), 4);
        u32x2 tb_lo = kvchunk(1, 16 + (n & 3), g), tb_hi = kvchunk(1, 16 + (n & 3), 4);
        s16x8 Ata = mk(n < 4 ? ta_lo : zz, (n < 4 && g == 0) ? ta_hi : zz);
        s16x8 Atb = mk((n >= 4 && n < 8) ? tb_lo : zz, (n >= 4 && n < 8 && g == 1) ? tb_hi : zz);
#pragma unroll
        for (int m = 0; m < 2; ++m) {
            f32x4 Da = mfma(Ama, Fa[m], z4), Db = mfma(Amb, Fb[m], z4);
            f32x4 Dt = mfma(Atb, Fb[m], mfma(Ata, Fa[m], z4));
            f32x4 Dza = mfma(Aza, Fa[m], z4), Dzb = mfma(Azb, Fb[m], z4);
            const float ra = __builtin_amdgcn_rcpf(Dza[0] + 1e-6f), rb = __builtin_amdgcn_rcpf(Dzb[0] + 1e-6f);
            const int mt = 8 * i1 + 2 * w + m;
            s16x8 of = pack8(Da * ra, Db * rb);
            st16_sc1((unsigned)((mt * 20 + j1) * 1024 + lane * 16), ws + WS_O, __builtin_bit_cast(u32x4, of));
            u32x2 tl = pack4(Dt * (g == 0 ? ra : rb));
            const int t = j1 >> 1;
            if (g < 2) st8_sc1((unsigned)((mt * 20 + 16 + (t >> 1)) * 1024 + (n + 16 * (2 * (j1 & 1) + g)) * 16 + (t & 1) * 8), ws + WS_O, tl);
        }
    }
    STAMP(40);
    drain_bar();
    if (tid < 16) flags_store(ws, 1, i1 * 16 + tid, j1, tag);
    STAMP(5);

    const int T0 = j2 < 12 ? 7 * j2 : 84 + 6 * (j2 - 12);
    const int odd = (j2 < 12) ? (j2 & 1) : 0;
    const int Tp0 = T0 + odd;
    const int Tsg = j2 < 12 ? (odd ? T0 : T0 + 6) : Tp0;
    const unsigned vlan16 = lane * 16;
    sfor<12, 25>([&](auto P) {
        constexpr int tt = P.v / 5, u = P.v % 5;
        glds16<tt * 20480 + u * 1024>(voff160[u], worun, ldsWw);
        if constexpr (u == 4) { worun += 128 * 1280; OPAQUE_S(worun); }
    });
    const int mtW = 8 * i2 + 4 * half + w;
    {
        const char* xs_ = x + (size_t)(8 * i2 + 4 * half) * 16 * 1280;
        if (w < 2) sfor<0, 5>([&](auto U) { glds16<U.v * 1024>(voff160[U.v], xs_ + (size_t)w * (32 * 1280), lb + L_XRES + w * 5120); });
    }
    STAMP(45);
    await<0>(ws, 1, i2 * 16 + jw, tag, w, lane);
    STAMP(6);
    {
        s16x8 O[20];
        unsigned orun = (unsigned)(mtW * 20 * 1024 + lane * 16);
#define ISSUE_O(i) do { ld16_sc1(O[i], orun, ws + WS_O); orun += 1024u; asm volatile("" : "+v"(orun)); } while (0)
        sfor<0, 8>([&](auto I) { ISSUE_O(I.v); });
        unsigned vA = lb + L_A + (4 * g + q4) * 160 + 8 * p4, vA2 = vA + 8 * 6400;
        const unsigned vB = lb + L_A + (20 * g + 16 + q4) * 160 + 8 * p4;
        unsigned vB2 = vB + 2 * 25600;
        asm volatile("" : "+v"(vA), "+v"(vA2), "+v"(vB2));
        f32x4 a[5];
#pragma unroll
        for (int t = 0; t < 5; ++t) a[t] = z4;
        s16x4 Wl[3][5], Wh[3][5];
        auto side = [&](auto I, auto J) {
            constexpr int i = I.v, j = J.v, r = i % 3, t = j >> 1;
            if constexpr (i < 16) {
                const unsigned a_ = (i < 8 ? vA : vA2) + (i & 7) * 6400 + 32 * t;
                if constexpr (j & 1) Wh[r][t] = trread(a_ + 3200); else Wl[r][t] = trread(a_);
            } else {
                const unsigned a_ = (i < 18 ? vB : vB2) + (i & 1) * 25600 + 32 * t;
                if constexpr (j & 1) Wh[r][t] = trread(a_ + 12800); else Wl[r][t] = trread(a_);
            }
        };
        sfor<0, 2>([&](auto I) { sfor<0, 10>([&](auto J) { side(I, J); }); });
        SB();
        sfor<0, 20>([&](auto I) {
            constexpr int i = I.v;
            constexpr int lastO = i + 7 < 19 ? i + 7 : 19;
            constexpr int firstk = i < 8 ? 0 : i - 8;
            constexpr int nW1 = (i + 1) / 2 - (firstk + 1) / 2;
            constexpr int nT = (i == 13 ? 1 : i > 13 ? 2 : 0);
            asm volatile("s_waitcnt vmcnt(%1)" : "+v"(O[i]) : "i"((lastO - i) + nW1 + nT) : "memory");
            SB();
            sfor<0, 5>([&](auto T) {
                constexpr int t = T.v;
                if constexpr (t == 0 && (i == 12 || i == 13)) {
                    int li = rr * 280 + w * 70 + (i == 12 ? lane : 64 + (lane < 6 ? lane : 0)); li = li < ((x8 & 1) ? 8320 : 8960) ? li : 8319;
                    glds4((unsigned)(li * 128), ws + WS_W1R + (x8 & 1) * 56 * 20480, lb + L_JUNK);
                }
                a[t] = mfma(cat(Wl[i % 3][t], Wh[i % 3][t]), O[i], a[t]);
                if constexpr (i + 2 < 20) { side(IC<i + 2>{}, IC<2 * t>{}); side(IC<i + 2>{}, IC<2 * t + 1>{}); }
                if constexpr (t == 1 && i + 8 < 20) ISSUE_O(i + 8);
                if constexpr (t == 3 && (i & 1) == 0) {
                    constexpr int pc_ = i / 2;
                    const int Tg = pc_ < 5 ? Tp0 + 5 : Tsg;
                    glds16c<(5 + pc_ / 5) * 20480 + (pc_ % 5) * 4096>(vlan16, ws + WS_W1R + (size_t)(Tg * 20 + (pc_ % 5) * 4 + w) * 1024, lb + w * 1024);
                }
                SB();
            });
        });
        STAMP(28);
        float su = 0.f, s2 = 0.f;
        u32x2 hpk[5];
#pragma unroll
        for (int t = 0; t < 5; ++t) {
            const u32x2 xr = *(LAS u32x2*)(lb + L_XRES + (16 * w + n) * 160 + 32 * t + 8 * g);
            const f32x4 hv = f32x4{bflo(xr[0]), bfhi(xr[0]), bflo(xr[1]), bfhi(xr[1])} + a[t];
            hpk[t] = pack4(hv);
            *(LAS u32x2*)(lb + L_XRES + (16 * w + n) * 160 + 32 * t + 8 * g) = hpk[t];
#pragma unroll
            for (int r = 0; r < 4; ++r) { su += hv[r]; s2 += hv[r] * hv[r]; }
        }
        const unsigned hb = (unsigned)((mtW * 20 + ((5 * c8) >> 1)) * 1024 + lane * 16);
        const int od = c8 & 1;
        st8_sc1(hb + (od ? 8 : 2048), ws + WS_H, od ? hpk[0] : hpk[4]);
        { const u32x2 p0 = od ? hpk[1] : hpk[0], p1 = od ? hpk[2] : hpk[1]; st16_sc1(hb + (od ? 1024 : 0), ws + WS_H, u32x4{p0[0], p0[1], p1[0], p1[1]}); }
        { const u32x2 p0 = od ? hpk[3] : hpk[2], p1 = od ? hpk[4] : hpk[3]; st16_sc1(hb + (od ? 2048 : 1024), ws + WS_H, u32x4{p0[0], p0[1], p1[0], p1[1]}); }
        su = add_xor32(add_xor16(su)); s2 = add_xor32(add_xor16(s2));
        if (g == 0) { u32x2 st = {__float_as_uint(su), __float_as_uint(s2)}; st8_sc1((unsigned)((mtW * 16 + n) * 64 + c8 * 8), ws + WS_STAT2, st); }
    }
    STAMP(42);
    drain_bar();
    if (tid < 16) flags_store(ws, 2, i2 * 16 + tid, jw, tag);
    if (b == 0 && tid == 0) __hip_atomic_store((GAS u64*)ws, tag, RLX_AGENT);
    STAMP(7);

    {
        const char* s1 = ws + WS_W1R + (size_t)(Tp0 * 20 + w) * 1024;
        const unsigned l1 = lb + L_A + w * 1024;
        sfor<0, 25>([&](auto U) { glds16c<U.v * 4096>(vlan16, s1, l1); s1 += 4096; OPAQUE_S(s1); });
    }
    STAMP(43);
    await<0>(ws, 2, i2 * 16 + j2, tag, w, lane);
    STAMP(8);
    {
        s16x8 H[20][2];
        const int mt0 = 8 * i2 + 2 * w;
        s16x8 stv[2];
#pragma unroll
        for (int m = 0; m < 2; ++m) ld16_sc1(stv[m], (unsigned)(((mt0 + m) * 16 + n) * 64 + g * 16), ws + WS_STAT2);
        unsigned hrun = (unsigned)(mt0 * 20 * 1024 + lane * 16);
#define ISSUE_H(ks) do { ld16_sc1(H[ks][0], hrun, ws + WS_H); ld16_sc1(H[ks][1], hrun + 20 * 1024, ws + WS_H); hrun += 1024; asm volatile("" : "+v"(hrun)); } while (0)
        unsigned vF0 = lb + vlan16, vF1 = lb + 61440 + vlan16, vF2 = lb + 122880 + vlan16;
        asm volatile("" : "+v"(vF0), "+v"(vF1), "+v"(vF2));
        auto wread = [&](auto TT, auto KS) -> s16x8 {
            constexpr int t = TT.v, ks = KS.v;
            return *(volatile LAS s16x8*)((t < 3 ? vF0 : t < 6 ? vF1 : vF2) + (t % 3) * 20480 + ks * 1024);
        };
        {
            f32x4 c0 = z4, c1 = z4;
            const unsigned ta = lb + vlan16 + w * 20480, tb = lb + vlan16 + (w < 3 ? w + 4 : w) * 20480;
            s16x8 Ca[4], Cb[4];
            sfor<0, 3>([&](auto K) { Ca[K.v] = *(volatile LAS s16x8*)(ta + K.v * 1024); Cb[K.v] = *(volatile LAS s16x8*)(tb + K.v * 1024); });
            SB();
            sfor<0, 20>([&](auto K) {
                constexpr int k = K.v;
                c0 = mfma(ones, Ca[k & 3], c0);
                if constexpr (k + 3 < 20) Ca[(k + 3) & 3] = *(volatile LAS s16x8*)(ta + (k + 3) * 1024);
                SB();
                c1 = mfma(ones, Cb[k & 3], c1);
                if constexpr (k + 3 < 20) Cb[(k + 3) & 3] = *(volatile LAS s16x8*)(tb + (k + 3) * 1024);
                if constexpr ((k & 1) == 0 && k < 16) ISSUE_H(k >> 1);
                SB();
            });
            auto split = [&](float c) { const unsigned hi = pk2(c, c); const float lo = c - bflo(hi); u32x2 r = {hi, pk2(lo, lo)}; return r; };
            if (g == 0) {
                *(LAS u32x2*)(lb + L_CS1 + w * 128 + n * 8) = split(c0[0]);
                if (w < 3) *(LAS u32x2*)(lb + L_CS1 + (w + 4) * 128 + n * 8) = split(c1[0]);
            }
            if (tid < 2) *(LAS u64*)(lb + L_CS1 + 896 + tid * 8) = 0;
        }
        STAMP(19);
        asm volatile("s_waitcnt vmcnt(16)" : "+v"(stv[0]), "+v"(stv[1])::"memory");
        s16x8 Hm[2];
#pragma unroll
        for (int m = 0; m < 2; ++m) {
            f32x4 s = __builtin_bit_cast(f32x4, stv[m]);
            const float nm = add_xor32(add_xor16(s[0] + s[2])) * (-1.f / 640.f);
            const unsigned hi = pk2(nm, nm);
            const unsigned pr = g == 0 ? ((hi & 0xffffu) | (pk2(nm - bflo(hi), 0.f) << 16)) : 0u;
            Hm[m] = __builtin_bit_cast(s16x8, u32x4{pr, pr, 0u, 0u});
        }
        BAR();
        const unsigned csa = lb + L_CS1 + (g == 0 ? n * 8 : 896);
        auto csfrag = [&](int t) { const u32x2 c = *(LAS u32x2*)(csa + (g == 0 ? t * 128 : 0)); return __builtin_bit_cast(s16x8, u32x4{c[0], c[1], 0u, 0u}); };
        const int ksb = w < 2 ? 14 * w : 28 + 13 * (w - 2);
        const int nks = w < 2 ? 14 : 13;
        s16x8 V[14][5];
        const char* w2run = ws + WS_W2R + (size_t)(c8 * 54 + ksb) * 5120;
        const int step13 = nks == 14 ? 5120 : 0;
#define ISSUE_V(it, t) do { if constexpr ((it) < 8) ld16a_sc1(V[it][t], vlan16 + (t) * 1024, w2run); else ld16_sc1(V[it][t], vlan16 + (t) * 1024, w2run); \
            if ((t) == 4) { w2run += ((it) == 12 ? step13 : 5120); OPAQUE_S(w2run); } } while (0)
        auto relu2 = [&](unsigned p) { typedef short s2_ __attribute__((ext_vector_type(2))); const s2_ z = {0, 0};
                                       return __builtin_bit_cast(unsigned, __builtin_elementwise_max(__builtin_bit_cast(s2_, p), z)); };
        auto fin8 = [&](f32x4 x0, f32x4 x1) { return u32x4{relu2(pk2(x0[0], x0[1])), relu2(pk2(x0[2], x0[3])), relu2(pk2(x1[0], x1[1])), relu2(pk2(x1[2], x1[3]))}; };
        f32x4 a1[4][2];
        sfor<0, 4>([&](auto TT) { const s16x8 cf = csfrag(TT.v); a1[TT.v][0] = mfma(cf, Hm[0], z4); a1[TT.v][1] = mfma(cf, Hm[1], z4); });
        {
            s16x8 Wf[3][4];
            sfor<0, 2>([&](auto KS) { sfor<0, 4>([&](auto TT) { Wf[KS.v][TT.v] = wread(TT, KS); }); });
            SB();
            sfor<0, 20>([&](auto KS) {
                constexpr int ks = KS.v;
                constexpr int last = (ks + 7 < 19 ? ks + 7 : 19);
                asm volatile("s_waitcnt vmcnt(%2)" : "+v"(H[ks][0]), "+v"(H[ks][1]) : "i"(2 * (last - ks) + (ks == 13 ? 1 : ks > 13 ? 2 : 0)) : "memory");
                SB();
                sfor<0, 8>([&](auto I) {
                    constexpr int i = I.v, t = i >> 1, m = i & 1;
                    a1[t][m] = mfma(Wf[ks % 3][t], H[ks][m], a1[t][m]);
                    if constexpr (ks + 2 < 20 && (i & 1) == 0) Wf[(ks + 2) % 3][t] = wread(IC<t>{}, IC<ks + 2>{});
                    if constexpr (i == 3 && ks + 8 < 20) ISSUE_H(ks + 8);
                    if constexpr (i == 3 && (ks == 12 || ks == 13)) {
                        int li = w * 68 + (ks == 12 ? lane : 64 + (lane < 4 ? lane : 0)); li = li < 270 ? li : 269;
                        glds4((unsigned)((rr * 270 + li) * 128), ws + WS_W2R + (size_t)(x8 & 1) * 4 * 54 * 5120, lb + L_JUNK);
                    }
                    SB();
                });
            });
        }
        STAMP(20);
        f32x4 a2[3][2];
        sfor<0, 3>([&](auto TT) { const s16x8 cf = csfrag(4 + TT.v); a2[TT.v][0] = mfma(cf, Hm[0], z4); a2[TT.v][1] = mfma(cf, Hm[1], z4); });
        {
            s16x8 Wf[3][3];
            sfor<0, 2>([&](auto KS) { sfor<0, 3>([&](auto TT) { Wf[KS.v][TT.v] = wread(IC<4 + TT.v>{}, KS); }); });
            u32x2 e0, e1;
            sfor<0, 20>([&](auto KS) {
                constexpr int ks = KS.v;
                sfor<0, 6>([&](auto I) {
                    constexpr int i = I.v, t = i >> 1, m = i & 1;
                    a2[t][m] = mfma(Wf[ks % 3][t], H[ks][m], a2[t][m]);
                    if constexpr (ks + 2 < 20 && (i & 1) == 0) Wf[(ks + 2) % 3][t] = wread(IC<4 + t>{}, IC<ks + 2>{});
                    if constexpr (i == 3 || (i == 5 && (ks & 1) == 0)) { constexpr int pv = (ks >> 1) * 3 + ((ks & 1) ? 2 : (i == 5 ? 1 : 0)); ISSUE_V(pv / 5, pv % 5); }
                    if constexpr (ks >= 1 && ks < 17 && ((ks - 1) & 3) < 3) {
                        constexpr int u = (ks - 1) >> 2, ph = (ks - 1) & 3, P = u >> 1, mm = u & 1;
                        if constexpr (ph == 0 && i == 1) e0 = u32x2{relu2(pk2(a1[2 * P][mm][0], a1[2 * P][mm][1])), relu2(pk2(a1[2 * P][mm][2], a1[2 * P][mm][3]))};
                        if constexpr (ph == 1 && i == 1) e1 = u32x2{relu2(pk2(a1[2 * P + 1][mm][0], a1[2 * P + 1][mm][1])), relu2(pk2(a1[2 * P + 1][mm][2], a1[2 * P + 1][mm][3]))};
                        if constexpr (ph == 2 && i == 1)
                            st16_sc1((unsigned)(((mt0 + mm) * 54 + (Tp0 >> 1) + P) * 1024 + lane * 16), ws + WS_H1, u32x4{e0[0], e0[1], e1[0], e1[1]});
                    }
                    SB();
                });
            });
        }
        STAMP(22);
#pragma unroll
        for (int m = 0; m < 2; ++m) {
            st16_sc1((unsigned)(((mt0 + m) * 54 + (Tp0 >> 1) + 2) * 1024 + lane * 16), ws + WS_H1, fin8(a2[0][m], a2[1][m]));
            if (j2 < 12) st8_sc1((unsigned)(((mt0 + m) * 54 + (Tsg >> 1)) * 1024 + lane * 16 + (Tsg & 1) * 8), ws + WS_H1,
                                 u32x2{relu2(pk2(a2[2][m][0], a2[2][m][1])), relu2(pk2(a2[2][m][2], a2[2][m][3]))});
        }
        STAMP(24);
        drain_bar();
        if (tid < 16) flags_store(ws, 3, i2 * 16 + tid, j2, tag);
        STAMP(9);

        sfor<30, 70>([&](auto U) { ISSUE_V(U.v / 5, U.v % 5); });
        STAMP(44);
        await<63>(ws, 3, i2 * 16 + jw, tag, w, lane);
        STAMP(10);
        unsigned arun = (unsigned)(((8 * i2 + 4 * half) * 54 + ksb) * 1024 + lane * 16);
        const unsigned astep13 = nks == 14 ? 1024u : 0u;
        s16x8 A[4][4];
        unsigned aoff[4];
#pragma unroll
        for (int m = 0; m < 4; ++m) aoff[m] = ((w + m) & 3) * 54 * 1024;
        s16x8 st2v;
        ld16_sc1(st2v, (unsigned)((mtW * 16 + n) * 64 + g * 16), ws + WS_STAT2);
#define ISSUE_A(it, m) do { ld16_sc1(A[(it) & 3][m], arun + aoff[m], ws + WS_H1); \
            if ((m) == 3) { arun += ((it) == 12 ? astep13 : 1024u); asm volatile("" : "+v"(arun)); } } while (0)
        sfor<0, 3>([&](auto IT) { sfor<0, 4>([&](auto M) { ISSUE_A(IT.v, M.v); }); });
        f32x4 a[4][5];
#pragma unroll
        for (int m = 0; m < 4; ++m)
#pragma unroll
            for (int t = 0; t < 5; ++t) a[m][t] = z4;
        sfor<0, 14>([&](auto IT) {
            constexpr int it = IT.v;
            constexpr int ny = 4 * ((it + 2 < 13 ? it + 2 : 13) - it);
            if constexpr (it == 1) STAMP(25);
            if constexpr (it == 5) STAMP(26);
            if constexpr (it == 9) STAMP(27);
            if constexpr (it < 8)
                asm volatile("s_waitcnt vmcnt(%9)" : "+v"(A[it & 3][0]), "+v"(A[it & 3][1]), "+v"(A[it & 3][2]), "+v"(A[it & 3][3]),
                             "+a"(V[it][0]), "+a"(V[it][1]), "+a"(V[it][2]), "+a"(V[it][3]), "+a"(V[it][4]) : "i"(ny) : "memory");
            else
                asm volatile("s_waitcnt vmcnt(%9)" : "+v"(A[it & 3][0]), "+v"(A[it & 3][1]), "+v"(A[it & 3][2]), "+v"(A[it & 3][3]),
                             "+v"(V[it][0]), "+v"(V[it][1]), "+v"(V[it][2]), "+v"(V[it][3]), "+v"(V[it][4]) : "i"(ny) : "memory");
            SB();
            if (it < 13 || nks == 14) {
                sfor<0, 20>([&](auto I) {
                    constexpr int i = I.v, t = i >> 2, m = i & 3;
                    a[m][t] = mfma(V[it][t], A[it & 3][m], a[m][t]);
                    if constexpr ((i & 3) == 1 && i < 16 && it + 3 < 14) ISSUE_A(it + 3, i >> 2);
                    SB();
                });
            }
        });
        STAMP(11);
        asm volatile("" : "+v"(st2v));
        const f32x4 sv = __builtin_bit_cast(f32x4, st2v);
        const float smean = add_xor32(add_xor16(sv[0] + sv[2])) * (1.f / 640.f);
        const float rstd2 = __builtin_amdgcn_rsqf(fmaxf(add_xor32(add_xor16(sv[1] + sv[3])) * (1.f / 640.f) - smean * smean, 0.f) + 1e-5f);
        BAR();
#pragma unroll
        for (int m = 1; m < 4; ++m)
#pragma unroll
            for (int t = 0; t < 5; ++t) *(LAS f32x4*)(lb + ((((w + m) & 3) * 3 + (m - 1)) * 5 + t) * 1024 + lane * 16) = a[m][t];
        BAR();
        STAMP(46);
        f32x4 fin[5];
#pragma unroll
        for (int t = 0; t < 5; ++t) {
            const LAS char* p = (const LAS char*)(lb + (w * 15 + t) * 1024 + lane * 16);
            fin[t] = ((a[0][t] + *(const LAS f32x4*)p) + (*(const LAS f32x4*)(p + 5120) + *(const LAS f32x4*)(p + 10240))) * rstd2;
        }
        STAMP(47);
        LAS char* ot = (LAS char*)(lb + L_B + w * 2560);
#pragma unroll
        for (int t = 0; t < 5; ++t) {
            u32x2 fp = pack4(fin[t]);
            const u32x2 hp = *(LAS u32x2*)(lb + L_XRES + (16 * w + n) * 160 + 32 * t + 8 * g);
            f32x4 ov = {bflo(hp[0]) + bflo(fp[0]), bfhi(hp[0]) + bfhi(fp[0]), bflo(hp[1]) + bflo(fp[1]), bfhi(hp[1]) + bfhi(fp[1])};
            *(LAS u32x2*)(ot + n * 160 + 32 * t + 8 * g) = pack4(ov);
        }
        asm volatile("s_waitcnt lgkmcnt(0)" ::: "memory");
        int lane3 = lane; asm volatile("" : "+v"(lane3));
#pragma unroll
        for (int u = 0; u < 3; ++u) {
            const int q = 64 * u + lane3, row = q / 10, sl = q - row * 10;
            if (q < 160) *(GAS u32x4*)(out + (size_t)(mtW * 16 + row) * 1280 + 160 * c8 + 16 * sl) = *(LAS u32x4*)(ot + q * 16);
        }
    }
    STAMP(12); STAMPRT(63);
}

extern "C" void kernel_launch(void* const* d_in, const int* in_sizes, int n_in, void* d_out, int out_size, void* d_ws, size_t ws_size,
                              hipStream_t stream) {
    (void)in_sizes; (void)n_in; (void)out_size; (void)ws_size;
    fused_block<<<256, 256, 0, stream>>>((const char*)d_in[0], (const char*)d_in[1], (const char*)d_in[2], (const char*)d_in[4],
                                         (const char*)d_in[5], (char*)d_out, (char*)d_ws);
}
```

```cpp
#include <hip/hip_runtime.h>
#include <stdint.h>
#pragma clang diagnostic ignored "-Wint-to-pointer-cast"

typedef short s16x4 __attribute__((ext_vector_type(4)));
typedef short s16x8 __attribute__((ext_vector_type(8)));
typedef float f32x4 __attribute__((ext_vector_type(4)));
typedef unsigned u32x4 __attribute__((ext_vector_type(4)));
typedef unsigned u32x2 __attribute__((ext_vector_type(2)));
typedef unsigned long long u64;
#define LAS __attribute__((address_space(3)))
#define GAS __attribute__((address_space(1)))
#define DI __device__ __forceinline__
#define RLX_AGENT __ATOMIC_RELAXED, __HIP_MEMORY_SCOPE_AGENT
#define SB() __builtin_amdgcn_sched_barrier(0)

constexpr size_t WS_FLAG = 4096;
constexpr size_t WS_KVP = 262144;
constexpr size_t WS_STAT2 = 0x120000;
constexpr size_t WS_O = 2u << 20;
constexpr size_t WS_H = 5u << 20;
constexpr size_t WS_H1 = 8u << 20;
constexpr size_t WS_W1R = 16u << 20;
constexpr size_t WS_W2R = 19u << 20;
constexpr size_t WS_STAMP = 24u << 20;

constexpr int L_A = 0;
constexpr int L_B = 102400;
constexpr int L_XRES = L_B + 40960;
constexpr int L_M = 155648;
constexpr int L_MEAN = L_M;
constexpr int L_RSTD = L_M + 512;
constexpr int L_CSKV = L_M + 1024;
constexpr int L_CS4 = L_M + 1280;
constexpr int L_KVT = L_M + 1536;
constexpr int L_CS1 = L_M + 3328;
constexpr int L_JUNK = L_M + 5632;
constexpr int LDS_BYTES = 163840;
constexpr int CODE_LINES = 368;

enum { OP_X, OP_W, OP_R, OP_O, OP_Q, OP_T, OP_S, OP_U };
struct P1Op { int kind, a, b, ks, slot; };
constexpr P1Op P1SEQ[] = {
    {OP_U, 0, 0, -1, 0}, {OP_U, 1, 0, -1, 0},
    {OP_X, 0, 0, -1, 0}, {OP_X, 0, 1, -1, 0}, {OP_X, 0, 2, -1, 0}, {OP_X, 0, 3, -1, 0},
    {OP_W, 0, 0, -1, 0}, {OP_W, 0, 1, -1, 0}, {OP_W, 0, 2, -1, 0}, {OP_W, 0, 3, -1, 0}, {OP_W, 0, 4, -1, 0},
    {OP_U, 2, 0, -1, 0}, {OP_U, 3, 0, -1, 0},
    {OP_X, 1, 0, -1, 0}, {OP_X, 1, 1, -1, 0}, {OP_X, 1, 2, -1, 0}, {OP_X, 1, 3, -1, 0},
    {OP_O, 1, 0, -1, 0},
    {OP_W, 1, 0, -1, 0}, {OP_W, 1, 1, -1, 0}, {OP_W, 1, 2, -1, 0}, {OP_W, 1, 3, -1, 0}, {OP_W, 1, 4, -1, 0},
    {OP_X, 2, 0, -1, 0}, {OP_X, 2, 1, -1, 0}, {OP_X, 2, 2, -1, 0}, {OP_X, 2, 3, -1, 0},
    {OP_W, 2, 0, 0, 3}, {OP_W, 2, 1, 0, 8}, {OP_W, 2, 2, 0, 13},
    {OP_X, 3, 0, 1, 2}, {OP_X, 3, 1, 1, 6}, {OP_X, 3, 2, 1, 10}, {OP_X, 3, 3, 1, 14},
    {OP_W, 2, 3, 2, 3}, {OP_O, 2, 0, 2, 5}, {OP_W, 2, 4, 2, 8}, {OP_W, 3, 0, 2, 13},
    {OP_X, 4, 0, 3, 2}, {OP_X, 4, 1, 3, 6}, {OP_X, 4, 2, 3, 10}, {OP_X, 4, 3, 3, 14},
    {OP_W, 3, 1, 4, 3}, {OP_U, 4, 0, 4, 5}, {OP_W, 3, 2, 4, 8}, {OP_W, 3, 3, 4, 13},
    {OP_X, 5, 0, 5, 2}, {OP_X, 5, 1, 5, 6}, {OP_X, 5, 2, 5, 10}, {OP_X, 5, 3, 5, 14},
    {OP_W, 3, 4, 6, 3}, {OP_W, 4, 0, 6, 8}, {OP_W, 4, 1, 6, 13},
    {OP_X, 6, 0, 7, 2}, {OP_X, 6, 1, 7, 6}, {OP_X, 6, 2, 7, 10}, {OP_X, 6, 3, 7, 14},
    {OP_W, 4, 2, 8, 3}, {OP_W, 4, 3, 8, 8}, {OP_W, 4, 4, 8, 13},
    {OP_X, 7, 0, 9, 2}, {OP_X, 7, 1, 9, 6}, {OP_X, 7, 2, 9, 10}, {OP_X, 7, 3, 9, 14},
    {OP_R, 0, 0, 10, 3}, {OP_R, 1, 0, 10, 8}, {OP_R, 2, 0, 10, 13},
    {OP_X, 8, 0, 11, 2}, {OP_X, 8, 1, 11, 6}, {OP_X, 8, 2, 11, 10}, {OP_X, 8, 3, 11, 14},
    {OP_R, 3, 0, 12, 3}, {OP_R, 4, 0, 12, 8}, {OP_U, 5, 0, 12, 13},
    {OP_X, 9, 0, 13, 2}, {OP_X, 9, 1, 13, 6}, {OP_X, 9, 2, 13, 10}, {OP_X, 9, 3, 13, 14},
    {OP_Q, 0, 0, 15, 3}, {OP_Q, 0, 1, 15, 8}, {OP_Q, 0, 2, 15, 13}, {OP_Q, 0, 3, 16, 3}, {OP_Q, 0, 4, 16, 8},
    {OP_T, 0, 0, 17, 1}, {OP_T, 1, 0, 17, 2}, {OP_Q, 1, 0, 17, 3}, {OP_T, 2, 0, 17, 5}, {OP_T, 3, 0, 17, 6}, {OP_T, 4, 0, 17, 7},
    {OP_Q, 1, 1, 17, 8}, {OP_Q, 1, 2, 17, 13},
    {OP_S, 0, 0, 18, 1}, {OP_Q, 1, 3, 18, 3}, {OP_S, 1, 0, 18, 6}, {OP_Q, 1, 4, 18, 8}, {OP_S, 2, 0, 18, 11}, {OP_S, 3, 0, 19, 2}, {OP_S, 4, 0, 19, 7},
};
constexpr int P1N = sizeof(P1SEQ) / sizeof(P1SEQ[0]);
constexpr int p1_wait(int s) {
    int issued = 0, need = 0;
    for (int i = 0; i < P1N; ++i) {
        if (P1SEQ[i].ks < 2 * s - 1 || P1SEQ[i].ks < 0) issued = i + 1;
        if ((P1SEQ[i].kind == OP_X && P1SEQ[i].a == s) || (P1SEQ[i].kind == OP_W && P1SEQ[i].a <= (s >> 1))) need = need > i + 1 ? need : i + 1;
    }
    return issued - need;
}
constexpr bool p1_ok() { for (int s = 0; s < 10; ++s) if (p1_wait(s) < 0 || p1_wait(s) > 63) return false; return true; }
static_assert(p1_ok(), "P1a schedule: an op is waited for before it is issued");

#define STAMP(i) do {} while (0)
#define STAMPRT(i) do {} while (0)

template <int I> struct IC { static constexpr int v = I; };
template <int B, int E, class F> DI void sfor(F&& f) { if constexpr (B < E) { f(IC<B>{}); sfor<B + 1, E>(f); } }
#define OPAQUE_S(p) asm volatile("" : "+s"(p))

DI f32x4 mfma(s16x8 a, s16x8 b, f32x4 c) { return __builtin_amdgcn_mfma_f32_16x16x32_bf16(a, b, c, 0, 0, 0); }
DI s16x4 trread(unsigned addr) { return __builtin_bit_cast(s16x4, __builtin_amdgcn_ds_read_tr16_b64_v4i16((LAS s16x4*)addr)); }
DI s16x8 cat(s16x4 a, s16x4 b) { return __builtin_shufflevector(a, b, 0, 1, 2, 3, 4, 5, 6, 7); }
DI unsigned pk2(float lo, float hi) {
    typedef __bf16 bf2 __attribute__((ext_vector_type(2)));
    typedef float f2 __attribute__((ext_vector_type(2)));
    f2 v = {lo, hi};
    return __builtin_bit_cast(unsigned, __builtin_convertvector(v, bf2));
}
DI s16x8 pack8(f32x4 a, f32x4 b) {
    u32x4 r = {pk2(a[0], a[1]), pk2(a[2], a[3]), pk2(b[0], b[1]), pk2(b[2], b[3])};
    return __builtin_bit_cast(s16x8, r);
}
DI u32x2 pack4(f32x4 a) { u32x2 r = {pk2(a[0], a[1]), pk2(a[2], a[3])}; return r; }
DI float bflo(unsigned u) { return __uint_as_float(u << 16); }
DI float bfhi(unsigned u) { return __uint_as_float(u & 0xffff0000u); }
DI float phi(float t) { return (fmaxf(t, 0.f) + 1e-6f) + __builtin_amdgcn_fmed3f(__builtin_amdgcn_exp2f(t * 1.44269504088896f), 0.f, 1.f); }
DI float add_xor16(float v) { const unsigned u = __float_as_uint(v); auto r = __builtin_amdgcn_permlane16_swap(u, u, false, false); return __uint_as_float(r[0]) + __uint_as_float(r[1]); }
DI float add_xor32(float v) { const unsigned u = __float_as_uint(v); auto r = __builtin_amdgcn_permlane32_swap(u, u, false, false); return __uint_as_float(r[0]) + __uint_as_float(r[1]); }
constexpr int wq_pos(int grp) { return grp < 3 ? 16384 + 5120 * grp : grp < 7 ? 32768 + 5120 * (grp - 3) : 5120 * (grp - 7); }

template <int LOFF> DI void glds16(unsigned voff, const void* sbase, unsigned lds) {
    asm volatile("s_add_u32 m0, %2, %3\n\ts_nop 0\n\tglobal_load_lds_dwordx4 %0, %1" ::"v"(voff), "s"(sbase), "s"(lds), "i"(LOFF) : "memory", "scc");
}
template <int LOFF, int GOFF> DI void glds16i(unsigned voff, const void* sbase, unsigned lds) {
    asm volatile("s_add_u32 m0, %2, %3\n\ts_nop 0\n\tglobal_load_lds_dwordx4 %0, %1 offset:%4" ::"v"(voff), "s"(sbase), "s"(lds), "i"(LOFF - GOFF), "i"(GOFF) : "memory", "scc");
}
template <int LOFF> DI void glds16c(unsigned voff, const void* sbase, unsigned lds) {
    asm volatile("s_add_u32 m0, %2, %3\n\ts_nop 0\n\tglobal_load_lds_dwordx4 %0, %1 sc1" ::"v"(voff), "s"(sbase), "s"(lds), "i"(LOFF) : "memory", "scc");
}
DI void glds4(unsigned voff, const void* sbase, unsigned lds) {
    asm volatile("s_mov_b32 m0, %2\n\ts_nop 0\n\tglobal_load_lds_dword %0, %1" ::"v"(voff), "s"(sbase), "s"(lds) : "memory");
}
#define WAITV(n) asm volatile("s_waitcnt vmcnt(%0)" ::"i"(n) : "memory")
#define BARX() do { __builtin_amdgcn_s_barrier(); asm volatile("" ::: "memory"); } while (0)
#define BAR() do { asm volatile("s_waitcnt lgkmcnt(0)" ::: "memory"); __builtin_amdgcn_s_barrier(); asm volatile("" ::: "memory"); } while (0)

DI void ld16_sc1(s16x8& d, unsigned voff, const void* sbase) {
    asm volatile("global_load_dwordx4 %0, %1, %2 sc1" : "=v"(d) : "v"(voff), "s"(sbase) : "memory");
}
DI void ld16a_sc1(s16x8& d, unsigned voff, const void* sbase) {
    asm volatile("global_load_dwordx4 %0, %1, %2 sc1" : "=a"(d) : "v"(voff), "s"(sbase) : "memory");
}
DI void ld8_sc1(u64& d, const void* sbase) { asm volatile("global_load_dwordx2 %0, %1, %2 sc1" : "=v"(d) : "v"(0u), "s"(sbase) : "memory"); }
DI void st16_sc1(unsigned voff, void* sbase, u32x4 v) {
    asm volatile("global_store_dwordx4 %0, %1, %2 sc1\n\ts_nop 1" ::"v"(voff), "v"(v), "s"(sbase) : "memory");
}
DI void st8_sc1(unsigned voff, void* sbase, u32x2 v) {
    asm volatile("global_store_dwordx2 %0, %1, %2 sc1" ::"v"(voff), "v"(v), "s"(sbase) : "memory");
}
DI void drain_bar() { WAITV(0); BAR(); }
DI void flags_store(char* ws, int arr, int line, int slot, u64 tag) {
    __hip_atomic_store((GAS u64*)(ws + WS_FLAG + (size_t)arr * 32768 + (size_t)line * 128) + slot, tag, RLX_AGENT);
}
template <int NV> DI void await(char* ws, int arr, int line, u64 tag, int w, int lane) {
    if (w == 0) {
        GAS u64* fl = (GAS u64*)(ws + WS_FLAG + (size_t)arr * 32768 + (size_t)line * 128) + (lane & 15);
        for (unsigned spins = 0; spins < (1u << 20); ++spins) {
            u64 f = __hip_atomic_load(fl, RLX_AGENT);
            if (__all(f == tag)) break;
        }
    }
    WAITV(NV);
    BAR();
}

__global__ void __launch_bounds__(256, 1)
fused_block(const char* __restrict__ x, const char* __restrict__ wqkv, const char* __restrict__ wo, const char* __restrict__ w1,
            const char* __restrict__ w2, char* __restrict__ out, char* __restrict__ ws) {
    __shared__ __attribute__((aligned(16))) char smem[LDS_BYTES];
    const unsigned lb = (unsigned)(uintptr_t)(LAS char*)smem;
    const int tid = threadIdx.x, lane = tid & 63;
    const int w = __builtin_amdgcn_readfirstlane(tid >> 6);
    const int b = blockIdx.x, x8 = b & 7, rr = b >> 3;
    const int i1 = 4 * (x8 >> 1) + (rr >> 3), j1 = 8 * (x8 & 1) + (rr & 7);
    const int i2 = i1, j2 = j1;
    const int half = (rr >> 2) & 1, c8 = 4 * (x8 & 1) + (rr & 3), jw = 8 * half + c8;
    {
        u64 pc;
        asm volatile("s_getpc_b64 %0" : "=s"(pc));
        const unsigned cl_ = (rr * 4 + w) * 3 + ((lane & 3) < 3 ? (lane & 3) : 0);
        glds4((cl_ < CODE_LINES ? cl_ : CODE_LINES - 1) * 128, (const void*)(pc & ~(u64)127), lb + L_JUNK);
        SB();
    }
    const int g = lane >> 4, n = lane & 15, q4 = n >> 2, p4 = lane & 3;
    const s16x8 ones = {0x3F80, 0x3F80, 0x3F80, 0x3F80, 0x3F80, 0x3F80, 0x3F80, 0x3F80};
    const f32x4 z4 = {0, 0, 0, 0};
    STAMP(0); STAMPRT(62);

    s16x8 X[20][2];
    f32x4 acc[5][2], sq[2], rs[2], csA, csB;
    unsigned voffW[5], voffX[4];
    const char* xb = x + (size_t)i1 * 128 * 1280;
    const unsigned ldsXw = lb + L_B + w * 4096, ldsWw = lb + L_A + w * 5120;
#pragma unroll
    for (int u = 0; u < 4; ++u)
        voffX[u] = (32 * w + 8 * u + (lane >> 3)) * 1280 + 16 * ((lane & 7) ^ ((lane >> 4) + 4 * (u & 1)));
#define ISSUE_X(s, u) glds16i<((s) % 3) * 16384 + (u) * 1024, (s) * 128>(voffX[u], xb, ldsXw)
#define ISSUE_XS(s) do { ISSUE_X(s, 0); ISSUE_X(s, 1); ISSUE_X(s, 2); ISSUE_X(s, 3); } while (0)
#pragma unroll
    for (int u = 0; u < 5; ++u) {
        int q = 64 * u + lane, row = q / 10, s = q - row * 10;
        row ^= ((row >> 3) & 1) << 2;
        voffW[u] = row * 3840 + (s < 5 ? (640 + 40 * j1) * 2 + 16 * s : (1280 + 40 * j1) * 2 + 16 * (s - 5));
    }
    const char* wkb = wqkv + (size_t)w * (32 * 3840);
    const char* wrun = wkb;
#define ISSUE_W(t, u) do { glds16<(t) * 20480 + (u) * 1024>(voffW[u], wrun, ldsWw); if ((u) == 4) { wrun += 128 * 3840; OPAQUE_S(wrun); } } while (0)
    unsigned voffQ[5];
#pragma unroll
    for (int u = 0; u < 5; ++u) {
        int q = 64 * u + lane, row = q / 5, s = q - row * 5;
        row ^= ((row >> 3) & 1) << 2;
        voffQ[u] = row * 3840 + 80 * j1 + 16 * s;
    }
    auto issueQ = [&](auto A, auto U) {
        constexpr int a = A.v, u = U.v;
        const int grp = a == 0 ? (w < 2 ? w : 2) : a == 1 ? 3 + w : 7 + (w < 2 ? w : 2);
        const int pos = a == 0 ? 16384 + 5120 * grp : a == 1 ? 32768 + 5120 * w : 5120 * (grp - 7);
        glds16<u * 1024>(voffQ[u], wqkv + (size_t)grp * (64 * 3840), lb + L_B + pos);
    };
    const unsigned raR = lb + L_A + (4 * g + q4) * 128 + 16 * ((2 * w + (p4 >> 1)) ^ ((((4 * g + q4) >> 1) & 3) << 1)) + 8 * (p4 & 1);
    s16x8 Rf[5];
    auto runit = [&](auto R, unsigned& isw1, unsigned& ksr, unsigned& cl) {
        constexpr int r = R.v;
        const unsigned ub = (unsigned)b;
        unsigned v;
        if constexpr (r < 2) { isw1 = 1; v = ub + 256 * r; }
        else if constexpr (r == 2) { isw1 = ub < 28; v = isw1 ? ub + 512 : ub - 28; }
        else if constexpr (r == 3) { isw1 = 0; v = ub + 228; }
        else { isw1 = 0; v = ub < 56 ? ub + 484 : ub + 228; }
        ksr = isw1 ? (v * 2428u) >> 16 : (v * 6554u) >> 16;
        cl = v - ksr * (isw1 ? 27u : 10u);
    };
    u64 epoch;
    auto p1issue = [&](auto N) {
        constexpr P1Op op = P1SEQ[N.v];
        if constexpr (op.kind == OP_X) ISSUE_X(op.a, op.b);
        else if constexpr (op.kind == OP_W) ISSUE_W(op.a, op.b);
        else if constexpr (op.kind == OP_R) {
            unsigned isw1, ksr, cl;
            runit(IC<op.a>{}, isw1, ksr, cl);
            const unsigned pitch = isw1 ? 3456u : 1280u;
            const unsigned rrow = 8 * w + (lane >> 3);
            glds16<op.a * 4096>(rrow * pitch + 16 * ((lane & 7) ^ (((rrow >> 1) & 3) << 1)), (isw1 ? w1 : w2) + (ksr * 32 * pitch + cl * 128), lb + L_A + w * 1024);
        } else if constexpr (op.kind == OP_U) {
            const unsigned wv = rr * 4 + w;
            if constexpr (op.a == 0 || op.a == 2) {
                const unsigned l = lane < 20 ? lane : 0, rw = (l * 52u) >> 8;
                glds4((4 * wv + rw) * 1280 + (l - 5 * rw + (op.a == 2 ? 5 : 0)) * 128, x + (size_t)(x8 >> 1) * 512 * 1280, lb + L_JUNK);
            } else if constexpr (op.a == 1 || op.a == 3) {
                const unsigned li = wv * 25 + (lane < 25 ? lane : 0), rw = (li * 6554u) >> 16, rem = li - rw * 10;
                glds4((rw + (op.a == 3 ? 320 : 0)) * 3840 + 1280 + (rem >= 5 ? 640 : 0) + (x8 & 1) * 640 + rem * 128, wqkv, lb + L_JUNK);
            } else if constexpr (op.a == 4) {
                const unsigned li = wv * 25 + (lane < 25 ? lane : 0), rw = (li * 13108u) >> 16, rem = li - rw * 5;
                glds4(rw * 3840 + (x8 & 1) * 640 + rem * 128, wqkv, lb + L_JUNK);
            } else {
                const unsigned li = wv * 25 + (lane < 25 ? lane : 0), rw = (li * 13108u) >> 16, rem = li - rw * 5;
                glds4(rw * 1280 + (x8 & 1) * 640 + rem * 128, wo, lb + L_JUNK);
            }
        } else if constexpr (op.kind == OP_Q) issueQ(IC<op.a>{}, IC<op.b>{});
        else if constexpr (op.kind == OP_T) {
            Rf[op.a] = cat(trread(raR + op.a * 4096), trread(raR + op.a * 4096 + 2048));
        } else if constexpr (op.kind == OP_S) {
            unsigned isw1, ksr, cl;
            runit(IC<op.a>{}, isw1, ksr, cl);
            const unsigned T = 4 * cl + w, c8r = (T * 52u) >> 8;
            const unsigned dst = isw1 ? (unsigned)WS_W1R + (T * 20 + ksr) * 1024 : (unsigned)WS_W2R + ((c8r * 54 + ksr) * 5 + (T - 5 * c8r)) * 1024;
            st16_sc1((unsigned)(lane * 16), ws + dst, __builtin_bit_cast(u32x4, Rf[op.a]));
        } else if constexpr (op.a == 0) {
            u64 pc;
            asm volatile("s_getpc_b64 %0" : "=s"(pc));
            pc &= ~(u64)127;
            const unsigned cl_ = (rr * 4 + w) * 3 + ((lane & 3) < 3 ? (lane & 3) : 0);
            glds4((cl_ < CODE_LINES ? cl_ : CODE_LINES - 1) * 128, (const void*)pc, lb + L_JUNK);
        } else if constexpr (op.a == 2) {
            const char* p = lane < 24 ? ws + (size_t)lane * (1 << 20) : lane < 27 ? out + (size_t)(lane - 24) * 1300000 : lane < 30 ? w1 + (size_t)(lane - 27) * 1100000
                          : lane < 33 ? w2 + (size_t)(lane - 30) * 1100000 : wo;
            asm volatile("s_mov_b32 m0, %1\n\ts_nop 0\n\tglobal_load_lds_dword %0, off" ::"v"(p), "s"(lb + L_JUNK) : "memory");
        } else ld8_sc1(epoch, ws);
    };
    sfor<0, P1N>([&](auto N) { if constexpr (P1SEQ[N.v].ks < 0) p1issue(N); });
    const int ro1 = 8 * g + q4 + 4 * (g & 1), ro2 = 8 * g + q4 + 4 * (1 - (g & 1));
    {
        unsigned vW1[2], vW2[2], vM1[2], vM2[2], vC1[2], vC2[2];
        const int mo = (p4 == 0 ? 32 : p4 == 1 ? 72 : p4 == 2 ? 112 : 152);
#pragma unroll
        for (int h = 0; h < 2; ++h) {
            vW1[h] = lb + L_A + h * 51200 + ro1 * 160 + 8 * p4; vW2[h] = lb + L_A + h * 51200 + ro2 * 160 + 8 * p4;
            vM1[h] = lb + L_A + h * 51200 + ro1 * 160 + mo; vM2[h] = lb + L_A + h * 51200 + ro2 * 160 + mo;
            vC1[h] = vW1[h] + 40 * w; vC2[h] = vW2[h] + 40 * w;
            asm volatile("" : "+v"(vW1[h]), "+v"(vW2[h]), "+v"(vM1[h]), "+v"(vM2[h]), "+v"(vC1[h]), "+v"(vC2[h]));
        }
        unsigned vD1 = vM1[0] + w * 5120, vD2 = vM2[0] + w * 5120;
        unsigned vX[2];
#pragma unroll
        for (int kl = 0; kl < 2; ++kl) vX[kl] = lb + L_B + w * 4096 + n * 128 + 16 * ((4 * kl + g) ^ (n >> 1));
#pragma unroll
        for (int t = 0; t < 5; ++t) acc[t][0] = acc[t][1] = z4;
        sq[0] = sq[1] = rs[0] = rs[1] = csA = csB = z4;
        s16x4 Wl[2][7], Wh[2][7];
        auto side = [&](auto KS, auto I) {
            constexpr int ks = KS.v, i = I.v, r = ks & 1, s = ks >> 1, kl = ks & 1, h = ks >= 10;
            constexpr unsigned ko = (ks - 10 * h) * 5120;
            if constexpr (i < 2) X[ks][i] = *(volatile LAS s16x8*)(vX[kl] + (s % 3) * 16384 + i * 2048);
            else if constexpr (i < 10) { constexpr int t = (i - 2) >> 1; if constexpr (i & 1) Wh[r][t] = trread(vW2[h] + ko + 40 * t); else Wl[r][t] = trread(vW1[h] + ko + 40 * t); }
            else if constexpr (i < 12) { if constexpr (i & 1) Wh[r][4] = trread(vM2[h] + ko); else Wl[r][4] = trread(vM1[h] + ko); }
            else if constexpr (i < 14) { if constexpr (i & 1) Wh[r][5] = trread(vC2[h] + ko); else Wl[r][5] = trread(vC1[h] + ko); }
            else {
                constexpr int u = ks >> 2;
                if constexpr (i & 1) { Wh[r][6] = trread(vD2 + (u % 3) * 20480); if constexpr (u == 2) { vD2 += 61440; asm volatile("" : "+v"(vD2)); } }
                else { Wl[r][6] = trread(vD1 + (u % 3) * 20480); if constexpr (u == 2) { vD1 += 61440; asm volatile("" : "+v"(vD1)); } }
            }
        };
        STAMP(48);
        WAITV(p1_wait(0));
        BARX();
        STAMP(49);
        sfor<0, 14>([&](auto I) { side(IC<0>{}, I); });
        SB();
        sfor<0, 20>([&](auto KS) {
            constexpr int ks = KS.v;
            if constexpr ((ks & 1) && ks < 19) {
                constexpr int s = (ks + 1) >> 1;
                WAITV(p1_wait(s));
                if constexpr (s >= 8) BAR();
                else if constexpr ((s & 1) == 0) BARX();
                SB();
            }
            if constexpr (ks == 4) STAMP(50);
            if constexpr (ks == 8) STAMP(51);
            if constexpr (ks == 12) STAMP(52);
            if constexpr (ks == 16) STAMP(53);
            constexpr int r = ks & 1;
            constexpr int nm = ((ks & 3) == 3) ? 16 : 15;
            constexpr int nside = ks < 19 ? (((ks + 1) & 3) == 3 ? 16 : 14) : 0;
            sfor<0, 16>([&](auto I) {
                constexpr int i = I.v;
                if constexpr (i < nm) {
                    if constexpr (i < 10) { constexpr int t = i >> 1, m = i & 1; acc[t][m] = mfma(X[ks][m], cat(Wl[r][t], Wh[r][t]), acc[t][m]); }
                    else if constexpr (i == 10) csA = mfma(ones, cat(Wl[r][5], Wh[r][5]), csA);
                    else if constexpr (i < 13) sq[i - 11] = mfma(X[ks][i - 11], X[ks][i - 11], sq[i - 11]);
                    else if constexpr (i < 15) rs[i - 13] = mfma(X[ks][i - 13], ones, rs[i - 13]);
                    else csB = mfma(ones, cat(Wl[r][6], Wh[r][6]), csB);
                }
                if constexpr (i < nside) side(IC<ks + 1>{}, I);
                sfor<0, P1N>([&](auto N) { if constexpr (P1SEQ[N.v].ks == ks && P1SEQ[N.v].slot == i) p1issue(N); });
                SB();
            });
        });
    }
    asm volatile("" : "+v"(epoch), "+v"(csA), "+v"(csB));
    const u64 tag = epoch + 1;
    STAMP(1);

    if (g == 0) { ((LAS float*)(lb + L_CSKV))[16 * w + n] = csA[0]; ((LAS float*)(lb + L_CS4))[16 * w + n] = csB[0]; }
    f32x4 lnA[2], lnB[2];
#pragma unroll
    for (int m = 0; m < 2; ++m) {
        f32x4 mean4, rstd4;
#pragma unroll
        for (int r = 0; r < 4; ++r) {
            const float s2 = __uint_as_float(__builtin_amdgcn_ds_bpermute(4 * (20 * g + r), __float_as_uint(sq[m][r])));
            const float mean = rs[m][r] * (1.f / 640.f);
            const float rstd = __builtin_amdgcn_rsqf(fmaxf(s2 * (1.f / 640.f) - mean * mean, 0.f) + 1e-5f);
            mean4[r] = mean; rstd4[r] = rstd;
            lnA[m][r] = rstd; lnB[m][r] = -mean * rstd;
        }
        *(LAS f32x4*)(lb + L_MEAN + (32 * w + 16 * m + 4 * g) * 4) = mean4;
        *(LAS f32x4*)(lb + L_RSTD + (32 * w + 16 * m + 4 * g) * 4) = rstd4;
    }
    BAR();
    STAMP(31);
    {
        float cs[5];
#pragma unroll
        for (int t = 0; t < 4; ++t) cs[t] = ((LAS float*)(lb + L_CSKV))[16 * t + n];
        {
            LAS float* c4 = (LAS float*)(lb + L_CS4);
            cs[4] = (c4[n] + c4[16 + n]) + (c4[32 + n] + c4[48 + n]);
        }
        s16x8 F[4], MA, MB;
        sfor<0, 5>([&](auto TT) {
            constexpr int t = TT.v;
            issueQ(IC<2>{}, TT);
            f32x4 v[2], vb_[2];
#pragma unroll
            for (int m = 0; m < 2; ++m)
#pragma unroll
                for (int r = 0; r < 4; ++r) {
                    const float y = lnA[m][r] * acc[t][m][r] + lnB[m][r] * cs[t];
                    if (t < 2) v[m][r] = phi(y);
                    else if (t < 4) v[m][r] = y;
                    else { v[m][r] = n < 8 ? phi(y) : y; vb_[m][r] = n < 8 ? 1.f : y; }
                }
            if constexpr (t < 4) F[t] = pack8(v[0], v[1]);
            else { MA = pack8(v[0], v[1]); MB = pack8(vb_[0], vb_[1]); }
            SB();
        });
        STAMP(33);
        f32x4 P1 = mfma(F[0], F[2], z4), P2 = mfma(F[0], MB, z4), P3 = mfma(MA, F[2], z4), P4 = mfma(MA, MB, z4);
        f32x4 P5 = mfma(F[1], F[3], z4), P6 = mfma(F[1], MB, z4), P7 = mfma(MA, F[3], z4);
        LAS char* red = (LAS char*)(lb + L_A + 20480 + w * 3360);
        *(LAS f32x4*)(red + (n * 20 + 4 * g) * 4) = P1;
        *(LAS f32x4*)(red + (420 + n * 20 + 4 * g) * 4) = P5;
        if (n == 0 || (n >= 8 && n < 12)) *(LAS f32x4*)(red + ((n == 0 ? 20 : 8 + n) * 20 + 4 * g) * 4) = P2;
        if (n == 0 || n >= 12) *(LAS f32x4*)(red + (420 + (n == 0 ? 20 : 4 + n) * 20 + 4 * g) * 4) = P6;
        if (g < 2) *(LAS f32x4*)(red + (420 * g + n * 20 + 16) * 4) = (g == 0 ? P3 : P7);
        if (g < 2 && (n == 0 || (g == 0 ? (n >= 8 && n < 12) : n >= 12)))
            *(LAS f32x4*)(red + (420 * g + (n == 0 ? 20 : (g == 0 ? 8 + n : 4 + n)) * 20 + 16) * 4) = P4;
    }
    STAMP(34);
    BAR();
    STAMP(35);
    if (tid < 210) {
        LAS char* red = (LAS char*)(lb + L_A + 20480 + tid * 16);
        f32x4 s = (*(LAS f32x4*)(red) + *(LAS f32x4*)(red + 3360)) + (*(LAS f32x4*)(red + 6720) + *(LAS f32x4*)(red + 10080));
        st16_sc1((unsigned)(tid * 16), ws + WS_KVP + (size_t)(j1 * 16 + i1) * 3360, __builtin_bit_cast(u32x4, s));
    }
    STAMP(36);
    drain_bar();
    if (tid < 16) flags_store(ws, 0, tid * 16 + j1, i1, tag);
    STAMP(2);

    unsigned voff160[5];
    {
        int lane2 = lane; asm volatile("" : "+v"(lane2));
#pragma unroll
        for (int u = 0; u < 5; ++u) { int q = 64 * u + lane2, row = q / 10, s = q - row * 10; voff160[u] = row * 1280 + 160 * c8 + 16 * s; }
    }
    s16x8 Fa[2], Fb[2], kp[16];
    const char* worun = wo + (size_t)w * (32 * 1280);
    {
        const unsigned vQ1 = lb + L_B + ro1 * 80 + 8 * p4, vQ2 = lb + L_B + ro2 * 80 + 8 * p4;
        const int to = (p4 == 0 ? 32 : p4 == 1 ? 72 : p4 == 2 ? 32 : 72);
        const unsigned vT1 = lb + L_B + ro1 * 80 + to, vT2 = lb + L_B + ro2 * 80 + to;
        f32x4 aq[3][2], cq[3];
#pragma unroll
        for (int t = 0; t < 3; ++t) aq[t][0] = aq[t][1] = cq[t] = z4;
        s16x4 Wl[2][3], Wh[2][3];
        auto side = [&](auto KS, auto I) {
            constexpr int ks = KS.v, i = I.v, r = ks & 1, t = i >> 1;
            constexpr unsigned ko = wq_pos(ks >> 1) + (ks & 1) * 2560;
            if constexpr (t < 2) { if constexpr (i & 1) Wh[r][t] = trread(vQ2 + ko + 40 * t); else Wl[r][t] = trread(vQ1 + ko + 40 * t); }
            else { if constexpr (i & 1) Wh[r][2] = trread(vT2 + ko); else Wl[r][2] = trread(vT1 + ko); }
        };
        sfor<0, 6>([&](auto I) { side(IC<0>{}, I); });
        SB();
        const char* kb = ws + WS_KVP + (size_t)j1 * 16 * 3360;
        const unsigned kvo = (tid < 210 ? tid : 209) * 16;
        sfor<0, 20>([&](auto KS) {
            constexpr int ks = KS.v, r = ks & 1;
            if constexpr (ks == 13) { STAMP(3); await<63>(ws, 0, i1 * 16 + j1, tag, w, lane); STAMP(4); SB(); }
            sfor<0, 9>([&](auto I) {
                constexpr int i = I.v, t = i / 3, m = i % 3;
                const s16x8 wf = cat(Wl[r][t], Wh[r][t]);
                if constexpr (m < 2) aq[t][m] = mfma(wf, X[ks][m], aq[t][m]);
                else cq[t] = mfma(wf, ones, cq[t]);
                if constexpr (i < 6 && ks < 19) side(IC<ks + 1>{}, I);
                if constexpr ((i == 3 || i == 8) && ks < 9) {
                    constexpr int pc_ = 2 * ks + (i == 8), tt = pc_ / 5, u = pc_ % 5;
                    glds16<tt * 20480 + u * 1024>(voff160[u], worun, ldsWw);
                    if constexpr (u == 4) { worun += 128 * 1280; OPAQUE_S(worun); }
                }
                if constexpr (ks >= 13 && ks < 19 && (i == 2 || i == 5 || i == 8) && 3 * (ks - 13) + i / 3 < 16) {
                    constexpr int pi = 3 * (ks - 13) + i / 3;
                    ld16_sc1(kp[pi], kvo + pi * 3360, kb);
                }
                SB();
            });
        });
        STAMP(37);
#pragma unroll
        for (int m = 0; m < 2; ++m) {
            const float mean = ((LAS float*)(lb + L_MEAN))[32 * w + 16 * m + n], rstd = ((LAS float*)(lb + L_RSTD))[32 * w + 16 * m + n];
            f32x4 v[3];
#pragma unroll
            for (int t = 0; t < 3; ++t)
#pragma unroll
                for (int r = 0; r < 4; ++r) {
                    float y = phi(rstd * aq[t][m][r] - (mean * rstd) * cq[t][r]);
                    v[t][r] = (t == 2 && g >= 2) ? 0.f : y;
                }
            Fa[m] = pack8(v[0], v[2]);
            Fb[m] = pack8(v[1], v[2]);
        }
        asm volatile("" : "+v"(Fa[0]), "+v"(Fa[1]), "+v"(Fb[0]), "+v"(Fb[1]));
    }

    {
        asm volatile("s_waitcnt vmcnt(0)" : "+v"(kp[0]), "+v"(kp[1]), "+v"(kp[2]), "+v"(kp[3]), "+v"(kp[4]), "+v"(kp[5]), "+v"(kp[6]), "+v"(kp[7]),
                     "+v"(kp[8]), "+v"(kp[9]), "+v"(kp[10]), "+v"(kp[11]), "+v"(kp[12]), "+v"(kp[13]), "+v"(kp[14]), "+v"(kp[15])::"memory");
        f32x4 s0 = __builtin_bit_cast(f32x4, kp[0]), s1 = __builtin_bit_cast(f32x4, kp[1]);
#pragma unroll
        for (int i = 2; i < 16; i += 2) { s0 += __builtin_bit_cast(f32x4, kp[i]); s1 += __builtin_bit_cast(f32x4, kp[i + 1]); }
        if (tid < 210) *(LAS u32x2*)(lb + L_KVT + tid * 8) = pack4(s0 + s1);
    }
    STAMP(38);
    BAR();
    STAMP(39);
    if (w < 2) sfor<0, 5>([&](auto U) { glds16<U.v * 1024>(voff160[U.v], x + (size_t)(8 * i2 + 4 * half + 2 * w) * 16 * 1280, lb + L_XRES + w * 5120); });
    {
        const u32x2 zz = {0, 0};
        auto kvchunk = [&](int head, int e, int c) { return *(LAS u32x2*)(lb + L_KVT + (head * 420 + e * 20 + 4 * c) * 2); };
        auto mk = [&](u32x2 lo, u32x2 hi) { u32x4 r = {lo[0], lo[1], hi[0], hi[1]}; return __builtin_bit_cast(s16x8, r); };
        s16x8 Ama = mk(kvchunk(0, n, g), g == 0 ? kvchunk(0, n, 4) : zz);
        s16x8 Amb = mk(kvchunk(1, n, g), g == 1 ? kvchunk(1, n, 4) : zz);
        s16x8 Aza = mk(kvchunk(0, 20, g), g == 0 ? kvchunk(0, 20, 4) : zz);
        s16x8 Azb = mk(kvchunk(1, 20, g), g == 1 ? kvchunk(1, 20, 4) : zz);
        u32x2 ta_lo = kvchunk(0, 16 + (n & 3), g), ta_hi = kvchunk(0, 16 + (n & 3), 4);
        u32x2 tb_lo = kvchunk(1, 16 + (n & 3), g), tb_hi = kvchunk(1, 16 + (n & 3), 4);
        s16x8 Ata = mk(n < 4 ? ta_lo : zz, (n < 4 && g == 0) ? ta_hi : zz);
        s16x8 Atb = mk((n >= 4 && n < 8) ? tb_lo : zz, (n >= 4 && n < 8 && g == 1) ? tb_hi : zz);
#pragma unroll
        for (int m = 0; m < 2; ++m) {
            f32x4 Da = mfma(Ama, Fa[m], z4), Db = mfma(Amb, Fb[m], z4);
            f32x4 Dt = mfma(Atb, Fb[m], mfma(Ata, Fa[m], z4));
            f32x4 Dza = mfma(Aza, Fa[m], z4), Dzb = mfma(Azb, Fb[m], z4);
            const float ra = __builtin_amdgcn_rcpf(Dza[0] + 1e-6f), rb = __builtin_amdgcn_rcpf(Dzb[0] + 1e-6f);
            const int mt = 8 * i1 + 2 * w + m;
            s16x8 of = pack8(Da * ra, Db * rb);
            st16_sc1((unsigned)((mt * 20 + j1) * 1024 + lane * 16), ws + WS_O, __builtin_bit_cast(u32x4, of));
            u32x2 tl = pack4(Dt * (g == 0 ? ra : rb));
            const int t = j1 >> 1;
            if (g < 2) st8_sc1((unsigned)((mt * 20 + 16 + (t >> 1)) * 1024 + (n + 16 * (2 * (j1 & 1) + g)) * 16 + (t & 1) * 8), ws + WS_O, tl);
        }
    }
    STAMP(40);
    drain_bar();
    if (tid < 16) flags_store(ws, 1, i1 * 16 + tid, j1, tag);
    STAMP(5);

    const int T0 = j2 < 12 ? 7 * j2 : 84 + 6 * (j2 - 12);
    const int odd = (j2 < 12) ? (j2 & 1) : 0;
    const int Tp0 = T0 + odd;
    const int Tsg = j2 < 12 ? (odd ? T0 : T0 + 6) : Tp0;
    const unsigned vlan16 = lane * 16;
    sfor<18, 25>([&](auto P) {
        constexpr int tt = P.v / 5, u = P.v % 5;
        glds16<tt * 20480 + u * 1024>(voff160[u], worun, ldsWw);
        if constexpr (u == 4) { worun += 128 * 1280; OPAQUE_S(worun); }
    });
    const int mtW = 8 * i2 + 4 * half + w;
    const int ksb = w < 2 ? 14 * w : 28 + 13 * (w - 2);
    const int nks = w < 2 ? 14 : 13;
    s16x8 V[14][5];
    const char* w2run = ws + WS_W2R + (size_t)(c8 * 54 + ksb) * 5120;
    const int step13 = nks == 14 ? 5120 : 0;
#define ISSUE_V(it, t) do { if constexpr ((it) < 8) ld16a_sc1(V[it][t], vlan16 + (t) * 1024, w2run); else ld16_sc1(V[it][t], vlan16 + (t) * 1024, w2run); \
            if ((t) == 4) { w2run += ((it) == 12 ? step13 : 5120); OPAQUE_S(w2run); } } while (0)
    STAMP(45);
    await<0>(ws, 1, i2 * 16 + jw, tag, w, lane);
    STAMP(6);
    {
        s16x8 O[20];
        unsigned orun = (unsigned)(mtW * 20 * 1024 + lane * 16);
#define ISSUE_O(i) do { ld16_sc1(O[i], orun, ws + WS_O); orun += 1024u; asm volatile("" : "+v"(orun)); } while (0)
        sfor<0, 8>([&](auto I) { ISSUE_O(I.v); });
        unsigned vA = lb + L_A + (4 * g + q4) * 160 + 8 * p4, vA2 = vA + 8 * 6400;
        const unsigned vB = lb + L_A + (20 * g + 16 + q4) * 160 + 8 * p4;
        unsigned vB2 = vB + 2 * 25600;
        asm volatile("" : "+v"(vA), "+v"(vA2), "+v"(vB2));
        f32x4 a[5];
#pragma unroll
        for (int t = 0; t < 5; ++t) a[t] = z4;
        s16x4 Wl[3][5], Wh[3][5];
        auto side = [&](auto I, auto J) {
            constexpr int i = I.v, j = J.v, r = i % 3, t = j >> 1;
            if constexpr (i < 16) {
                const unsigned a_ = (i < 8 ? vA : vA2) + (i & 7) * 6400 + 32 * t;
                if constexpr (j & 1) Wh[r][t] = trread(a_ + 3200); else Wl[r][t] = trread(a_);
            } else {
                const unsigned a_ = (i < 18 ? vB : vB2) + (i & 1) * 25600 + 32 * t;
                if constexpr (j & 1) Wh[r][t] = trread(a_ + 12800); else Wl[r][t] = trread(a_);
            }
        };
        sfor<0, 2>([&](auto I) { sfor<0, 10>([&](auto J) { side(I, J); }); });
        SB();
        sfor<0, 20>([&](auto I) {
            constexpr int i = I.v;
            constexpr int ny = ((i + 7 < 19 ? i + 7 : 19) - i) + (i > 12 ? (4 * (i - 12) < 30 ? 4 * (i - 12) : 30) : 0);
            asm volatile("s_waitcnt vmcnt(%1)" : "+v"(O[i]) : "i"(ny) : "memory");
            SB();
            sfor<0, 5>([&](auto T) {
                constexpr int t = T.v;
                a[t] = mfma(cat(Wl[i % 3][t], Wh[i % 3][t]), O[i], a[t]);
                if constexpr (i + 2 < 20) { side(IC<i + 2>{}, IC<2 * t>{}); side(IC<i + 2>{}, IC<2 * t + 1>{}); }
                if constexpr (t == 1 && i + 8 < 20) ISSUE_O(i + 8);
                if constexpr (i >= 12 && t < 4) {
                    constexpr int bg = 4 * (i - 12) + t;
                    if constexpr (bg < 10) {
                        const int Tg = bg < 5 ? Tp0 + 5 : Tsg;
                        glds16c<(5 + bg / 5) * 20480 + (bg % 5) * 4096>(vlan16, ws + WS_W1R + (size_t)(Tg * 20 + (bg % 5) * 4 + w) * 1024, lb + w * 1024);
                    } else if constexpr (bg < 30) ISSUE_V((bg - 10) / 5, (bg - 10) % 5);
                }
                SB();
            });
        });
        STAMP(28);
        float su = 0.f, s2 = 0.f;
        u32x2 hpk[5];
#pragma unroll
        for (int t = 0; t < 5; ++t) {
            const u32x2 xr = *(LAS u32x2*)(lb + L_XRES + (16 * w + n) * 160 + 32 * t + 8 * g);
            const f32x4 hv = f32x4{bflo(xr[0]), bfhi(xr[0]), bflo(xr[1]), bfhi(xr[1])} + a[t];
            hpk[t] = pack4(hv);
            *(LAS u32x2*)(lb + L_XRES + (16 * w + n) * 160 + 32 * t + 8 * g) = hpk[t];
#pragma unroll
            for (int r = 0; r < 4; ++r) { su += hv[r]; s2 += hv[r] * hv[r]; }
        }
        const unsigned hb = (unsigned)((mtW * 20 + ((5 * c8) >> 1)) * 1024 + lane * 16);
        const int od = c8 & 1;
        st8_sc1(hb + (od ? 8 : 2048), ws + WS_H, od ? hpk[0] : hpk[4]);
        { const u32x2 p0 = od ? hpk[1] : hpk[0], p1 = od ? hpk[2] : hpk[1]; st16_sc1(hb + (od ? 1024 : 0), ws + WS_H, u32x4{p0[0], p0[1], p1[0], p1[1]}); }
        { const u32x2 p0 = od ? hpk[3] : hpk[2], p1 = od ? hpk[4] : hpk[3]; st16_sc1(hb + (od ? 2048 : 1024), ws + WS_H, u32x4{p0[0], p0[1], p1[0], p1[1]}); }
        su = add_xor32(add_xor16(su)); s2 = add_xor32(add_xor16(s2));
        if (g == 0) { u32x2 st = {__float_as_uint(su), __float_as_uint(s2)}; st8_sc1((unsigned)((mtW * 16 + n) * 64 + c8 * 8), ws + WS_STAT2, st); }
    }
    STAMP(42);
    drain_bar();
    if (tid < 16) flags_store(ws, 2, i2 * 16 + tid, jw, tag);
    if (b == 0 && tid == 0) __hip_atomic_store((GAS u64*)ws, tag, RLX_AGENT);
    STAMP(7);

    {
        const char* s1 = ws + WS_W1R + (size_t)(Tp0 * 20 + w) * 1024;
        const unsigned l1 = lb + L_A + w * 1024;
        sfor<0, 25>([&](auto U) { glds16c<U.v * 4096>(vlan16, s1, l1); s1 += 4096; OPAQUE_S(s1); });
    }
    STAMP(43);
    await<0>(ws, 2, i2 * 16 + j2, tag, w, lane);
    STAMP(8);
    {
        s16x8 H[20][2];
        const int mt0 = 8 * i2 + 2 * w;
        s16x8 stv[2];
#pragma unroll
        for (int m = 0; m < 2; ++m) ld16_sc1(stv[m], (unsigned)(((mt0 + m) * 16 + n) * 64 + g * 16), ws + WS_STAT2);
        unsigned hrun = (unsigned)(mt0 * 20 * 1024 + lane * 16);
#define ISSUE_H(ks) do { ld16_sc1(H[ks][0], hrun, ws + WS_H); ld16_sc1(H[ks][1], hrun + 20 * 1024, ws + WS_H); hrun += 1024; asm volatile("" : "+v"(hrun)); } while (0)
        unsigned vF0 = lb + vlan16, vF1 = lb + 61440 + vlan16, vF2 = lb + 122880 + vlan16;
        asm volatile("" : "+v"(vF0), "+v"(vF1), "+v"(vF2));
        auto wread = [&](auto TT, auto KS) -> s16x8 {
            constexpr int t = TT.v, ks = KS.v;
            return *(volatile LAS s16x8*)((t < 3 ? vF0 : t < 6 ? vF1 : vF2) + (t % 3) * 20480 + ks * 1024);
        };
        {
            f32x4 c0 = z4, c1 = z4;
            const unsigned ta = lb + vlan16 + w * 20480, tb = lb + vlan16 + (w < 3 ? w + 4 : w) * 20480;
            s16x8 Ca[4], Cb[4];
            sfor<0, 3>([&](auto K) { Ca[K.v] = *(volatile LAS s16x8*)(ta + K.v * 1024); Cb[K.v] = *(volatile LAS s16x8*)(tb + K.v * 1024); });
            SB();
            sfor<0, 20>([&](auto K) {
                constexpr int k = K.v;
                c0 = mfma(ones, Ca[k & 3], c0);
                if constexpr (k + 3 < 20) Ca[(k + 3) & 3] = *(volatile LAS s16x8*)(ta + (k + 3) * 1024);
                SB();
                c1 = mfma(ones, Cb[k & 3], c1);
                if constexpr (k + 3 < 20) Cb[(k + 3) & 3] = *(volatile LAS s16x8*)(tb + (k + 3) * 1024);
                if constexpr ((k & 1) == 0 && k < 16) ISSUE_H(k >> 1);
                SB();
            });
            auto split = [&](float c) { const unsigned hi = pk2(c, c); const float lo = c - bflo(hi); u32x2 r = {hi, pk2(lo, lo)}; return r; };
            if (g == 0) {
                *(LAS u32x2*)(lb + L_CS1 + w * 128 + n * 8) = split(c0[0]);
                if (w < 3) *(LAS u32x2*)(lb + L_CS1 + (w + 4) * 128 + n * 8) = split(c1[0]);
            }
            if (tid < 2) *(LAS u64*)(lb + L_CS1 + 896 + tid * 8) = 0;
        }
        STAMP(19);
        asm volatile("s_waitcnt vmcnt(16)" : "+v"(stv[0]), "+v"(stv[1])::"memory");
        s16x8 Hm[2];
#pragma unroll
        for (int m = 0; m < 2; ++m) {
            f32x4 s = __builtin_bit_cast(f32x4, stv[m]);
            const float nm = add_xor32(add_xor16(s[0] + s[2])) * (-1.f / 640.f);
            const unsigned hi = pk2(nm, nm);
            const unsigned pr = g == 0 ? ((hi & 0xffffu) | (pk2(nm - bflo(hi), 0.f) << 16)) : 0u;
            Hm[m] = __builtin_bit_cast(s16x8, u32x4{pr, pr, 0u, 0u});
        }
        BAR();
        const unsigned csa = lb + L_CS1 + (g == 0 ? n * 8 : 896);
        auto csfrag = [&](int t) { const u32x2 c = *(LAS u32x2*)(csa + (g == 0 ? t * 128 : 0)); return __builtin_bit_cast(s16x8, u32x4{c[0], c[1], 0u, 0u}); };
        auto relu2 = [&](unsigned p) { typedef short s2_ __attribute__((ext_vector_type(2))); const s2_ z = {0, 0};
                                       return __builtin_bit_cast(unsigned, __builtin_elementwise_max(__builtin_bit_cast(s2_, p), z)); };
        auto fin8 = [&](f32x4 x0, f32x4 x1) { return u32x4{relu2(pk2(x0[0], x0[1])), relu2(pk2(x0[2], x0[3])), relu2(pk2(x1[0], x1[1])), relu2(pk2(x1[2], x1[3]))}; };
        f32x4 a1[4][2];
        sfor<0, 4>([&](auto TT) { const s16x8 cf = csfrag(TT.v); a1[TT.v][0] = mfma(cf, Hm[0], z4); a1[TT.v][1] = mfma(cf, Hm[1], z4); });
        {
            s16x8 Wf[3][4];
            sfor<0, 2>([&](auto KS) { sfor<0, 4>([&](auto TT) { Wf[KS.v][TT.v] = wread(TT, KS); }); });
            SB();
            sfor<0, 20>([&](auto KS) {
                constexpr int ks = KS.v;
                constexpr int ny = 2 * ((ks + 7 < 19 ? ks + 7 : 19) - ks) + (ks > 12 ? (5 * (ks - 12) + 1) / 2 : 0);
                asm volatile("s_waitcnt vmcnt(%2)" : "+v"(H[ks][0]), "+v"(H[ks][1]) : "i"(ny) : "memory");
                SB();
                sfor<0, 8>([&](auto I) {
                    constexpr int i = I.v, t = i >> 1, m = i & 1;
                    a1[t][m] = mfma(Wf[ks % 3][t], H[ks][m], a1[t][m]);
                    if constexpr (ks + 2 < 20 && (i & 1) == 0) Wf[(ks + 2) % 3][t] = wread(IC<t>{}, IC<ks + 2>{});
                    if constexpr (i == 3 && ks + 8 < 20) ISSUE_H(ks + 8);
                    if constexpr (ks >= 12 && (i == 1 || i == 4 || (i == 7 && (ks & 1) == 0))) {
                        constexpr int pv = 20 + (5 * (ks - 12) + 1) / 2 + (i == 1 ? 0 : i == 4 ? 1 : 2); ISSUE_V(pv / 5, pv % 5); }
                    SB();
                });
            });
        }
        STAMP(20);
        f32x4 a2[3][2];
        sfor<0, 3>([&](auto TT) { const s16x8 cf = csfrag(4 + TT.v); a2[TT.v][0] = mfma(cf, Hm[0], z4); a2[TT.v][1] = mfma(cf, Hm[1], z4); });
        {
            s16x8 Wf[3][3];
            sfor<0, 2>([&](auto KS) { sfor<0, 3>([&](auto TT) { Wf[KS.v][TT.v] = wread(IC<4 + TT.v>{}, KS); }); });
            u32x2 e0, e1;
            sfor<0, 20>([&](auto KS) {
                constexpr int ks = KS.v;
                sfor<0, 6>([&](auto I) {
                    constexpr int i = I.v, t = i >> 1, m = i & 1;
                    a2[t][m] = mfma(Wf[ks % 3][t], H[ks][m], a2[t][m]);
                    if constexpr (ks + 2 < 20 && (i & 1) == 0) Wf[(ks + 2) % 3][t] = wread(IC<4 + t>{}, IC<ks + 2>{});
                    if constexpr (ks >= 5 && (i == 3 || i == 5)) { constexpr int pv = 40 + 2 * (ks - 5) + (i == 5); ISSUE_V(pv / 5, pv % 5); }
                    if constexpr (ks >= 1 && ks < 17 && ((ks - 1) & 3) < 3) {
                        constexpr int u = (ks - 1) >> 2, ph = (ks - 1) & 3, P = u >> 1, mm = u & 1;
                        if constexpr (ph == 0 && i == 1) e0 = u32x2{relu2(pk2(a1[2 * P][mm][0], a1[2 * P][mm][1])), relu2(pk2(a1[2 * P][mm][2], a1[2 * P][mm][3]))};
                        if constexpr (ph == 1 && i == 1) e1 = u32x2{relu2(pk2(a1[2 * P + 1][mm][0], a1[2 * P + 1][mm][1])), relu2(pk2(a1[2 * P + 1][mm][2], a1[2 * P + 1][mm][3]))};
                        if constexpr (ph == 2 && i == 1)
                            st16_sc1((unsigned)(((mt0 + mm) * 54 + (Tp0 >> 1) + P) * 1024 + lane * 16), ws + WS_H1, u32x4{e0[0], e0[1], e1[0], e1[1]});
                    }
                    SB();
                });
            });
        }
        STAMP(22);
#pragma unroll
        for (int m = 0; m < 2; ++m) {
            st16_sc1((unsigned)(((mt0 + m) * 54 + (Tp0 >> 1) + 2) * 1024 + lane * 16), ws + WS_H1, fin8(a2[0][m], a2[1][m]));
            if (j2 < 12) st8_sc1((unsigned)(((mt0 + m) * 54 + (Tsg >> 1)) * 1024 + lane * 16 + (Tsg & 1) * 8), ws + WS_H1,
                                 u32x2{relu2(pk2(a2[2][m][0], a2[2][m][1])), relu2(pk2(a2[2][m][2], a2[2][m][3]))});
        }
        STAMP(24);
        drain_bar();
        if (tid < 16) flags_store(ws, 3, i2 * 16 + tid, j2, tag);
        STAMP(9);

        STAMP(44);
        await<63>(ws, 3, i2 * 16 + jw, tag, w, lane);
        STAMP(10);
        unsigned arun = (unsigned)(((8 * i2 + 4 * half) * 54 + ksb) * 1024 + lane * 16);
        const unsigned astep13 = nks == 14 ? 1024u : 0u;
        s16x8 A[4][4];
        unsigned aoff[4];
#pragma unroll
        for (int m = 0; m < 4; ++m) aoff[m] = ((w + m) & 3) * 54 * 1024;
        s16x8 st2v;
        ld16_sc1(st2v, (unsigned)((mtW * 16 + n) * 64 + g * 16), ws + WS_STAT2);
#define ISSUE_A(it, m) do { ld16_sc1(A[(it) & 3][m], arun + aoff[m], ws + WS_H1); \
            if ((m) == 3) { arun += ((it) == 12 ? astep13 : 1024u); asm volatile("" : "+v"(arun)); } } while (0)
        sfor<0, 3>([&](auto IT) { sfor<0, 4>([&](auto M) { ISSUE_A(IT.v, M.v); }); });
        f32x4 a[4][5];
#pragma unroll
        for (int m = 0; m < 4; ++m)
#pragma unroll
            for (int t = 0; t < 5; ++t) a[m][t] = z4;
        sfor<0, 14>([&](auto IT) {
            constexpr int it = IT.v;
            constexpr int ny = 4 * ((it + 2 < 13 ? it + 2 : 13) - it);
            if constexpr (it == 1) STAMP(25);
            if constexpr (it == 5) STAMP(26);
            if constexpr (it == 9) STAMP(27);
            if constexpr (it < 8)
                asm volatile("s_waitcnt vmcnt(%9)" : "+v"(A[it & 3][0]), "+v"(A[it & 3][1]), "+v"(A[it & 3][2]), "+v"(A[it & 3][3]),
                             "+a"(V[it][0]), "+a"(V[it][1]), "+a"(V[it][2]), "+a"(V[it][3]), "+a"(V[it][4]) : "i"(ny) : "memory");
            else
                asm volatile("s_waitcnt vmcnt(%9)" : "+v"(A[it & 3][0]), "+v"(A[it & 3][1]), "+v"(A[it & 3][2]), "+v"(A[it & 3][3]),
                             "+v"(V[it][0]), "+v"(V[it][1]), "+v"(V[it][2]), "+v"(V[it][3]), "+v"(V[it][4]) : "i"(ny) : "memory");
            SB();
            if (it < 13 || nks == 14) {
                sfor<0, 20>([&](auto I) {
                    constexpr int i = I.v, t = i >> 2, m = i & 3;
                    a[m][t] = mfma(V[it][t], A[it & 3][m], a[m][t]);
                    if constexpr ((i & 3) == 1 && i < 16 && it + 3 < 14) ISSUE_A(it + 3, i >> 2);
                    SB();
                });
            }
        });
        STAMP(11);
        asm volatile("" : "+v"(st2v));
        const f32x4 sv = __builtin_bit_cast(f32x4, st2v);
        const float smean = add_xor32(add_xor16(sv[0] + sv[2])) * (1.f / 640.f);
        const float rstd2 = __builtin_amdgcn_rsqf(fmaxf(add_xor32(add_xor16(sv[1] + sv[3])) * (1.f / 640.f) - smean * smean, 0.f) + 1e-5f);
        BAR();
#pragma unroll
        for (int m = 1; m < 4; ++m)
#pragma unroll
            for (int t = 0; t < 5; ++t) *(LAS f32x4*)(lb + ((((w + m) & 3) * 3 + (m - 1)) * 5 + t) * 1024 + lane * 16) = a[m][t];
        BAR();
        STAMP(46);
        f32x4 fin[5];
#pragma unroll
        for (int t = 0; t < 5; ++t) {
            const LAS char* p = (const LAS char*)(lb + (w * 15 + t) * 1024 + lane * 16);
            fin[t] = ((a[0][t] + *(const LAS f32x4*)p) + (*(const LAS f32x4*)(p + 5120) + *(const LAS f32x4*)(p + 10240))) * rstd2;
        }
        STAMP(47);
        LAS char* ot = (LAS char*)(lb + L_B + w * 2560);
#pragma unroll
        for (int t = 0; t < 5; ++t) {
            u32x2 fp = pack4(fin[t]);
            const u32x2 hp = *(LAS u32x2*)(lb + L_XRES + (16 * w + n) * 160 + 32 * t + 8 * g);
            f32x4 ov = {bflo(hp[0]) + bflo(fp[0]), bfhi(hp[0]) + bfhi(fp[0]), bflo(hp[1]) + bflo(fp[1]), bfhi(hp[1]) + bfhi(fp[1])};
            *(LAS u32x2*)(ot + n * 160 + 32 * t + 8 * g) = pack4(ov);
        }
        asm volatile("s_waitcnt lgkmcnt(0)" ::: "memory");
        int lane3 = lane; asm volatile("" : "+v"(lane3));
#pragma unroll
        for (int u = 0; u < 3; ++u) {
            const int q = 64 * u + lane3, row = q / 10, sl = q - row * 10;
            if (q < 160) st16_sc1((unsigned)((mtW * 16 + row) * 1280 + 160 * c8 + 16 * sl), out, *(LAS u32x4*)(ot + q * 16));
        }
    }
    STAMP(12); STAMPRT(63);
}

extern "C" void kernel_launch(void* const* d_in, const int* in_sizes, int n_in, void* d_out, int out_size, void* d_ws, size_t ws_size,
                              hipStream_t stream) {
    (void)in_sizes; (void)n_in; (void)out_size; (void)ws_size;
    fused_block<<<256, 256, 0, stream>>>((const char*)d_in[0], (const char*)d_in[1], (const char*)d_in[2], (const char*)d_in[4],
                                         (const char*)d_in[5], (char*)d_out, (char*)d_ws);
}
```

```cpp
#include <hip/hip_runtime.h>
#include <stdint.h>
#pragma clang diagnostic ignored "-Wint-to-pointer-cast"

typedef short s16x4 __attribute__((ext_vector_type(4)));
typedef short s16x8 __attribute__((ext_vector_type(8)));
typedef float f32x4 __attribute__((ext_vector_type(4)));
typedef unsigned u32x4 __attribute__((ext_vector_type(4)));
typedef unsigned u32x2 __attribute__((ext_vector_type(2)));
typedef unsigned long long u64;
#define LAS __attribute__((address_space(3)))
#define GAS __attribute__((address_space(1)))
#define DI __device__ __forceinline__
#define RLX_AGENT __ATOMIC_RELAXED, __HIP_MEMORY_SCOPE_AGENT
#define SB() __builtin_amdgcn_sched_barrier(0)

constexpr size_t WS_FLAG = 4096;
constexpr size_t WS_GFLAG = 2048;
constexpr size_t WS_KVP = 262144;
constexpr size_t WS_STAT2 = 0x120000;
constexpr size_t WS_O = 2u << 20;
constexpr size_t WS_H = 5u << 20;
constexpr size_t WS_H1 = 8u << 20;
constexpr size_t WS_W1R = 16u << 20;
constexpr size_t WS_W2R = 19u << 20;
constexpr size_t WS_STAMP = 24u << 20;

constexpr int L_A = 0;
constexpr int L_B = 102400;
constexpr int L_XRES = L_B + 40960;
constexpr int L_M = 155648;
constexpr int L_MEAN = L_M;
constexpr int L_RSTD = L_M + 512;
constexpr int L_CSKV = L_M + 1024;
constexpr int L_CS4 = L_M + 1280;
constexpr int L_KVT = L_M + 1536;
constexpr int L_CS1 = L_M + 3328;
constexpr int L_JUNK = L_M + 5632;
constexpr int L_ZERO = L_M + 5888;
constexpr int L_CSQ = L_M + 5904 + 16;
constexpr int L_EPOCH = L_M + 7424;
constexpr int L_TRASH = L_M + 6400;
constexpr int LDS_BYTES = 163840;
constexpr int CODE_LINES = 368;

enum { OP_X, OP_W, OP_R, OP_O, OP_Q, OP_T, OP_S, OP_U };
struct P1Op { int kind, a, b, ks, slot; };
constexpr P1Op P1SEQ[] = {
    {OP_O, 0, 0, -1, 0},
    {OP_U, 0, 0, -1, 0}, {OP_U, 1, 0, -1, 0},
    {OP_X, 0, 0, -1, 0}, {OP_X, 0, 1, -1, 0}, {OP_X, 0, 2, -1, 0}, {OP_X, 0, 3, -1, 0},
    {OP_W, 0, 0, -1, 0}, {OP_W, 0, 1, -1, 0}, {OP_W, 0, 2, -1, 0}, {OP_W, 0, 3, -1, 0}, {OP_W, 0, 4, -1, 0},
    {OP_U, 2, 0, -1, 0}, {OP_U, 3, 0, -1, 0},
    {OP_X, 1, 0, -1, 0}, {OP_X, 1, 1, -1, 0}, {OP_X, 1, 2, -1, 0}, {OP_X, 1, 3, -1, 0},
    {OP_O, 1, 0, -1, 0},
    {OP_W, 1, 0, -1, 0}, {OP_W, 1, 1, -1, 0}, {OP_W, 1, 2, -1, 0}, {OP_W, 1, 3, -1, 0}, {OP_W, 1, 4, -1, 0},
    {OP_X, 2, 0, -1, 0}, {OP_X, 2, 1, -1, 0}, {OP_X, 2, 2, -1, 0}, {OP_X, 2, 3, -1, 0},
    {OP_W, 2, 0, 0, 3}, {OP_W, 2, 1, 0, 8}, {OP_W, 2, 2, 0, 13},
    {OP_X, 3, 0, 1, 2}, {OP_X, 3, 1, 1, 6}, {OP_X, 3, 2, 1, 10}, {OP_X, 3, 3, 1, 14},
    {OP_W, 2, 3, 2, 3}, {OP_O, 2, 0, 2, 5}, {OP_W, 2, 4, 2, 8}, {OP_W, 3, 0, 2, 13},
    {OP_X, 4, 0, 3, 2}, {OP_X, 4, 1, 3, 6}, {OP_X, 4, 2, 3, 10}, {OP_X, 4, 3, 3, 14},
    {OP_W, 3, 1, 4, 3}, {OP_U, 4, 0, 4, 5}, {OP_W, 3, 2, 4, 8}, {OP_W, 3, 3, 4, 13},
    {OP_X, 5, 0, 5, 2}, {OP_X, 5, 1, 5, 6}, {OP_X, 5, 2, 5, 10}, {OP_X, 5, 3, 5, 14},
    {OP_W, 3, 4, 6, 3}, {OP_W, 4, 0, 6, 8}, {OP_W, 4, 1, 6, 13},
    {OP_X, 6, 0, 7, 2}, {OP_X, 6, 1, 7, 6}, {OP_X, 6, 2, 7, 10}, {OP_X, 6, 3, 7, 14},
    {OP_W, 4, 2, 8, 3}, {OP_W, 4, 3, 8, 8}, {OP_W, 4, 4, 8, 13},
    {OP_X, 7, 0, 9, 2}, {OP_X, 7, 1, 9, 6}, {OP_X, 7, 2, 9, 10}, {OP_X, 7, 3, 9, 14},
    {OP_R, 0, 0, 10, 3}, {OP_R, 1, 0, 10, 8}, {OP_R, 2, 0, 10, 13},
    {OP_X, 8, 0, 11, 2}, {OP_X, 8, 1, 11, 6}, {OP_X, 8, 2, 11, 10}, {OP_X, 8, 3, 11, 14},
    {OP_R, 3, 0, 12, 3}, {OP_R, 4, 0, 12, 8}, {OP_U, 5, 0, 12, 13},
    {OP_X, 9, 0, 13, 2}, {OP_X, 9, 1, 13, 6}, {OP_X, 9, 2, 13, 10}, {OP_X, 9, 3, 13, 14},
    {OP_Q, 0, 0, 15, 3}, {OP_Q, 0, 1, 15, 8}, {OP_Q, 0, 2, 15, 13}, {OP_Q, 0, 3, 16, 3}, {OP_Q, 0, 4, 16, 8},
    {OP_T, 0, 0, 17, 1}, {OP_T, 1, 0, 17, 2}, {OP_Q, 1, 0, 17, 3}, {OP_T, 2, 0, 17, 5}, {OP_T, 3, 0, 17, 6}, {OP_T, 4, 0, 17, 7},
    {OP_Q, 1, 1, 17, 8}, {OP_Q, 1, 2, 17, 13},
    {OP_S, 0, 0, 18, 1}, {OP_Q, 1, 3, 18, 3}, {OP_S, 1, 0, 18, 6}, {OP_Q, 1, 4, 18, 8}, {OP_S, 2, 0, 18, 11}, {OP_S, 3, 0, 19, 2}, {OP_S, 4, 0, 19, 7},
};
constexpr int P1N = sizeof(P1SEQ) / sizeof(P1SEQ[0]);
constexpr int p1_wait(int s) {
    int issued = 0, need = 0;
    for (int i = 0; i < P1N; ++i) {
        if (P1SEQ[i].ks < 2 * s - 1 || P1SEQ[i].ks < 0) issued = i + 1;
        if ((P1SEQ[i].kind == OP_X && P1SEQ[i].a == s) || (P1SEQ[i].kind == OP_W && P1SEQ[i].a <= (s >> 1))) need = need > i + 1 ? need : i + 1;
    }
    return issued - need;
}
constexpr bool p1_ok() { for (int s = 0; s < 10; ++s) if (p1_wait(s) < 0 || p1_wait(s) > 63) return false; return true; }
static_assert(p1_ok(), "P1a schedule: an op is waited for before it is issued");

#define STAMP(i) do {} while (0)
#define STAMPRT(i) do {} while (0)

template <int I> struct IC { static constexpr int v = I; };
template <int B, int E, class F> DI void sfor(F&& f) { if constexpr (B < E) { f(IC<B>{}); sfor<B + 1, E>(f); } }
#define OPAQUE_S(p) asm volatile("" : "+s"(p))

DI f32x4 mfma(s16x8 a, s16x8 b, f32x4 c) { return __builtin_amdgcn_mfma_f32_16x16x32_bf16(a, b, c, 0, 0, 0); }
DI s16x4 trread(unsigned addr) { return __builtin_bit_cast(s16x4, __builtin_amdgcn_ds_read_tr16_b64_v4i16((LAS s16x4*)addr)); }
DI s16x8 cat(s16x4 a, s16x4 b) { return __builtin_shufflevector(a, b, 0, 1, 2, 3, 4, 5, 6, 7); }
DI unsigned pk2(float lo, float hi) {
    typedef __bf16 bf2 __attribute__((ext_vector_type(2)));
    typedef float f2 __attribute__((ext_vector_type(2)));
    f2 v = {lo, hi};
    return __builtin_bit_cast(unsigned, __builtin_convertvector(v, bf2));
}
DI s16x8 pack8(f32x4 a, f32x4 b) {
    u32x4 r = {pk2(a[0], a[1]), pk2(a[2], a[3]), pk2(b[0], b[1]), pk2(b[2], b[3])};
    return __builtin_bit_cast(s16x8, r);
}
DI u32x2 pack4(f32x4 a) { u32x2 r = {pk2(a[0], a[1]), pk2(a[2], a[3])}; return r; }
DI float bflo(unsigned u) { return __uint_as_float(u << 16); }
DI float bfhi(unsigned u) { return __uint_as_float(u & 0xffff0000u); }
DI float phi(float t) { return (fmaxf(t, 0.f) + 1e-6f) + __builtin_amdgcn_fmed3f(__builtin_amdgcn_exp2f(t * 1.44269504088896f), 0.f, 1.f); }
DI float add_xor16(float v) { const unsigned u = __float_as_uint(v); auto r = __builtin_amdgcn_permlane16_swap(u, u, false, false); return __uint_as_float(r[0]) + __uint_as_float(r[1]); }
DI float add_xor32(float v) { const unsigned u = __float_as_uint(v); auto r = __builtin_amdgcn_permlane32_swap(u, u, false, false); return __uint_as_float(r[0]) + __uint_as_float(r[1]); }
constexpr int wq_pos(int grp) { return grp < 3 ? 16384 + 5120 * grp : grp < 7 ? 32768 + 5120 * (grp - 3) : 5120 * (grp - 7); }

template <int LOFF> DI void glds16(unsigned voff, const void* sbase, unsigned lds) {
    asm volatile("s_add_u32 m0, %2, %3\n\ts_nop 0\n\tglobal_load_lds_dwordx4 %0, %1" ::"v"(voff), "s"(sbase), "s"(lds), "i"(LOFF) : "memory", "scc");
}
template <int LOFF, int GOFF> DI void glds16i(unsigned voff, const void* sbase, unsigned lds) {
    asm volatile("s_add_u32 m0, %2, %3\n\ts_nop 0\n\tglobal_load_lds_dwordx4 %0, %1 offset:%4" ::"v"(voff), "s"(sbase), "s"(lds), "i"(LOFF - GOFF), "i"(GOFF) : "memory", "scc");
}
template <int LOFF> DI void glds16c(unsigned voff, const void* sbase, unsigned lds) {
    asm volatile("s_add_u32 m0, %2, %3\n\ts_nop 0\n\tglobal_load_lds_dwordx4 %0, %1 sc1" ::"v"(voff), "s"(sbase), "s"(lds), "i"(LOFF) : "memory", "scc");
}
DI void glds4(unsigned voff, const void* sbase, unsigned lds) {
    asm volatile("s_mov_b32 m0, %2\n\ts_nop 0\n\tglobal_load_lds_dword %0, %1" ::"v"(voff), "s"(sbase), "s"(lds) : "memory");
}
#define WAITV(n) asm volatile("s_waitcnt vmcnt(%0)" ::"i"(n) : "memory")
#define BARX() do { __builtin_amdgcn_s_barrier(); asm volatile("" ::: "memory"); } while (0)
#define BAR() do { asm volatile("s_waitcnt lgkmcnt(0)" ::: "memory"); __builtin_amdgcn_s_barrier(); asm volatile("" ::: "memory"); } while (0)

DI void ld16_sc1(s16x8& d, unsigned voff, const void* sbase) {
    asm volatile("global_load_dwordx4 %0, %1, %2 sc1" : "=v"(d) : "v"(voff), "s"(sbase) : "memory");
}
DI void ld16a_sc1(s16x8& d, unsigned voff, const void* sbase) {
    asm volatile("global_load_dwordx4 %0, %1, %2 sc1" : "=a"(d) : "v"(voff), "s"(sbase) : "memory");
}
DI void ld8_sc1(u64& d, unsigned voff, const void* sbase) { asm volatile("global_load_dwordx2 %0, %1, %2 sc1" : "=v"(d) : "v"(voff), "s"(sbase) : "memory"); }
DI void st16_sc1(unsigned voff, void* sbase, u32x4 v) {
    asm volatile("global_store_dwordx4 %0, %1, %2 sc1\n\ts_nop 1" ::"v"(voff), "v"(v), "s"(sbase) : "memory");
}
DI void st8_sc1(unsigned voff, void* sbase, u32x2 v) {
    asm volatile("global_store_dwordx2 %0, %1, %2 sc1" ::"v"(voff), "v"(v), "s"(sbase) : "memory");
}
DI void drain_bar() { WAITV(0); BAR(); }
DI void flags_store(char* ws, int arr, int line, int slot, u64 tag) {
    __hip_atomic_store((GAS u64*)(ws + WS_FLAG + (size_t)arr * 32768 + (size_t)line * 128) + slot, tag, RLX_AGENT);
}
template <int NV> DI void await(char* ws, int arr, int line, u64 tag, int w, int lane) {
    if (w == 0) {
        GAS u64* fl = (GAS u64*)(ws + WS_FLAG + (size_t)arr * 32768 + (size_t)line * 128) + (lane & 15);
        for (unsigned spins = 0; spins < (1u << 20); ++spins) {
            u64 f = __hip_atomic_load(fl, RLX_AGENT);
            if (__all(f == tag)) break;
        }
    }
    WAITV(NV);
    BAR();
}

__global__ void __launch_bounds__(256, 1)
fused_block(const char* __restrict__ x, const char* __restrict__ wqkv, const char* __restrict__ wo, const char* __restrict__ w1,
            const char* __restrict__ w2, char* __restrict__ out, char* __restrict__ ws) {
    __shared__ __attribute__((aligned(16))) char smem[LDS_BYTES];
    const unsigned lb = (unsigned)(uintptr_t)(LAS char*)smem;
    const int tid = threadIdx.x, lane = tid & 63;
    const int w = __builtin_amdgcn_readfirstlane(tid >> 6);
    const int b = blockIdx.x, x8 = b & 7, rr = b >> 3;
    const int i1 = 4 * (x8 >> 1) + (rr >> 3), j1 = 8 * (x8 & 1) + (rr & 7);
    const int i2 = i1, j2 = j1;
    const int half = (rr >> 2) & 1, c8 = 4 * (x8 & 1) + (rr & 3), jw = 8 * half + c8;
    const int g = lane >> 4, n = lane & 15, q4 = n >> 2, p4 = lane & 3;
    const s16x8 ones = {0x3F80, 0x3F80, 0x3F80, 0x3F80, 0x3F80, 0x3F80, 0x3F80, 0x3F80};
    const f32x4 z4 = {0, 0, 0, 0};
    STAMP(0); STAMPRT(62);

    s16x8 X[20][2];
    f32x4 acc[5][2], sq[2], rs[2], csA, csB;
    unsigned voffW[5], voffX[4];
    const char* xb = x + (size_t)i1 * 128 * 1280;
    const unsigned ldsXw = lb + L_B + w * 4096, ldsWw = lb + L_A + w * 5120;
#pragma unroll
    for (int u = 0; u < 4; ++u)
        voffX[u] = (32 * w + 8 * u + (lane >> 3)) * 1280 + 16 * ((lane & 7) ^ ((lane >> 4) + 4 * (u & 1)));
#define ISSUE_X(s, u) glds16i<((s) % 3) * 16384 + (u) * 1024, (s) * 128>(voffX[u], xb, ldsXw)
#define ISSUE_XS(s) do { ISSUE_X(s, 0); ISSUE_X(s, 1); ISSUE_X(s, 2); ISSUE_X(s, 3); } while (0)
#pragma unroll
    for (int u = 0; u < 5; ++u) {
        int q = 64 * u + lane, row = q / 10, s = q - row * 10;
        row ^= ((row >> 3) & 1) << 2;
        voffW[u] = row * 3840 + (s < 5 ? (640 + 40 * j1) * 2 + 16 * s : (1280 + 40 * j1) * 2 + 16 * (s - 5));
    }
    const char* wkb = wqkv + (size_t)w * (32 * 3840);
    const char* wrun = wkb;
#define ISSUE_W(t, u) do { glds16<(t) * 20480 + (u) * 1024>(voffW[u], wrun, ldsWw); if ((u) == 4) { wrun += 128 * 3840; OPAQUE_S(wrun); } } while (0)
    unsigned voffQ[5];
    auto make_voffQ = [&] {
        int l2 = lane; asm volatile("" : "+v"(l2));
#pragma unroll
        for (int u = 0; u < 5; ++u) {
            int q = 64 * u + l2, row = q / 5, s = q - row * 5;
            row ^= ((row >> 3) & 1) << 2;
            voffQ[u] = row * 3840 + 80 * j1 + 16 * s;
        }
        asm volatile("" : "+v"(voffQ[0]), "+v"(voffQ[1]), "+v"(voffQ[2]), "+v"(voffQ[3]), "+v"(voffQ[4]));
    };
    auto issueQ = [&](auto A, auto U) {
        constexpr int a = A.v, u = U.v;
        const int grp = a == 0 ? (w < 2 ? w : 2) : a == 1 ? 3 + w : 7 + (w < 2 ? w : 2);
        const int pos = a == 0 ? 16384 + 5120 * grp : a == 1 ? 32768 + 5120 * w : 5120 * (grp - 7);
        glds16<u * 1024>(voffQ[u], wqkv + (size_t)grp * (64 * 3840), lb + L_B + pos);
    };
    const unsigned raR = lb + L_A + (4 * g + q4) * 128 + 16 * ((2 * w + (p4 >> 1)) ^ ((((4 * g + q4) >> 1) & 3) << 1)) + 8 * (p4 & 1);
    s16x8 Rf[5];
    auto runit = [&](auto R, unsigned& isw1, unsigned& ksr, unsigned& cl) {
        constexpr int r = R.v;
        const unsigned ub = (unsigned)b;
        unsigned v;
        if constexpr (r < 2) { isw1 = 1; v = ub + 256 * r; }
        else if constexpr (r == 2) { isw1 = ub < 28; v = isw1 ? ub + 512 : ub - 28; }
        else if constexpr (r == 3) { isw1 = 0; v = ub + 228; }
        else { isw1 = 0; v = ub < 56 ? ub + 484 : ub + 228; }
        ksr = isw1 ? (v * 2428u) >> 16 : (v * 6554u) >> 16;
        cl = v - ksr * (isw1 ? 27u : 10u);
    };
    auto p1issue = [&](auto N) {
        constexpr P1Op op = P1SEQ[N.v];
        if constexpr (op.kind == OP_X) ISSUE_X(op.a, op.b);
        else if constexpr (op.kind == OP_W) ISSUE_W(op.a, op.b);
        else if constexpr (op.kind == OP_R) {
            unsigned isw1, ksr, cl;
            runit(IC<op.a>{}, isw1, ksr, cl);
            const unsigned pitch = isw1 ? 3456u : 1280u;
            const unsigned rrow = 8 * w + (lane >> 3);
            glds16<op.a * 4096>(rrow * pitch + 16 * ((lane & 7) ^ (((rrow >> 1) & 3) << 1)), (isw1 ? w1 : w2) + (ksr * 32 * pitch + cl * 128), lb + L_A + w * 1024);
        } else if constexpr (op.kind == OP_U) {
            const unsigned wv = rr * 4 + w;
            if constexpr (op.a == 0 || op.a == 2) {
                const unsigned l = lane < 20 ? lane : 0, rw = (l * 52u) >> 8;
                glds4((4 * wv + rw) * 1280 + (l - 5 * rw + (op.a == 2 ? 5 : 0)) * 128, x + (size_t)(x8 >> 1) * 512 * 1280, lb + L_JUNK);
            } else if constexpr (op.a == 1 || op.a == 3) {
                const unsigned li = wv * 25 + (lane < 25 ? lane : 0), rw = (li * 6554u) >> 16, rem = li - rw * 10;
                glds4((rw + (op.a == 3 ? 320 : 0)) * 3840 + 1280 + (rem >= 5 ? 640 : 0) + (x8 & 1) * 640 + rem * 128, wqkv, lb + L_JUNK);
            } else if constexpr (op.a == 4) {
                const unsigned li = wv * 25 + (lane < 25 ? lane : 0), rw = (li * 13108u) >> 16, rem = li - rw * 5;
                glds4(rw * 3840 + (x8 & 1) * 640 + rem * 128, wqkv, lb + L_JUNK);
            } else {
                const unsigned li = wv * 25 + (lane < 25 ? lane : 0), rw = (li * 13108u) >> 16, rem = li - rw * 5;
                glds4(rw * 1280 + (x8 & 1) * 640 + rem * 128, wo, lb + L_JUNK);
            }
        } else if constexpr (op.kind == OP_Q) issueQ(IC<op.a>{}, IC<op.b>{});
        else if constexpr (op.kind == OP_T) {
            Rf[op.a] = cat(trread(raR + op.a * 4096), trread(raR + op.a * 4096 + 2048));
        } else if constexpr (op.kind == OP_S) {
            unsigned isw1, ksr, cl;
            runit(IC<op.a>{}, isw1, ksr, cl);
            const unsigned T = 4 * cl + w, c8r = (T * 52u) >> 8;
            const unsigned dst = isw1 ? (unsigned)WS_W1R + (T * 20 + ksr) * 1024 : (unsigned)WS_W2R + ((c8r * 54 + ksr) * 5 + (T - 5 * c8r)) * 1024;
            st16_sc1((unsigned)(lane * 16), ws + dst, __builtin_bit_cast(u32x4, Rf[op.a]));
        } else if constexpr (op.a == 0) {
            u64 pc;
            asm volatile("s_getpc_b64 %0" : "=s"(pc));
            pc &= ~(u64)127;
            const unsigned cl_ = (rr * 4 + w) * 3 + ((lane & 3) < 3 ? (lane & 3) : 0);
            glds4((cl_ < CODE_LINES ? cl_ : CODE_LINES - 1) * 128, (const void*)pc, lb + L_JUNK);
        } else if constexpr (op.a == 2) {
            const char* p = lane < 24 ? ws + (size_t)lane * (1 << 20) : lane < 27 ? out + (size_t)(lane - 24) * 1300000 : lane < 30 ? w1 + (size_t)(lane - 27) * 1100000
                          : lane < 33 ? w2 + (size_t)(lane - 30) * 1100000 : wo;
            asm volatile("s_mov_b32 m0, %1\n\ts_nop 0\n\tglobal_load_lds_dword %0, off" ::"v"(p), "s"(lb + L_JUNK) : "memory");
        } else asm volatile("s_mov_b32 m0, %2\n\ts_nop 0\n\tglobal_load_lds_dword %0, %1 sc1" ::"v"((lane & 1) * 4), "s"(ws), "s"(lb + L_EPOCH) : "memory");
    };
    sfor<0, P1N>([&](auto N) { if constexpr (P1SEQ[N.v].ks < 0) p1issue(N); });
    unsigned rda[5];
    auto make_rda = [&] {
        int l2 = lane; asm volatile("" : "+v"(l2));
        const int g2 = l2 >> 4, n2 = l2 & 15;
        const unsigned red = lb + L_A + 20480 + w * 3360, trash = lb + L_TRASH + l2 * 16;
        rda[0] = red + (n2 * 20 + 4 * g2) * 4;
        rda[1] = (n2 == 0 || (n2 >= 8 && n2 < 12)) ? red + ((n2 == 0 ? 20 : 8 + n2) * 20 + 4 * g2) * 4 : trash;
        rda[2] = (n2 == 0 || n2 >= 12) ? red + (420 + (n2 == 0 ? 20 : 4 + n2) * 20 + 4 * g2) * 4 : trash;
        rda[3] = g2 < 2 ? red + (420 * g2 + n2 * 20 + 16) * 4 : trash;
        rda[4] = (g2 < 2 && (n2 == 0 || (g2 == 0 ? (n2 >= 8 && n2 < 12) : n2 >= 12))) ? red + (420 * g2 + (n2 == 0 ? 20 : (g2 == 0 ? 8 + n2 : 4 + n2)) * 20 + 16) * 4 : trash;
        asm volatile("" : "+v"(rda[0]), "+v"(rda[1]), "+v"(rda[2]), "+v"(rda[3]), "+v"(rda[4]));
        if (tid == 0) { *(LAS u64*)(lb + L_ZERO) = 0; *(LAS u64*)(lb + L_ZERO + 8) = 0; }
    };
    const int ro1 = 8 * g + q4 + 4 * (g & 1), ro2 = 8 * g + q4 + 4 * (1 - (g & 1));
    {
        unsigned vW1[2], vW2[2], vM1[2], vM2[2], vC1[2], vC2[2];
        const int mo = (p4 == 0 ? 32 : p4 == 1 ? 72 : p4 == 2 ? 112 : 152);
#pragma unroll
        for (int h = 0; h < 2; ++h) {
            vW1[h] = lb + L_A + h * 51200 + ro1 * 160 + 8 * p4; vW2[h] = lb + L_A + h * 51200 + ro2 * 160 + 8 * p4;
            vM1[h] = lb + L_A + h * 51200 + ro1 * 160 + mo; vM2[h] = lb + L_A + h * 51200 + ro2 * 160 + mo;
            vC1[h] = vW1[h] + 40 * w; vC2[h] = vW2[h] + 40 * w;
            asm volatile("" : "+v"(vW1[h]), "+v"(vW2[h]), "+v"(vM1[h]), "+v"(vM2[h]), "+v"(vC1[h]), "+v"(vC2[h]));
        }
        unsigned vD1 = vM1[0] + w * 5120, vD2 = vM2[0] + w * 5120;
        unsigned vX[2];
#pragma unroll
        for (int kl = 0; kl < 2; ++kl) vX[kl] = lb + L_B + w * 4096 + n * 128 + 16 * ((4 * kl + g) ^ (n >> 1));
#pragma unroll
        for (int t = 0; t < 5; ++t) acc[t][0] = acc[t][1] = z4;
        sq[0] = sq[1] = rs[0] = rs[1] = csA = csB = z4;
        s16x4 Wl[2][7], Wh[2][7];
        auto side = [&](auto KS, auto I) {
            constexpr int ks = KS.v, i = I.v, r = ks & 1, s = ks >> 1, kl = ks & 1, h = ks >= 10;
            constexpr unsigned ko = (ks - 10 * h) * 5120;
            if constexpr (i < 2) X[ks][i] = *(volatile LAS s16x8*)(vX[kl] + (s % 3) * 16384 + i * 2048);
            else if constexpr (i < 10) { constexpr int t = (i - 2) >> 1; if constexpr (i & 1) Wh[r][t] = trread(vW2[h] + ko + 40 * t); else Wl[r][t] = trread(vW1[h] + ko + 40 * t); }
            else if constexpr (i < 12) { if constexpr (i & 1) Wh[r][4] = trread(vM2[h] + ko); else Wl[r][4] = trread(vM1[h] + ko); }
            else if constexpr (i < 14) { if constexpr (i & 1) Wh[r][5] = trread(vC2[h] + ko); else Wl[r][5] = trread(vC1[h] + ko); }
            else {
                constexpr int u = ks >> 2;
                if constexpr (i & 1) { Wh[r][6] = trread(vD2 + (u % 3) * 20480); if constexpr (u == 2) { vD2 += 61440; asm volatile("" : "+v"(vD2)); } }
                else { Wl[r][6] = trread(vD1 + (u % 3) * 20480); if constexpr (u == 2) { vD1 += 61440; asm volatile("" : "+v"(vD1)); } }
            }
        };
        STAMP(48);
        WAITV(p1_wait(0));
        BARX();
        STAMP(49);
        sfor<0, 14>([&](auto I) { side(IC<0>{}, I); });
        SB();
        sfor<0, 20>([&](auto KS) {
            constexpr int ks = KS.v;
            if constexpr ((ks & 1) && ks < 19) {
                constexpr int s = (ks + 1) >> 1;
                WAITV(p1_wait(s));
                if constexpr (s >= 8) BAR();
                else if constexpr ((s & 1) == 0) BARX();
                SB();
            }
            if constexpr (ks == 4) STAMP(50);
            if constexpr (ks == 8) STAMP(51);
            if constexpr (ks == 12) STAMP(52);
            if constexpr (ks == 16) STAMP(53);
            constexpr int r = ks & 1;
            constexpr int nm = ((ks & 3) == 3) ? 16 : 15;
            constexpr int nside = ks < 19 ? (((ks + 1) & 3) == 3 ? 16 : 14) : 0;
            sfor<0, 16>([&](auto I) {
                constexpr int i = I.v;
                if constexpr (i < nm) {
                    if constexpr (i < 10) { constexpr int t = i >> 1, m = i & 1; acc[t][m] = mfma(X[ks][m], cat(Wl[r][t], Wh[r][t]), acc[t][m]); }
                    else if constexpr (i == 10) csA = mfma(ones, cat(Wl[r][5], Wh[r][5]), csA);
                    else if constexpr (i < 13) sq[i - 11] = mfma(X[ks][i - 11], X[ks][i - 11], sq[i - 11]);
                    else if constexpr (i < 15) rs[i - 13] = mfma(X[ks][i - 13], ones, rs[i - 13]);
                    else csB = mfma(ones, cat(Wl[r][6], Wh[r][6]), csB);
                }
                if constexpr (i < nside) side(IC<ks + 1>{}, I);
                sfor<0, P1N>([&](auto N) { if constexpr (P1SEQ[N.v].ks == ks && P1SEQ[N.v].slot == i) p1issue(N); });
                if constexpr (ks == 3 && i == 12) make_rda();
                if constexpr (ks == 5 && i == 12) make_voffQ();
                SB();
            });
        });
    }
    asm volatile("" : "+v"(csA), "+v"(csB));
    const u64 tag = *(volatile LAS u64*)(lb + L_EPOCH) + 1;
    STAMP(1);

    if (g == 0) { ((LAS float*)(lb + L_CSKV))[16 * w + n] = csA[0]; ((LAS float*)(lb + L_CS4))[16 * w + n] = csB[0]; }
    f32x4 lnA[2], lnB[2];
#pragma unroll
    for (int m = 0; m < 2; ++m) {
        f32x4 mean4, rstd4;
#pragma unroll
        for (int r = 0; r < 4; ++r) {
            const float s2 = __uint_as_float(__builtin_amdgcn_ds_bpermute(4 * (20 * g + r), __float_as_uint(sq[m][r])));
            const float mean = rs[m][r] * (1.f / 640.f);
            const float rstd = __builtin_amdgcn_rsqf(fmaxf(s2 * (1.f / 640.f) - mean * mean, 0.f) + 1e-5f);
            mean4[r] = mean; rstd4[r] = rstd;
            lnA[m][r] = rstd; lnB[m][r] = -mean * rstd;
        }
        *(LAS f32x4*)(lb + L_MEAN + (32 * w + 16 * m + 4 * g) * 4) = mean4;
        *(LAS f32x4*)(lb + L_RSTD + (32 * w + 16 * m + 4 * g) * 4) = rstd4;
    }
    BAR();
    STAMP(31);
    {
        float cs[5];
#pragma unroll
        for (int t = 0; t < 4; ++t) cs[t] = ((LAS float*)(lb + L_CSKV))[16 * t + n];
        {
            LAS float* c4 = (LAS float*)(lb + L_CS4);
            cs[4] = (c4[n] + c4[16 + n]) + (c4[32 + n] + c4[48 + n]);
        }
        s16x8 F[4], MA, MB;
        sfor<0, 5>([&](auto TT) {
            constexpr int t = TT.v;
            issueQ(IC<2>{}, TT);
            f32x4 v[2], vb_[2];
#pragma unroll
            for (int m = 0; m < 2; ++m)
#pragma unroll
                for (int r = 0; r < 4; ++r) {
                    const float y = lnA[m][r] * acc[t][m][r] + lnB[m][r] * cs[t];
                    if (t < 2) v[m][r] = phi(y);
                    else if (t < 4) v[m][r] = y;
                    else { v[m][r] = n < 8 ? phi(y) : y; vb_[m][r] = n < 8 ? 1.f : y; }
                }
            if constexpr (t < 4) F[t] = pack8(v[0], v[1]);
            else { MA = pack8(v[0], v[1]); MB = pack8(vb_[0], vb_[1]); }
            SB();
        });
        STAMP(33);
        f32x4 P1 = mfma(F[0], F[2], z4), P2 = mfma(F[0], MB, z4), P3 = mfma(MA, F[2], z4), P4 = mfma(MA, MB, z4);
        f32x4 P5 = mfma(F[1], F[3], z4), P6 = mfma(F[1], MB, z4), P7 = mfma(MA, F[3], z4);
        *(LAS f32x4*)(rda[0]) = P1;
        *(LAS f32x4*)(rda[0] + 1680) = P5;
        *(LAS f32x4*)(rda[1]) = P2;
        *(LAS f32x4*)(rda[2]) = P6;
        *(LAS f32x4*)(rda[3]) = (g == 0 ? P3 : P7);
        *(LAS f32x4*)(rda[4]) = P4;
    }
    STAMP(34);
    BAR();
    STAMP(35);
    if (tid < 210) {
        LAS char* red = (LAS char*)(lb + L_A + 20480 + tid * 16);
        f32x4 s = (*(LAS f32x4*)(red) + *(LAS f32x4*)(red + 3360)) + (*(LAS f32x4*)(red + 6720) + *(LAS f32x4*)(red + 10080));
        st16_sc1((unsigned)(tid * 16), ws + WS_KVP + (size_t)(j1 * 16 + i1) * 3360, __builtin_bit_cast(u32x4, s));
    }
    STAMP(36);
    drain_bar();
    if (tid < 16) flags_store(ws, 0, tid * 16 + j1, i1, tag);
    if (tid == 16) __hip_atomic_store((GAS u64*)(ws + WS_GFLAG) + b, tag, RLX_AGENT);
    STAMP(2);

    unsigned voff160[5];
    {
        int lane2 = lane; asm volatile("" : "+v"(lane2));
#pragma unroll
        for (int u = 0; u < 5; ++u) { int q = 64 * u + lane2, row = q / 10, s = q - row * 10; voff160[u] = row * 1280 + 160 * c8 + 16 * s; }
    }
    s16x8 kp[16];
    u64 gf[4];
    f32x4 aq[3][2];
    unsigned oa[12];
    const char* worun = wo + (size_t)w * (32 * 1280);
    {
        const unsigned vQ1 = lb + L_B + ro1 * 80 + 8 * p4, vQ2 = lb + L_B + ro2 * 80 + 8 * p4;
        const int to = (p4 == 0 ? 32 : p4 == 1 ? 72 : p4 == 2 ? 32 : 72);
        const unsigned vT1 = lb + L_B + ro1 * 80 + to, vT2 = lb + L_B + ro2 * 80 + to;
        const int to2 = (p4 & 1) ? 72 : 32;
        const unsigned vC1 = w < 2 ? vQ1 + 40 * w : vT1, vC2 = w < 2 ? vQ2 + 40 * w : vT2;
        (void)to2;
        f32x4 cq = z4;
#pragma unroll
        for (int t = 0; t < 3; ++t) aq[t][0] = aq[t][1] = z4;
        s16x4 Wl[2][4], Wh[2][4];
        auto side = [&](auto KS, auto I) {
            constexpr int ks = KS.v, i = I.v, r = ks & 1, t = i >> 1;
            constexpr unsigned ko = wq_pos(ks >> 1) + (ks & 1) * 2560;
            if constexpr (t < 2) { if constexpr (i & 1) Wh[r][t] = trread(vQ2 + ko + 40 * t); else Wl[r][t] = trread(vQ1 + ko + 40 * t); }
            else if constexpr (t == 2) { if constexpr (i & 1) Wh[r][2] = trread(vT2 + ko); else Wl[r][2] = trread(vT1 + ko); }
            else { if constexpr (i & 1) Wh[r][3] = trread(vC2 + ko); else Wl[r][3] = trread(vC1 + ko); }
        };
        sfor<0, 8>([&](auto I) { side(IC<0>{}, I); });
        SB();
        const char* kb = ws + WS_KVP + (size_t)j1 * 16 * 3360;
        const unsigned kvo = (tid < 210 ? tid : 209) * 16;
        {
            const unsigned kt = lb + L_KVT, zz = lb + L_ZERO;
            auto ka = [&](int head, int e, int c) { return kt + (head * 420 + e * 20 + 4 * c) * 2; };
            oa[0] = ka(0, n, g);  oa[1] = g == 0 ? ka(0, n, 4) : zz;
            oa[2] = ka(1, n, g);  oa[3] = g == 1 ? ka(1, n, 4) : zz;
            oa[4] = ka(0, 20, g); oa[5] = g == 0 ? ka(0, 20, 4) : zz;
            oa[6] = ka(1, 20, g); oa[7] = g == 1 ? ka(1, 20, 4) : zz;
            oa[8] = n < 4 ? ka(0, 16 + n, g) : zz;                oa[9] = (n < 4 && g == 0) ? ka(0, 16 + n, 4) : zz;
            oa[10] = (n >= 4 && n < 8) ? ka(1, 12 + n, g) : zz;   oa[11] = (n >= 4 && n < 8 && g == 1) ? ka(1, 12 + n, 4) : zz;
            asm volatile("" : "+v"(oa[0]), "+v"(oa[1]), "+v"(oa[2]), "+v"(oa[3]), "+v"(oa[4]), "+v"(oa[5]), "+v"(oa[6]), "+v"(oa[7]),
                         "+v"(oa[8]), "+v"(oa[9]), "+v"(oa[10]), "+v"(oa[11]));
        }
        sfor<0, 20>([&](auto KS) {
            constexpr int ks = KS.v, r = ks & 1;
            if constexpr (ks == 12) { STAMP(3); await<63>(ws, 0, i1 * 16 + j1, tag, w, lane); STAMP(4); SB(); }
            sfor<0, 8>([&](auto I) {
                constexpr int i = I.v;
                if constexpr (i < 6) { constexpr int t = i >> 1, m = i & 1; aq[t][m] = mfma(cat(Wl[r][t], Wh[r][t]), X[ks][m], aq[t][m]); }
                else if constexpr (i == 6) cq = mfma(ones, cat(Wl[r][3], Wh[r][3]), cq);
                if constexpr (ks < 19) side(IC<ks + 1>{}, I);
                if constexpr ((i == 2 || i == 6) && ks < 9) {
                    constexpr int pc_ = 2 * ks + (i == 6), tt = pc_ / 5, u = pc_ % 5;
                    glds16<tt * 20480 + u * 1024>(voff160[u], worun, ldsWw);
                    if constexpr (u == 4) { worun += 128 * 1280; OPAQUE_S(worun); }
                }
                if constexpr (ks == 18 && i < 4) ld8_sc1(gf[i], (unsigned)(lane * 8 + i * 512), ws + WS_GFLAG);
                if constexpr (ks >= 12 && ks < 18 && (i == 1 || i == 4 || i == 7) && 3 * (ks - 12) + i / 3 < 16) {
                    constexpr int pi = 3 * (ks - 12) + i / 3;
                    ld16_sc1(kp[pi], kvo + pi * 3360, kb);
                }
                SB();
            });
        });
        STAMP(37);
        { const unsigned hi = pk2(cq[0], cq[0]); const float lo = cq[0] - bflo(hi);
          if (g == 0 && w < 3) *(LAS u32x2*)(lb + L_CSQ + w * 128 + n * 8) = u32x2{hi, pk2(lo, lo)}; }
    }

    bool gok;
    {
        asm volatile("s_waitcnt vmcnt(0)" : "+v"(kp[0]), "+v"(kp[1]), "+v"(kp[2]), "+v"(kp[3]), "+v"(kp[4]), "+v"(kp[5]), "+v"(kp[6]), "+v"(kp[7]),
                     "+v"(kp[8]), "+v"(kp[9]), "+v"(kp[10]), "+v"(kp[11]), "+v"(kp[12]), "+v"(kp[13]), "+v"(kp[14]), "+v"(kp[15]),
                     "+v"(gf[0]), "+v"(gf[1]), "+v"(gf[2]), "+v"(gf[3])::"memory");
        gok = __all(gf[0] == tag && gf[1] == tag && gf[2] == tag && gf[3] == tag);
        f32x4 s0 = __builtin_bit_cast(f32x4, kp[0]), s1 = __builtin_bit_cast(f32x4, kp[1]);
#pragma unroll
        for (int i = 2; i < 16; i += 2) { s0 += __builtin_bit_cast(f32x4, kp[i]); s1 += __builtin_bit_cast(f32x4, kp[i + 1]); }
        if (tid < 210) *(LAS u32x2*)(lb + L_KVT + tid * 8) = pack4(s0 + s1);
    }
    STAMP(38);
    BAR();
    STAMP(39);
    if (w < 2) sfor<0, 5>([&](auto U) { glds16<U.v * 1024>(voff160[U.v], x + (size_t)(8 * i2 + 4 * half + 2 * w) * 16 * 1280, lb + L_XRES + w * 5120); });
    {
        auto rd2 = [&](unsigned a0, unsigned a1) { const u32x2 lo = *(LAS u32x2*)a0, hi = *(LAS u32x2*)a1; return __builtin_bit_cast(s16x8, u32x4{lo[0], lo[1], hi[0], hi[1]}); };
        s16x8 Fa[2], Fb[2];
        {
            const unsigned csa = g == 0 ? lb + L_CSQ + n * 8 : lb + L_ZERO;
            s16x8 cf[3];
#pragma unroll
            for (int t = 0; t < 3; ++t) { const u32x2 c = *(LAS u32x2*)(csa + (g == 0 ? t * 128 : 0)); cf[t] = __builtin_bit_cast(s16x8, u32x4{c[0], c[1], 0u, 0u}); }
#pragma unroll
            for (int m = 0; m < 2; ++m) {
                const float nm = -((LAS float*)(lb + L_MEAN))[32 * w + 16 * m + n], rstd = ((LAS float*)(lb + L_RSTD))[32 * w + 16 * m + n];
                const unsigned hi = pk2(nm, nm);
                const unsigned pr = g == 0 ? ((hi & 0xffffu) | (pk2(nm - bflo(hi), 0.f) << 16)) : 0u;
                const s16x8 hm = __builtin_bit_cast(s16x8, u32x4{pr, pr, 0u, 0u});
                f32x4 v[3];
#pragma unroll
                for (int t = 0; t < 3; ++t) {
                    const f32x4 y = mfma(cf[t], hm, aq[t][m]) * rstd;
#pragma unroll
                    for (int r = 0; r < 4; ++r) v[t][r] = phi(y[r]);
                }
                const s16x8 q2 = pack8(v[0], v[2]), q3 = pack8(v[1], v[2]);
                u32x4 u2 = __builtin_bit_cast(u32x4, q2), u3 = __builtin_bit_cast(u32x4, q3);
                if (g >= 2) { u2[2] = u2[3] = 0u; u3[2] = u3[3] = 0u; }
                Fa[m] = __builtin_bit_cast(s16x8, u2); Fb[m] = __builtin_bit_cast(s16x8, u3);
            }
        }
        const s16x8 Ama = rd2(oa[0], oa[1]), Amb = rd2(oa[2], oa[3]), Aza = rd2(oa[4], oa[5]), Azb = rd2(oa[6], oa[7]), Ata = rd2(oa[8], oa[9]), Atb = rd2(oa[10], oa[11]);
#pragma unroll
        for (int m = 0; m < 2; ++m) {
            f32x4 Da = mfma(Ama, Fa[m], z4), Db = mfma(Amb, Fb[m], z4);
            f32x4 Dt = mfma(Atb, Fb[m], mfma(Ata, Fa[m], z4));
            f32x4 Dza = mfma(Aza, Fa[m], z4), Dzb = mfma(Azb, Fb[m], z4);
            const float ra = __builtin_amdgcn_rcpf(Dza[0] + 1e-6f), rb = __builtin_amdgcn_rcpf(Dzb[0] + 1e-6f);
            const int mt = 8 * i1 + 2 * w + m;
            s16x8 of = pack8(Da * ra, Db * rb);
            st16_sc1((unsigned)((mt * 20 + j1) * 1024 + lane * 16), ws + WS_O, __builtin_bit_cast(u32x4, of));
            u32x2 tl = pack4(Dt * (g == 0 ? ra : rb));
            const int t = j1 >> 1;
            if (g < 2) st8_sc1((unsigned)((mt * 20 + 16 + (t >> 1)) * 1024 + (n + 16 * (2 * (j1 & 1) + g)) * 16 + (t & 1) * 8), ws + WS_O, tl);
        }
    }
    STAMP(40);
    drain_bar();
    if (tid < 16) flags_store(ws, 1, i1 * 16 + tid, j1, tag);
    STAMP(5);

    const int T0 = j2 < 12 ? 7 * j2 : 84 + 6 * (j2 - 12);
    const int odd = (j2 < 12) ? (j2 & 1) : 0;
    const int Tp0 = T0 + odd;
    const int Tsg = j2 < 12 ? (odd ? T0 : T0 + 6) : Tp0;
    const unsigned vlan16 = lane * 16;
    const int ksb = w < 2 ? 14 * w : 28 + 13 * (w - 2);
    const int nks = w < 2 ? 14 : 13;
    s16x8 V[14][5];
    const char* w2run = ws + WS_W2R + (size_t)(c8 * 54 + ksb) * 5120;
    const int step13 = nks == 14 ? 5120 : 0;
#define ISSUE_V(it, t) do { if constexpr ((it) < 8) ld16a_sc1(V[it][t], vlan16 + (t) * 1024, w2run); else ld16_sc1(V[it][t], vlan16 + (t) * 1024, w2run); \
            if ((t) == 4) { w2run += ((it) == 12 ? step13 : 5120); OPAQUE_S(w2run); } } while (0)
    if (w == 3 && gok) {
#pragma unroll
        for (int k = 0; k < 5; ++k) {
            int li = k * 64 + lane; li = li < 280 ? li : 279; li += rr * 280; li = li < ((x8 & 1) ? 8320 : 8960) ? li : 8319;
            glds4((unsigned)(li * 128), ws + WS_W1R + (x8 & 1) * 56 * 20480, lb + L_JUNK);
        }
    }
    sfor<18, 25>([&](auto P) {
        constexpr int tt = P.v / 5, u = P.v % 5;
        glds16<tt * 20480 + u * 1024>(voff160[u], worun, ldsWw);
        if constexpr (u == 4) { worun += 128 * 1280; OPAQUE_S(worun); }
    });
    const int mtW = 8 * i2 + 4 * half + w;
    STAMP(45);
    await<0>(ws, 1, i2 * 16 + jw, tag, w, lane);
    STAMP(6);
    {
        s16x8 O[20];
        unsigned orun = (unsigned)(mtW * 20 * 1024 + lane * 16);
#define ISSUE_O(i) do { ld16_sc1(O[i], orun, ws + WS_O); orun += 1024u; asm volatile("" : "+v"(orun)); } while (0)
        sfor<0, 8>([&](auto I) { ISSUE_O(I.v); });
        unsigned vA = lb + L_A + (4 * g + q4) * 160 + 8 * p4, vA2 = vA + 8 * 6400;
        const unsigned vB = lb + L_A + (20 * g + 16 + q4) * 160 + 8 * p4;
        unsigned vB2 = vB + 2 * 25600;
        asm volatile("" : "+v"(vA), "+v"(vA2), "+v"(vB2));
        f32x4 a[5];
#pragma unroll
        for (int t = 0; t < 5; ++t) a[t] = z4;
        s16x4 Wl[3][5], Wh[3][5];
        auto side = [&](auto I, auto J) {
            constexpr int i = I.v, j = J.v, r = i % 3, t = j >> 1;
            if constexpr (i < 16) {
                const unsigned a_ = (i < 8 ? vA : vA2) + (i & 7) * 6400 + 32 * t;
                if constexpr (j & 1) Wh[r][t] = trread(a_ + 3200); else Wl[r][t] = trread(a_);
            } else {
                const unsigned a_ = (i < 18 ? vB : vB2) + (i & 1) * 25600 + 32 * t;
                if constexpr (j & 1) Wh[r][t] = trread(a_ + 12800); else Wl[r][t] = trread(a_);
            }
        };
        sfor<0, 2>([&](auto I) { sfor<0, 10>([&](auto J) { side(I, J); }); });
        SB();
        sfor<0, 20>([&](auto I) {
            constexpr int i = I.v;
            constexpr int ny = ((i + 7 < 19 ? i + 7 : 19) - i) + (i > 12 ? (i < 17 ? 2 * (i - 12) : 10) : 0) + (i == 18 ? 1 : i == 19 ? 2 : 0);
            asm volatile("s_waitcnt vmcnt(%1)" : "+v"(O[i]) : "i"(ny) : "memory");
            SB();
            sfor<0, 5>([&](auto T) {
                constexpr int t = T.v;
                a[t] = mfma(cat(Wl[i % 3][t], Wh[i % 3][t]), O[i], a[t]);
                if constexpr (i + 2 < 20) { side(IC<i + 2>{}, IC<2 * t>{}); side(IC<i + 2>{}, IC<2 * t + 1>{}); }
                if constexpr (t == 1 && i + 8 < 20) ISSUE_O(i + 8);
                if constexpr ((i == 17 || i == 18) && t == 1) {
                    int li = w * 68 + (i == 17 ? lane : 64 + (lane < 4 ? lane : 0)); li = li < 270 ? li : 269;
                    glds4((unsigned)((rr * 270 + li) * 128), ws + WS_W2R + (size_t)(x8 & 1) * 4 * 54 * 5120, lb + L_JUNK);
                }
                if constexpr (i >= 12 && i < 17 && (t == 1 || t == 3)) {
                    constexpr int bg = 2 * (i - 12) + (t == 3);
                    const int Tg = bg < 5 ? Tp0 + 5 : Tsg;
                    glds16c<(5 + bg / 5) * 20480 + (bg % 5) * 4096>(vlan16, ws + WS_W1R + (size_t)(Tg * 20 + (bg % 5) * 4 + w) * 1024, lb + w * 1024);
                }
                SB();
            });
        });
        STAMP(28);
        float su = 0.f, s2 = 0.f;
        u32x2 hpk[5];
#pragma unroll
        for (int t = 0; t < 5; ++t) {
            const u32x2 xr = *(LAS u32x2*)(lb + L_XRES + (16 * w + n) * 160 + 32 * t + 8 * g);
            const f32x4 hv = f32x4{bflo(xr[0]), bfhi(xr[0]), bflo(xr[1]), bfhi(xr[1])} + a[t];
            hpk[t] = pack4(hv);
            *(LAS u32x2*)(lb + L_XRES + (16 * w + n) * 160 + 32 * t + 8 * g) = hpk[t];
#pragma unroll
            for (int r = 0; r < 4; ++r) { su += hv[r]; s2 += hv[r] * hv[r]; }
        }
        const unsigned hb = (unsigned)((mtW * 20 + ((5 * c8) >> 1)) * 1024 + lane * 16);
        const int od = c8 & 1;
        st8_sc1(hb + (od ? 8 : 2048), ws + WS_H, od ? hpk[0] : hpk[4]);
        { const u32x2 p0 = od ? hpk[1] : hpk[0], p1 = od ? hpk[2] : hpk[1]; st16_sc1(hb + (od ? 1024 : 0), ws + WS_H, u32x4{p0[0], p0[1], p1[0], p1[1]}); }
        { const u32x2 p0 = od ? hpk[3] : hpk[2], p1 = od ? hpk[4] : hpk[3]; st16_sc1(hb + (od ? 2048 : 1024), ws + WS_H, u32x4{p0[0], p0[1], p1[0], p1[1]}); }
        su = add_xor32(add_xor16(su)); s2 = add_xor32(add_xor16(s2));
        if (g == 0) { u32x2 st = {__float_as_uint(su), __float_as_uint(s2)}; st8_sc1((unsigned)((mtW * 16 + n) * 64 + c8 * 8), ws + WS_STAT2, st); }
    }
    STAMP(42);
    drain_bar();
    if (tid < 16) flags_store(ws, 2, i2 * 16 + tid, jw, tag);
    if (b == 0 && tid == 0) __hip_atomic_store((GAS u64*)ws, tag, RLX_AGENT);
    STAMP(7);

    {
        const char* s1 = ws + WS_W1R + (size_t)(Tp0 * 20 + w) * 1024;
        const unsigned l1 = lb + L_A + w * 1024;
        sfor<0, 25>([&](auto U) { glds16c<U.v * 4096>(vlan16, s1, l1); s1 += 4096; OPAQUE_S(s1); });
    }
    STAMP(43);
    await<0>(ws, 2, i2 * 16 + j2, tag, w, lane);
    STAMP(8);
    {
        s16x8 H[20][2];
        const int mt0 = 8 * i2 + 2 * w;
        s16x8 stv[2];
#pragma unroll
        for (int m = 0; m < 2; ++m) ld16_sc1(stv[m], (unsigned)(((mt0 + m) * 16 + n) * 64 + g * 16), ws + WS_STAT2);
        unsigned hrun = (unsigned)(mt0 * 20 * 1024 + lane * 16);
#define ISSUE_H(ks) do { ld16_sc1(H[ks][0], hrun, ws + WS_H); ld16_sc1(H[ks][1], hrun + 20 * 1024, ws + WS_H); hrun += 1024; asm volatile("" : "+v"(hrun)); } while (0)
        unsigned vF0 = lb + vlan16, vF1 = lb + 61440 + vlan16, vF2 = lb + 122880 + vlan16;
        asm volatile("" : "+v"(vF0), "+v"(vF1), "+v"(vF2));
        auto wread = [&](auto TT, auto KS) -> s16x8 {
            constexpr int t = TT.v, ks = KS.v;
            return *(volatile LAS s16x8*)((t < 3 ? vF0 : t < 6 ? vF1 : vF2) + (t % 3) * 20480 + ks * 1024);
        };
        {
            f32x4 c0 = z4, c1 = z4;
            const unsigned ta = lb + vlan16 + w * 20480, tb = lb + vlan16 + (w < 3 ? w + 4 : w) * 20480;
            s16x8 Ca[4], Cb[4];
            sfor<0, 3>([&](auto K) { Ca[K.v] = *(volatile LAS s16x8*)(ta + K.v * 1024); Cb[K.v] = *(volatile LAS s16x8*)(tb + K.v * 1024); });
            SB();
            sfor<0, 20>([&](auto K) {
                constexpr int k = K.v;
                c0 = mfma(ones, Ca[k & 3], c0);
                if constexpr (k + 3 < 20) Ca[(k + 3) & 3] = *(volatile LAS s16x8*)(ta + (k + 3) * 1024);
                SB();
                c1 = mfma(ones, Cb[k & 3], c1);
                if constexpr (k + 3 < 20) Cb[(k + 3) & 3] = *(volatile LAS s16x8*)(tb + (k + 3) * 1024);
                if constexpr ((k & 1) == 0 && k < 16) ISSUE_H(k >> 1);
                SB();
            });
            auto split = [&](float c) { const unsigned hi = pk2(c, c); const float lo = c - bflo(hi); u32x2 r = {hi, pk2(lo, lo)}; return r; };
            if (g == 0) {
                *(LAS u32x2*)(lb + L_CS1 + w * 128 + n * 8) = split(c0[0]);
                if (w < 3) *(LAS u32x2*)(lb + L_CS1 + (w + 4) * 128 + n * 8) = split(c1[0]);
            }
            if (tid < 2) *(LAS u64*)(lb + L_CS1 + 896 + tid * 8) = 0;
        }
        STAMP(19);
        asm volatile("s_waitcnt vmcnt(16)" : "+v"(stv[0]), "+v"(stv[1])::"memory");
        s16x8 Hm[2];
#pragma unroll
        for (int m = 0; m < 2; ++m) {
            f32x4 s = __builtin_bit_cast(f32x4, stv[m]);
            const float nm = add_xor32(add_xor16(s[0] + s[2])) * (-1.f / 640.f);
            const unsigned hi = pk2(nm, nm);
            const unsigned pr = g == 0 ? ((hi & 0xffffu) | (pk2(nm - bflo(hi), 0.f) << 16)) : 0u;
            Hm[m] = __builtin_bit_cast(s16x8, u32x4{pr, pr, 0u, 0u});
        }
        s16x8 Wf1[3][4];
        sfor<0, 2>([&](auto KS) { sfor<0, 4>([&](auto TT) { Wf1[KS.v][TT.v] = wread(TT, KS); }); });
        __builtin_amdgcn_s_waitcnt(0xC87F);
        BARX();
        const unsigned csa = lb + L_CS1 + (g == 0 ? n * 8 : 896);
        auto csfrag = [&](int t) { const u32x2 c = *(LAS u32x2*)(csa + (g == 0 ? t * 128 : 0)); return __builtin_bit_cast(s16x8, u32x4{c[0], c[1], 0u, 0u}); };
        auto relu2 = [&](unsigned p) { typedef short s2_ __attribute__((ext_vector_type(2))); const s2_ z = {0, 0};
                                       return __builtin_bit_cast(unsigned, __builtin_elementwise_max(__builtin_bit_cast(s2_, p), z)); };
        auto fin8 = [&](f32x4 x0, f32x4 x1) { return u32x4{relu2(pk2(x0[0], x0[1])), relu2(pk2(x0[2], x0[3])), relu2(pk2(x1[0], x1[1])), relu2(pk2(x1[2], x1[3]))}; };
        f32x4 a1[4][2];
        sfor<0, 4>([&](auto TT) { const s16x8 cf = csfrag(TT.v); a1[TT.v][0] = mfma(cf, Hm[0], z4); a1[TT.v][1] = mfma(cf, Hm[1], z4); });
        {
            auto& Wf = Wf1;
            SB();
            sfor<0, 20>([&](auto KS) {
                constexpr int ks = KS.v;
                constexpr int ny = 2 * ((ks + 7 < 19 ? ks + 7 : 19) - ks) + (ks > 12 ? (ks < 17 ? 2 * (ks - 12) : 10) : 0);
                asm volatile("s_waitcnt vmcnt(%2)" : "+v"(H[ks][0]), "+v"(H[ks][1]) : "i"(ny) : "memory");
                SB();
                sfor<0, 8>([&](auto I) {
                    constexpr int i = I.v, t = i >> 1, m = i & 1;
                    a1[t][m] = mfma(Wf[ks % 3][t], H[ks][m], a1[t][m]);
                    if constexpr (ks + 2 < 20 && (i & 1) == 0) Wf[(ks + 2) % 3][t] = wread(IC<t>{}, IC<ks + 2>{});
                    if constexpr (i == 3 && ks + 8 < 20) ISSUE_H(ks + 8);
                    if constexpr (ks >= 12 && ks < 17 && (i == 1 || i == 5)) { constexpr int pv = 2 * (ks - 12) + (i == 5); ISSUE_V(pv / 5, pv % 5); }
                    SB();
                });
            });
        }
        STAMP(20);
        f32x4 a2[3][2];
        sfor<0, 3>([&](auto TT) { const s16x8 cf = csfrag(4 + TT.v); a2[TT.v][0] = mfma(cf, Hm[0], z4); a2[TT.v][1] = mfma(cf, Hm[1], z4); });
        {
            s16x8 Wf[3][3];
            sfor<0, 2>([&](auto KS) { sfor<0, 3>([&](auto TT) { Wf[KS.v][TT.v] = wread(IC<4 + TT.v>{}, KS); }); });
            u32x2 e0, e1;
            sfor<0, 20>([&](auto KS) {
                constexpr int ks = KS.v;
                sfor<0, 6>([&](auto I) {
                    constexpr int i = I.v, t = i >> 1, m = i & 1;
                    a2[t][m] = mfma(Wf[ks % 3][t], H[ks][m], a2[t][m]);
                    if constexpr (ks + 2 < 20 && (i & 1) == 0) Wf[(ks + 2) % 3][t] = wread(IC<4 + t>{}, IC<ks + 2>{});
                    if constexpr (i == 3 || (i == 5 && (ks & 1) == 0)) { constexpr int pv = 10 + (ks >> 1) * 3 + ((ks & 1) ? 2 : (i == 5 ? 1 : 0)); ISSUE_V(pv / 5, pv % 5); }
                    if constexpr (ks >= 1 && ks < 17 && ((ks - 1) & 3) < 3) {
                        constexpr int u = (ks - 1) >> 2, ph = (ks - 1) & 3, P = u >> 1, mm = u & 1;
                        if constexpr (ph == 0 && i == 1) e0 = u32x2{relu2(pk2(a1[2 * P][mm][0], a1[2 * P][mm][1])), relu2(pk2(a1[2 * P][mm][2], a1[2 * P][mm][3]))};
                        if constexpr (ph == 1 && i == 1) e1 = u32x2{relu2(pk2(a1[2 * P + 1][mm][0], a1[2 * P + 1][mm][1])), relu2(pk2(a1[2 * P + 1][mm][2], a1[2 * P + 1][mm][3]))};
                        if constexpr (ph == 2 && i == 1)
                            st16_sc1((unsigned)(((mt0 + mm) * 54 + (Tp0 >> 1) + P) * 1024 + lane * 16), ws + WS_H1, u32x4{e0[0], e0[1], e1[0], e1[1]});
                    }
                    SB();
                });
            });
        }
        STAMP(22);
#pragma unroll
        for (int m = 0; m < 2; ++m) {
            st16_sc1((unsigned)(((mt0 + m) * 54 + (Tp0 >> 1) + 2) * 1024 + lane * 16), ws + WS_H1, fin8(a2[0][m], a2[1][m]));
            if (j2 < 12) st8_sc1((unsigned)(((mt0 + m) * 54 + (Tsg >> 1)) * 1024 + lane * 16 + (Tsg & 1) * 8), ws + WS_H1,
                                 u32x2{relu2(pk2(a2[2][m][0], a2[2][m][1])), relu2(pk2(a2[2][m][2], a2[2][m][3]))});
        }
        STAMP(24);
        drain_bar();
        if (tid < 16) flags_store(ws, 3, i2 * 16 + tid, j2, tag);
        STAMP(9);

        sfor<40, 60>([&](auto U) { ISSUE_V(U.v / 5, U.v % 5); });
        STAMP(44);
        await<63>(ws, 3, i2 * 16 + jw, tag, w, lane);
        STAMP(10);
        unsigned arun = (unsigned)(((8 * i2 + 4 * half) * 54 + ksb) * 1024 + lane * 16);
        const unsigned astep13 = nks == 14 ? 1024u : 0u;
        s16x8 A[4][4];
        unsigned aoff[4];
#pragma unroll
        for (int m = 0; m < 4; ++m) aoff[m] = ((w + m) & 3) * 54 * 1024;
        s16x8 st2v;
        ld16_sc1(st2v, (unsigned)((mtW * 16 + n) * 64 + g * 16), ws + WS_STAT2);
#define ISSUE_A(it, m) do { ld16_sc1(A[(it) & 3][m], arun + aoff[m], ws + WS_H1); \
            if ((m) == 3) { arun += ((it) == 12 ? astep13 : 1024u); asm volatile("" : "+v"(arun)); } } while (0)
        sfor<0, 3>([&](auto IT) { sfor<0, 4>([&](auto M) { ISSUE_A(IT.v, M.v); }); });
        f32x4 a[4][5];
#pragma unroll
        for (int m = 0; m < 4; ++m)
#pragma unroll
            for (int t = 0; t < 5; ++t) a[m][t] = z4;
        sfor<0, 14>([&](auto IT) {
            constexpr int it = IT.v;
            constexpr int ny = [] {
                int seq[200] = {}, n_ = 0;
                for (int a_ = 0; a_ < 3; ++a_) for (int m = 0; m < 4; ++m) seq[n_++] = 1000 + a_;
                for (int j = 0; j < it; ++j)
                    for (int i = 0; i < 20; ++i) {
                        if ((i & 3) == 1 && i < 16 && j + 3 < 14) seq[n_++] = 1000 + j + 3;
                        if ((i == 3 || i == 11) && j < 5) seq[n_++] = 2000 + (60 + 2 * j + (i == 11)) / 5;
                    }
                int need = 0;
                for (int q = 0; q < n_; ++q) if (seq[q] == 1000 + it || seq[q] == 2000 + it) need = q + 1;
                return n_ - need;
            }();
            if constexpr (it == 1) STAMP(25);
            if constexpr (it == 5) STAMP(26);
            if constexpr (it == 9) STAMP(27);
            if constexpr (it < 8)
                asm volatile("s_waitcnt vmcnt(%9)" : "+v"(A[it & 3][0]), "+v"(A[it & 3][1]), "+v"(A[it & 3][2]), "+v"(A[it & 3][3]),
                             "+a"(V[it][0]), "+a"(V[it][1]), "+a"(V[it][2]), "+a"(V[it][3]), "+a"(V[it][4]) : "i"(ny) : "memory");
            else
                asm volatile("s_waitcnt vmcnt(%9)" : "+v"(A[it & 3][0]), "+v"(A[it & 3][1]), "+v"(A[it & 3][2]), "+v"(A[it & 3][3]),
                             "+v"(V[it][0]), "+v"(V[it][1]), "+v"(V[it][2]), "+v"(V[it][3]), "+v"(V[it][4]) : "i"(ny) : "memory");
            SB();
            if (it < 13 || nks == 14) {
                sfor<0, 20>([&](auto I) {
                    constexpr int i = I.v, t = i >> 2, m = i & 3;
                    a[m][t] = mfma(V[it][t], A[it & 3][m], a[m][t]);
                    if constexpr ((i & 3) == 1 && i < 16 && it + 3 < 14) ISSUE_A(it + 3, i >> 2);
                    if constexpr ((i == 3 || i == 11) && it < 5) { constexpr int pv = 60 + 2 * it + (i == 11); ISSUE_V(pv / 5, pv % 5); }
                    SB();
                });
            }
        });
        STAMP(11);
        asm volatile("" : "+v"(st2v));
        const f32x4 sv = __builtin_bit_cast(f32x4, st2v);
        const float smean = add_xor32(add_xor16(sv[0] + sv[2])) * (1.f / 640.f);
        const float rstd2 = __builtin_amdgcn_rsqf(fmaxf(add_xor32(add_xor16(sv[1] + sv[3])) * (1.f / 640.f) - smean * smean, 0.f) + 1e-5f);
#pragma unroll
        for (int m = 1; m < 4; ++m)
#pragma unroll
            for (int t = 0; t < 5; ++t) *(LAS f32x4*)(lb + ((((w + m) & 3) * 3 + (m - 1)) * 5 + t) * 1024 + lane * 16) = a[m][t];
        BAR();
        STAMP(46);
        f32x4 fin[5];
#pragma unroll
        for (int t = 0; t < 5; ++t) {
            const LAS char* p = (const LAS char*)(lb + (w * 15 + t) * 1024 + lane * 16);
            fin[t] = ((a[0][t] + *(const LAS f32x4*)p) + (*(const LAS f32x4*)(p + 5120) + *(const LAS f32x4*)(p + 10240))) * rstd2;
        }
        STAMP(47);
        LAS char* ot = (LAS char*)(lb + L_B + w * 2560);
#pragma unroll
        for (int t = 0; t < 5; ++t) {
            const u32x2 hp = *(LAS u32x2*)(lb + L_XRES + (16 * w + n) * 160 + 32 * t + 8 * g);
            const f32x4 ov = f32x4{bflo(hp[0]), bfhi(hp[0]), bflo(hp[1]), bfhi(hp[1])} + fin[t];
            *(LAS u32x2*)(ot + n * 160 + 32 * t + 8 * g) = pack4(ov);
        }
        asm volatile("s_waitcnt lgkmcnt(0)" ::: "memory");
        int lane3 = lane; asm volatile("" : "+v"(lane3));
#pragma unroll
        for (int u = 0; u < 3; ++u) {
            const int q = 64 * u + lane3, row = q / 10, sl = q - row * 10;
            if (q < 160) *(GAS u32x4*)(out + (size_t)(mtW * 16 + row) * 1280 + 160 * c8 + 16 * sl) = *(LAS u32x4*)(ot + q * 16);
        }
    }
    STAMP(12); STAMPRT(63);
}

extern "C" void kernel_launch(void* const* d_in, const int* in_sizes, int n_in, void* d_out, int out_size, void* d_ws, size_t ws_size,
                              hipStream_t stream) {
    (void)in_sizes; (void)n_in; (void)out_size; (void)ws_size;
    fused_block<<<256, 256, 0, stream>>>((const char*)d_in[0], (const char*)d_in[1], (const char*)d_in[2], (const char*)d_in[4],
                                         (const char*)d_in[5], (char*)d_out, (char*)d_ws);
}
```

```cpp
#include <hip/hip_runtime.h>
#include <stdint.h>
#pragma clang diagnostic ignored "-Wint-to-pointer-cast"

typedef short s16x4 __attribute__((ext_vector_type(4)));
typedef short s16x8 __attribute__((ext_vector_type(8)));
typedef float f32x4 __attribute__((ext_vector_type(4)));
typedef unsigned u32x4 __attribute__((ext_vector_type(4)));
typedef unsigned u32x2 __attribute__((ext_vector_type(2)));
typedef unsigned long long u64;
#define LAS __attribute__((address_space(3)))
#define GAS __attribute__((address_space(1)))
#define DI __device__ __forceinline__
#define RLX_AGENT __ATOMIC_RELAXED, __HIP_MEMORY_SCOPE_AGENT
#define SB() __builtin_amdgcn_sched_barrier(0)

constexpr size_t WS_FLAG = 4096;
constexpr size_t WS_KVP = 262144;
constexpr size_t WS_STAT2 = 0x120000;
constexpr size_t WS_O = 2u << 20;
constexpr size_t WS_H = 5u << 20;
constexpr size_t WS_H1 = 8u << 20;
constexpr size_t WS_W1R = 16u << 20;
constexpr size_t WS_W2R = 19u << 20;
constexpr size_t WS_STAMP = 24u << 20;
constexpr size_t WS_XN = 28u << 20;
constexpr size_t WS_WQR = 32u << 20;
constexpr size_t WS_W0R = 36u << 20;

constexpr int L_A = 0;
constexpr int L_B = 102400;
constexpr int L_XRES = L_B + 40960;
constexpr int L_M = 155648;
constexpr int L_MEAN = L_M;
constexpr int L_RSTD = L_M + 512;
constexpr int L_CSKV = L_M + 1024;
constexpr int L_CS4 = L_M + 1280;
constexpr int L_KVT = L_M + 1536;
constexpr int L_CS1 = L_M + 3328;
constexpr int L_JUNK = L_M + 5632;
constexpr int L_ZERO = L_M + 5888;
constexpr int L_CSQ = L_M + 5904 + 16;
constexpr int L_EPOCH = L_M + 7424;
constexpr int L_TRASH = L_M + 6400;
constexpr int LDS_BYTES = 163840;
constexpr int CODE_LINES = 288;

#define STAMP(i) do {} while (0)
#define STAMPRT(i) do {} while (0)

template <int I> struct IC { static constexpr int v = I; };
template <int B, int E, class F> DI void sfor(F&& f) { if constexpr (B < E) { f(IC<B>{}); sfor<B + 1, E>(f); } }
#define OPAQUE_S(p) asm volatile("" : "+s"(p))

DI f32x4 mfma(s16x8 a, s16x8 b, f32x4 c) { return __builtin_amdgcn_mfma_f32_16x16x32_bf16(a, b, c, 0, 0, 0); }
DI s16x4 trread(unsigned addr) { return __builtin_bit_cast(s16x4, __builtin_amdgcn_ds_read_tr16_b64_v4i16((LAS s16x4*)addr)); }
DI s16x8 cat(s16x4 a, s16x4 b) { return __builtin_shufflevector(a, b, 0, 1, 2, 3, 4, 5, 6, 7); }
DI unsigned pk2(float lo, float hi) {
    typedef __bf16 bf2 __attribute__((ext_vector_type(2)));
    typedef float f2 __attribute__((ext_vector_type(2)));
    f2 v = {lo, hi};
    return __builtin_bit_cast(unsigned, __builtin_convertvector(v, bf2));
}
DI s16x8 pack8(f32x4 a, f32x4 b) {
    u32x4 r = {pk2(a[0], a[1]), pk2(a[2], a[3]), pk2(b[0], b[1]), pk2(b[2], b[3])};
    return __builtin_bit_cast(s16x8, r);
}
DI u32x2 pack4(f32x4 a) { u32x2 r = {pk2(a[0], a[1]), pk2(a[2], a[3])}; return r; }
DI float bflo(unsigned u) { return __uint_as_float(u << 16); }
DI float bfhi(unsigned u) { return __uint_as_float(u & 0xffff0000u); }
DI float phi(float t) { return (fmaxf(t, 0.f) + 1e-6f) + __builtin_amdgcn_fmed3f(__builtin_amdgcn_exp2f(t * 1.44269504088896f), 0.f, 1.f); }
DI float add_xor16(float v) { const unsigned u = __float_as_uint(v); auto r = __builtin_amdgcn_permlane16_swap(u, u, false, false); return __uint_as_float(r[0]) + __uint_as_float(r[1]); }
DI float add_xor32(float v) { const unsigned u = __float_as_uint(v); auto r = __builtin_amdgcn_permlane32_swap(u, u, false, false); return __uint_as_float(r[0]) + __uint_as_float(r[1]); }
constexpr int wq_pos(int grp) { return grp < 3 ? 16384 + 5120 * grp : grp < 7 ? 32768 + 5120 * (grp - 3) : 5120 * (grp - 7); }

template <int LOFF> DI void glds16(unsigned voff, const void* sbase, unsigned lds) {
    asm volatile("s_add_u32 m0, %2, %3\n\ts_nop 0\n\tglobal_load_lds_dwordx4 %0, %1" ::"v"(voff), "s"(sbase), "s"(lds), "i"(LOFF) : "memory", "scc");
}
template <int LOFF, int GOFF> DI void glds16i(unsigned voff, const void* sbase, unsigned lds) {
    asm volatile("s_add_u32 m0, %2, %3\n\ts_nop 0\n\tglobal_load_lds_dwordx4 %0, %1 offset:%4" ::"v"(voff), "s"(sbase), "s"(lds), "i"(LOFF - GOFF), "i"(GOFF) : "memory", "scc");
}
template <int LOFF> DI void glds16c(unsigned voff, const void* sbase, unsigned lds) {
    asm volatile("s_add_u32 m0, %2, %3\n\ts_nop 0\n\tglobal_load_lds_dwordx4 %0, %1 sc1" ::"v"(voff), "s"(sbase), "s"(lds), "i"(LOFF) : "memory", "scc");
}
DI void glds4(unsigned voff, const void* sbase, unsigned lds) {
    asm volatile("s_mov_b32 m0, %2\n\ts_nop 0\n\tglobal_load_lds_dword %0, %1" ::"v"(voff), "s"(sbase), "s"(lds) : "memory");
}
#define WAITV(n) asm volatile("s_waitcnt vmcnt(%0)" ::"i"(n) : "memory")
#define BARX() do { __builtin_amdgcn_s_barrier(); asm volatile("" ::: "memory"); } while (0)
#define BAR() do { asm volatile("s_waitcnt lgkmcnt(0)" ::: "memory"); __builtin_amdgcn_s_barrier(); asm volatile("" ::: "memory"); } while (0)

DI void ld16_sc1(s16x8& d, unsigned voff, const void* sbase) {
    asm volatile("global_load_dwordx4 %0, %1, %2 sc1" : "=v"(d) : "v"(voff), "s"(sbase) : "memory");
}
DI void ld16(s16x8& d, unsigned voff, const void* sbase) {
    asm volatile("global_load_dwordx4 %0, %1, %2" : "=v"(d) : "v"(voff), "s"(sbase) : "memory");
}
DI void ld16a(s16x8& d, unsigned voff, const void* sbase) {
    asm volatile("global_load_dwordx4 %0, %1, %2" : "=a"(d) : "v"(voff), "s"(sbase) : "memory");
}
DI void ld8_sc1(u64& d, unsigned voff, const void* sbase) { asm volatile("global_load_dwordx2 %0, %1, %2 sc1" : "=v"(d) : "v"(voff), "s"(sbase) : "memory"); }
DI void st16_sc1(unsigned voff, void* sbase, u32x4 v) {
    asm volatile("global_store_dwordx4 %0, %1, %2 sc1\n\ts_nop 1" ::"v"(voff), "v"(v), "s"(sbase) : "memory");
}
DI void st8_sc1(unsigned voff, void* sbase, u32x2 v) {
    asm volatile("global_store_dwordx2 %0, %1, %2 sc1" ::"v"(voff), "v"(v), "s"(sbase) : "memory");
}
DI void drain_bar() { WAITV(0); BAR(); }
DI void flags_store(char* ws, int arr, int line, int slot, u64 tag) {
    __hip_atomic_store((GAS u64*)(ws + WS_FLAG + (size_t)arr * 32768 + (size_t)line * 128) + slot, tag, RLX_AGENT);
}
template <int NV> DI void await(char* ws, int arr, int line, u64 tag, int w, int lane) {
    if (w == 0) {
        GAS u64* fl = (GAS u64*)(ws + WS_FLAG + (size_t)arr * 32768 + (size_t)line * 128) + (lane & 15);
        for (unsigned spins = 0; spins < (1u << 20); ++spins) {
            u64 f = __hip_atomic_load(fl, RLX_AGENT);
            if (__all(f == tag)) break;
        }
    }
    WAITV(NV);
    BAR();
}

__global__ void __launch_bounds__(256)
prep(const char* __restrict__ x, const char* __restrict__ wqkv, const char* __restrict__ wo, const char* __restrict__ w1,
     const char* __restrict__ w2, char* __restrict__ ws) {
    __shared__ __attribute__((aligned(16))) char smem[20992];
    const unsigned lb = (unsigned)(uintptr_t)(LAS char*)smem;
    const int tid = threadIdx.x, lane = tid & 63, w = tid >> 6;
    const int g = lane >> 4, n = lane & 15, q4 = n >> 2, p4 = lane & 3;
    const int u = blockIdx.x;
    if (u < 128) {
        const int row = tid >> 4, c = tid & 15;
        u32x4 d[5];
#pragma unroll
        for (int i = 0; i < 5; ++i) d[i] = *(const GAS u32x4*)(x + (size_t)(u * 16 + row) * 1280 + (c + 16 * i) * 16);
        float v[40], su = 0.f;
#pragma unroll
        for (int i = 0; i < 5; ++i)
#pragma unroll
            for (int j = 0; j < 4; ++j) { v[8 * i + 2 * j] = bflo(d[i][j]); v[8 * i + 2 * j + 1] = bfhi(d[i][j]); su += v[8 * i + 2 * j] + v[8 * i + 2 * j + 1]; }
#pragma unroll
        for (int m = 1; m < 16; m <<= 1) su += __shfl_xor(su, m);
        const float mean = su * (1.f / 640.f);
        float s2 = 0.f;
#pragma unroll
        for (int i = 0; i < 40; ++i) { v[i] -= mean; s2 += v[i] * v[i]; }
#pragma unroll
        for (int m = 1; m < 16; m <<= 1) s2 += __shfl_xor(s2, m);
        const float rstd = __builtin_amdgcn_rsqf(s2 * (1.f / 640.f) + 1e-5f);
#pragma unroll
        for (int i = 0; i < 5; ++i)
            *(LAS u32x4*)(lb + row * 1296 + (c + 16 * i) * 16) = u32x4{pk2(v[8 * i] * rstd, v[8 * i + 1] * rstd), pk2(v[8 * i + 2] * rstd, v[8 * i + 3] * rstd),
                                                                      pk2(v[8 * i + 4] * rstd, v[8 * i + 5] * rstd), pk2(v[8 * i + 6] * rstd, v[8 * i + 7] * rstd)};
        __syncthreads();
#pragma unroll
        for (int k = 0; k < 5; ++k) {
            const int ks = 5 * w + k;
            const u32x2 lo = *(LAS u32x2*)(lb + n * 1296 + 64 * ks + 8 * g), hi = *(LAS u32x2*)(lb + n * 1296 + 64 * ks + 32 + 8 * g);
            *(GAS u32x4*)(ws + WS_XN + (size_t)(u * 20 + ks) * 1024 + lane * 16) = u32x4{lo[0], lo[1], hi[0], hi[1]};
        }
    } else if (u < 448) {
        const int v_ = u - 128, j = v_ / 20, ks = v_ - 20 * j;
#pragma unroll
        for (int r = 0; r < 2; ++r) {
            const int idx = tid + 256 * r;
            if (idx < 480) {
                const int row = idx / 15, sl = idx - 15 * row, sec = sl / 5;
                *(LAS u32x4*)(lb + row * 272 + sl * 16) = *(const GAS u32x4*)(wqkv + (size_t)(32 * ks + row) * 3840 + sec * 1280 + 80 * j + (sl - 5 * sec) * 16);
            }
        }
        __syncthreads();
#pragma unroll
        for (int k = 0; k < 2; ++k) {
            const int t = 2 * w + k;
            const int off = t == 0 ? 80 + 8 * p4 : t == 1 ? 120 + 8 * p4 : t == 2 ? 160 + 8 * p4 : t == 3 ? 200 + 8 * p4
                          : t == 4 ? (p4 == 0 ? 112 : p4 == 1 ? 152 : p4 == 2 ? 192 : 232) : t == 5 ? 8 * p4 : t == 6 ? 40 + 8 * p4 : ((p4 & 1) ? 72 : 32);
            const unsigned a_ = lb + (4 * g + q4) * 272 + off;
            s16x8 f = cat(trread(a_), trread(a_ + 16 * 272));
            if (t == 7 && n >= 8) f = s16x8{0, 0, 0, 0, 0, 0, 0, 0};
            *(GAS s16x8*)(ws + WS_WQR + (size_t)((j * 20 + ks) * 8 + t) * 1024 + lane * 16) = f;
        }
    } else {
        const int row = tid >> 3, sl = tid & 7;
        const char* src; char* dst;
        const int T4 = w;
        if (u < 648) {
            const int v_ = u - 448, ks = v_ / 10, cl = v_ - 10 * ks;
            const int srow = ks < 16 ? 40 * ks + (row < 16 ? row : row + 4) : 20 * (4 * (2 * (ks - 16) + (row >> 4)) + ((row & 15) >> 2)) + 16 + (row & 3);
            src = wo + (size_t)srow * 1280 + cl * 128;
            const int T = 4 * cl + T4, c8r = T / 5;
            dst = ws + WS_W0R + (size_t)((c8r * 20 + ks) * 5 + (T - 5 * c8r)) * 1024;
        } else if (u < 1188) {
            const int v_ = u - 648, ks = v_ / 27, cl = v_ - 27 * ks;
            src = w1 + (size_t)(32 * ks + row) * 3456 + cl * 128;
            dst = ws + WS_W1R + (size_t)((4 * cl + T4) * 20 + ks) * 1024;
        } else {
            const int v_ = u - 1188, ks = v_ / 10, cl = v_ - 10 * ks;
            src = w2 + (size_t)(32 * ks + row) * 1280 + cl * 128;
            const int T = 4 * cl + T4, c8r = T / 5;
            dst = ws + WS_W2R + (size_t)((c8r * 54 + ks) * 5 + (T - 5 * c8r)) * 1024;
        }
        *(LAS u32x4*)(lb + row * 128 + 16 * (sl ^ (((row >> 1) & 3) << 1))) = *(const GAS u32x4*)(src + sl * 16);
        __syncthreads();
        const int rw = 4 * g + q4;
        const unsigned a_ = lb + rw * 128 + 16 * ((2 * w + (p4 >> 1)) ^ (((rw >> 1) & 3) << 1)) + 8 * (p4 & 1);
        *(GAS s16x8*)(dst + lane * 16) = cat(trread(a_), trread(a_ + 2048));
    }
}

__global__ void __launch_bounds__(256, 1)
fused_block(const char* __restrict__ x, const char* __restrict__ wqkv, const char* __restrict__ wo, const char* __restrict__ w1,
            const char* __restrict__ w2, char* __restrict__ out, char* __restrict__ ws) {
    __shared__ __attribute__((aligned(16))) char smem[LDS_BYTES];
    const unsigned lb = (unsigned)(uintptr_t)(LAS char*)smem;
    const int tid = threadIdx.x, lane = tid & 63;
    const int w = __builtin_amdgcn_readfirstlane(tid >> 6);
    const int b = blockIdx.x, x8 = b & 7, rr = b >> 3;
    const int i1 = 4 * (x8 >> 1) + (rr >> 3), j1 = 8 * (x8 & 1) + (rr & 7);
    const int i2 = i1, j2 = j1;
    const int half = (rr >> 2) & 1, c8 = 4 * (x8 & 1) + (rr & 3), jw = 8 * half + c8;
    const int g = lane >> 4, n = lane & 15, q4 = n >> 2, p4 = lane & 3;
    const s16x8 ones = {0x3F80, 0x3F80, 0x3F80, 0x3F80, 0x3F80, 0x3F80, 0x3F80, 0x3F80};
    const f32x4 z4 = {0, 0, 0, 0};
    STAMP(0); STAMPRT(62);

    {
        u64 pc;
        asm volatile("s_getpc_b64 %0" : "=s"(pc));
        const unsigned cl_ = (rr * 4 + w) * 3 + ((lane & 3) < 3 ? (lane & 3) : 0);
        glds4((cl_ < CODE_LINES ? cl_ : CODE_LINES - 1) * 128, (const void*)(pc & ~(u64)127), lb + L_JUNK);
    }
    const unsigned vlan16 = lane * 16;

    f32x4 acc[8][2];
    {
        s16x8 X[20][2];
        const char* wsrc = ws + WS_WQR + (size_t)(j1 * 20 * 8 + 2 * w) * 1024;
        unsigned xrun = (unsigned)((8 * i1 + 2 * w) * 20 * 1024) + vlan16;
        const unsigned ldsW = lb + w * 2048;
        auto issue = [&](auto KS) {
            constexpr int ks = KS.v;
            glds16<(ks % 18) * 8192>(vlan16, wsrc, ldsW);
            glds16<(ks % 18) * 8192 + 1024>(vlan16 + 1024, wsrc, ldsW);
            wsrc += 8192; OPAQUE_S(wsrc);
            ld16(X[ks][0], xrun, ws + WS_XN);
            ld16(X[ks][1], xrun + 20 * 1024, ws + WS_XN);
            xrun += 1024; asm volatile("" : "+v"(xrun));
        };
        sfor<0, 8>([&](auto KS) { issue(KS); });
        asm volatile("s_mov_b32 m0, %2\n\ts_nop 0\n\tglobal_load_lds_dword %0, %1 sc1" ::"v"((lane & 1) * 4), "s"(ws), "s"(lb + L_EPOCH) : "memory");
        unsigned vW0 = lb + vlan16, vW1 = lb + 65536 + vlan16, vW2 = lb + 131072 + vlan16;
        asm volatile("" : "+v"(vW0), "+v"(vW1), "+v"(vW2));
        auto wread = [&](auto KS, auto TT) -> s16x8 {
            constexpr int o = (KS.v % 18) * 8192 + TT.v * 1024;
            return *(volatile LAS s16x8*)((o < 65536 ? vW0 : o < 131072 ? vW1 : vW2) + (o & 65535));
        };
#pragma unroll
        for (int t = 0; t < 8; ++t) acc[t][0] = acc[t][1] = z4;
        s16x8 Wf[2][8];
        WAITV(4 * 6 + 1);
        BARX();
        sfor<0, 8>([&](auto TT) { Wf[0][TT.v] = wread(IC<0>{}, TT); });
        SB();
        sfor<0, 20>([&](auto KS) {
            constexpr int ks = KS.v;
            if constexpr ((ks & 1) == 0) {
                constexpr int issued = 4 * ((ks + 7 < 19 ? ks + 7 : 19) + 1), need = 4 * (ks + 3 < 20 ? ks + 3 : 20);
                WAITV(issued - need + (ks + 2 < 8 ? 1 : 0));
                BARX();
                SB();
            }
            asm volatile("" : "+v"(X[ks][0]), "+v"(X[ks][1]));
            sfor<0, 16>([&](auto I) {
                constexpr int i = I.v, t = i >> 1, m = i & 1;
                if constexpr (t < 5) acc[t][m] = mfma(X[ks][m], Wf[ks & 1][t], acc[t][m]);
                else acc[t][m] = mfma(Wf[ks & 1][t], X[ks][m], acc[t][m]);
                if constexpr (ks < 19 && (i & 1) == 0) Wf[(ks + 1) & 1][t] = wread(IC<ks + 1>{}, IC<t>{});
                if constexpr (i == 5 && ks + 8 < 20) issue(IC<(ks + 8 < 20 ? ks + 8 : 19)>{});
                SB();
            });
        });
    }
    const u64 tag = *(volatile LAS u64*)(lb + L_EPOCH) + 1;
    STAMP(1);

    {
        const unsigned red = lb + 16384 + w * 3360, trash = lb + L_TRASH + vlan16;
        const unsigned r0 = red + (n * 20 + 4 * g) * 4;
        const unsigned r1 = (n == 0 || (n >= 8 && n < 12)) ? red + ((n == 0 ? 20 : 8 + n) * 20 + 4 * g) * 4 : trash;
        const unsigned r2 = (n == 0 || n >= 12) ? red + (420 + (n == 0 ? 20 : 4 + n) * 20 + 4 * g) * 4 : trash;
        const unsigned r3 = g < 2 ? red + (420 * g + n * 20 + 16) * 4 : trash;
        const unsigned r4 = (g < 2 && (n == 0 || (g == 0 ? (n >= 8 && n < 12) : n >= 12))) ? red + (420 * g + (n == 0 ? 20 : (g == 0 ? 8 + n : 4 + n)) * 20 + 16) * 4 : trash;
        s16x8 F[4], MA, MB;
        sfor<0, 5>([&](auto TT) {
            constexpr int t = TT.v;
            f32x4 v[2], vb_[2];
#pragma unroll
            for (int m = 0; m < 2; ++m)
#pragma unroll
                for (int r = 0; r < 4; ++r) {
                    const float y = acc[t][m][r];
                    if (t < 2) v[m][r] = phi(y);
                    else if (t < 4) v[m][r] = y;
                    else { v[m][r] = n < 8 ? phi(y) : y; vb_[m][r] = n < 8 ? 1.f : y; }
                }
            if constexpr (t < 4) F[t] = pack8(v[0], v[1]);
            else { MA = pack8(v[0], v[1]); MB = pack8(vb_[0], vb_[1]); }
        });
        STAMP(33);
        f32x4 P1 = mfma(F[0], F[2], z4), P2 = mfma(F[0], MB, z4), P3 = mfma(MA, F[2], z4), P4 = mfma(MA, MB, z4);
        f32x4 P5 = mfma(F[1], F[3], z4), P6 = mfma(F[1], MB, z4), P7 = mfma(MA, F[3], z4);
        *(LAS f32x4*)(r0) = P1;
        *(LAS f32x4*)(r0 + 1680) = P5;
        *(LAS f32x4*)(r1) = P2;
        *(LAS f32x4*)(r2) = P6;
        *(LAS f32x4*)(r3) = (g == 0 ? P3 : P7);
        *(LAS f32x4*)(r4) = P4;
        if (tid == 0) { *(LAS u64*)(lb + L_ZERO) = 0; *(LAS u64*)(lb + L_ZERO + 8) = 0; }
    }
    STAMP(34);
    BAR();
    if (tid < 210) {
        LAS char* red = (LAS char*)(lb + 16384 + tid * 16);
        f32x4 s = (*(LAS f32x4*)(red) + *(LAS f32x4*)(red + 3360)) + (*(LAS f32x4*)(red + 6720) + *(LAS f32x4*)(red + 10080));
        st16_sc1((unsigned)(tid * 16), ws + WS_KVP + (size_t)(j1 * 16 + i1) * 3360, __builtin_bit_cast(u32x4, s));
    }
    STAMP(36);
    drain_bar();
    if (tid < 16) flags_store(ws, 0, tid * 16 + j1, i1, tag);
    STAMP(2);

    {
        const char* s0 = ws + WS_W0R + (size_t)(c8 * 100 + w) * 1024;
        sfor<0, 25>([&](auto U) { glds16<U.v * 4096>(vlan16, s0, lb + w * 1024); s0 += 4096; OPAQUE_S(s0); });
        if (w < 2) {
            int lane2 = lane; asm volatile("" : "+v"(lane2));
            sfor<0, 5>([&](auto U) { const int q = 64 * U.v + lane2, row = q / 10, sl = q - row * 10;
                glds16<U.v * 1024>((unsigned)(row * 1280 + 160 * c8 + 16 * sl), x + (size_t)(8 * i2 + 4 * half + 2 * w) * 16 * 1280, lb + L_XRES + w * 5120); });
        }
    }
    s16x8 Fa[2], Fb[2];
#pragma unroll
    for (int m = 0; m < 2; ++m) {
        f32x4 v[3];
#pragma unroll
        for (int t = 0; t < 3; ++t)
#pragma unroll
            for (int r = 0; r < 4; ++r) v[t][r] = phi(acc[5 + t][m][r]);
        u32x4 u2 = __builtin_bit_cast(u32x4, pack8(v[0], v[2])), u3 = __builtin_bit_cast(u32x4, pack8(v[1], v[2]));
        if (g >= 2) { u2[2] = u2[3] = 0u; u3[2] = u3[3] = 0u; }
        Fa[m] = __builtin_bit_cast(s16x8, u2); Fb[m] = __builtin_bit_cast(s16x8, u3);
    }
    unsigned oa[12];
    {
        const unsigned kt = lb + L_KVT, zz = lb + L_ZERO;
        auto ka = [&](int head, int e, int c) { return kt + (head * 420 + e * 20 + 4 * c) * 2; };
        oa[0] = ka(0, n, g);  oa[1] = g == 0 ? ka(0, n, 4) : zz;
        oa[2] = ka(1, n, g);  oa[3] = g == 1 ? ka(1, n, 4) : zz;
        oa[4] = ka(0, 20, g); oa[5] = g == 0 ? ka(0, 20, 4) : zz;
        oa[6] = ka(1, 20, g); oa[7] = g == 1 ? ka(1, 20, 4) : zz;
        oa[8] = n < 4 ? ka(0, 16 + n, g) : zz;                oa[9] = (n < 4 && g == 0) ? ka(0, 16 + n, 4) : zz;
        oa[10] = (n >= 4 && n < 8) ? ka(1, 12 + n, g) : zz;   oa[11] = (n >= 4 && n < 8 && g == 1) ? ka(1, 12 + n, 4) : zz;
    }
    asm volatile("" : "+v"(Fa[0]), "+v"(Fa[1]), "+v"(Fb[0]), "+v"(Fb[1]), "+v"(oa[0]), "+v"(oa[1]), "+v"(oa[2]), "+v"(oa[3]), "+v"(oa[4]), "+v"(oa[5]),
                 "+v"(oa[6]), "+v"(oa[7]), "+v"(oa[8]), "+v"(oa[9]), "+v"(oa[10]), "+v"(oa[11]));
    STAMP(3);

    await<63>(ws, 0, i1 * 16 + j1, tag, w, lane);
    STAMP(4);
    {
        s16x8 kp[16];
        const char* kb = ws + WS_KVP + (size_t)j1 * 16 * 3360;
        const unsigned kvo = (tid < 210 ? tid : 209) * 16;
#pragma unroll
        for (int i = 0; i < 16; ++i) ld16_sc1(kp[i], kvo + i * 3360, kb);
        asm volatile("s_waitcnt vmcnt(0)" : "+v"(kp[0]), "+v"(kp[1]), "+v"(kp[2]), "+v"(kp[3]), "+v"(kp[4]), "+v"(kp[5]), "+v"(kp[6]), "+v"(kp[7]),
                     "+v"(kp[8]), "+v"(kp[9]), "+v"(kp[10]), "+v"(kp[11]), "+v"(kp[12]), "+v"(kp[13]), "+v"(kp[14]), "+v"(kp[15])::"memory");
        f32x4 s0 = __builtin_bit_cast(f32x4, kp[0]), s1 = __builtin_bit_cast(f32x4, kp[1]);
#pragma unroll
        for (int i = 2; i < 16; i += 2) { s0 += __builtin_bit_cast(f32x4, kp[i]); s1 += __builtin_bit_cast(f32x4, kp[i + 1]); }
        if (tid < 210) *(LAS u32x2*)(lb + L_KVT + tid * 8) = pack4(s0 + s1);
    }
    STAMP(38);
    BAR();
    STAMP(39);
    {
        auto rd2 = [&](unsigned a0, unsigned a1) { const u32x2 lo = *(LAS u32x2*)a0, hi = *(LAS u32x2*)a1; return __builtin_bit_cast(s16x8, u32x4{lo[0], lo[1], hi[0], hi[1]}); };
        const s16x8 Ama = rd2(oa[0], oa[1]), Amb = rd2(oa[2], oa[3]), Aza = rd2(oa[4], oa[5]), Azb = rd2(oa[6], oa[7]), Ata = rd2(oa[8], oa[9]), Atb = rd2(oa[10], oa[11]);
#pragma unroll
        for (int m = 0; m < 2; ++m) {
            f32x4 Da = mfma(Ama, Fa[m], z4), Db = mfma(Amb, Fb[m], z4);
            f32x4 Dt = mfma(Atb, Fb[m], mfma(Ata, Fa[m], z4));
            f32x4 Dza = mfma(Aza, Fa[m], z4), Dzb = mfma(Azb, Fb[m], z4);
            const float ra = __builtin_amdgcn_rcpf(Dza[0] + 1e-6f), rb = __builtin_amdgcn_rcpf(Dzb[0] + 1e-6f);
            const int mt = 8 * i1 + 2 * w + m;
            s16x8 of = pack8(Da * ra, Db * rb);
            st16_sc1((unsigned)((mt * 20 + j1) * 1024 + lane * 16), ws + WS_O, __builtin_bit_cast(u32x4, of));
            u32x2 tl = pack4(Dt * (g == 0 ? ra : rb));
            const int t = j1 >> 1;
            if (g < 2) st8_sc1((unsigned)((mt * 20 + 16 + (t >> 1)) * 1024 + (n + 16 * (2 * (j1 & 1) + g)) * 16 + (t & 1) * 8), ws + WS_O, tl);
        }
    }
    STAMP(40);
    drain_bar();
    if (tid < 16) flags_store(ws, 1, i1 * 16 + tid, j1, tag);
    STAMP(5);

    const int T0 = j2 < 12 ? 7 * j2 : 84 + 6 * (j2 - 12);
    const int odd = (j2 < 12) ? (j2 & 1) : 0;
    const int Tp0 = T0 + odd;
    const int Tsg = j2 < 12 ? (odd ? T0 : T0 + 6) : Tp0;
    const int ksb = w < 2 ? 14 * w : 28 + 13 * (w - 2);
    const int nks = w < 2 ? 14 : 13;
    s16x8 V[14][5];
    const char* w2run = ws + WS_W2R + (size_t)(c8 * 54 + ksb) * 5120;
    const int step13 = nks == 14 ? 5120 : 0;
#define ISSUE_V(it, t) do { if constexpr ((it) < 8) ld16a(V[it][t], vlan16 + (t) * 1024, w2run); else ld16(V[it][t], vlan16 + (t) * 1024, w2run); \
            if ((t) == 4) { w2run += ((it) == 12 ? step13 : 5120); OPAQUE_S(w2run); } } while (0)
    sfor<0, 10>([&](auto P) {
        constexpr int bg = P.v;
        const int Tg = bg < 5 ? Tp0 + 5 : Tsg;
        glds16<(5 + bg / 5) * 20480 + (bg % 5) * 4096>(vlan16, ws + WS_W1R + (size_t)(Tg * 20 + (bg % 5) * 4 + w) * 1024, lb + w * 1024);
    });
    sfor<0, 10>([&](auto P) { ISSUE_V(P.v / 5, P.v % 5); });
    const int mtW = 8 * i2 + 4 * half + w;
    STAMP(45);
    await<63>(ws, 1, i2 * 16 + jw, tag, w, lane);
    STAMP(6);
    {
        s16x8 O[20];
        unsigned orun = (unsigned)(mtW * 20 * 1024 + lane * 16);
#define ISSUE_O(i) do { ld16_sc1(O[i], orun, ws + WS_O); orun += 1024u; asm volatile("" : "+v"(orun)); } while (0)
        sfor<0, 8>([&](auto I) { ISSUE_O(I.v); });
        unsigned vA = lb + L_A + vlan16, vA2 = vA + 61440;
        asm volatile("" : "+v"(vA), "+v"(vA2));
        f32x4 a[5];
#pragma unroll
        for (int t = 0; t < 5; ++t) a[t] = z4;
        s16x8 Wf[3][5];
        auto wrd = [&](auto I, auto T) -> s16x8 { constexpr int o = (5 * I.v + T.v) * 1024; return *(volatile LAS s16x8*)((o < 61440 ? vA : vA2) + (o < 61440 ? o : o - 61440)); };
        sfor<0, 2>([&](auto I) { sfor<0, 5>([&](auto T) { Wf[I.v][T.v] = wrd(I, T); }); });
        SB();
        sfor<0, 20>([&](auto I) {
            constexpr int i = I.v;
            asm volatile("s_waitcnt vmcnt(%1)" : "+v"(O[i]) : "i"((i + 7 < 19 ? i + 7 : 19) - i) : "memory");
            SB();
            sfor<0, 5>([&](auto T) {
                constexpr int t = T.v;
                a[t] = mfma(Wf[i % 3][t], O[i], a[t]);
                if constexpr (i + 2 < 20) Wf[(i + 2) % 3][t] = wrd(IC<i + 2>{}, T);
                if constexpr (t == 1 && i + 8 < 20) ISSUE_O(i + 8);
                SB();
            });
        });
        STAMP(28);
        float su = 0.f, s2 = 0.f;
        u32x2 hpk[5];
#pragma unroll
        for (int t = 0; t < 5; ++t) {
            const u32x2 xr = *(LAS u32x2*)(lb + L_XRES + (16 * w + n) * 160 + 32 * t + 8 * g);
            const f32x4 hv = f32x4{bflo(xr[0]), bfhi(xr[0]), bflo(xr[1]), bfhi(xr[1])} + a[t];
            hpk[t] = pack4(hv);
            *(LAS u32x2*)(lb + L_XRES + (16 * w + n) * 160 + 32 * t + 8 * g) = hpk[t];
#pragma unroll
            for (int r = 0; r < 4; ++r) { su += hv[r]; s2 += hv[r] * hv[r]; }
        }
        const unsigned hb = (unsigned)((mtW * 20 + ((5 * c8) >> 1)) * 1024 + lane * 16);
        const int od = c8 & 1;
        st8_sc1(hb + (od ? 8 : 2048), ws + WS_H, od ? hpk[0] : hpk[4]);
        { const u32x2 p0 = od ? hpk[1] : hpk[0], p1 = od ? hpk[2] : hpk[1]; st16_sc1(hb + (od ? 1024 : 0), ws + WS_H, u32x4{p0[0], p0[1], p1[0], p1[1]}); }
        { const u32x2 p0 = od ? hpk[3] : hpk[2], p1 = od ? hpk[4] : hpk[3]; st16_sc1(hb + (od ? 2048 : 1024), ws + WS_H, u32x4{p0[0], p0[1], p1[0], p1[1]}); }
        su = add_xor32(add_xor16(su)); s2 = add_xor32(add_xor16(s2));
        if (g == 0) { u32x2 st = {__float_as_uint(su), __float_as_uint(s2)}; st8_sc1((unsigned)((mtW * 16 + n) * 64 + c8 * 8), ws + WS_STAT2, st); }
    }
    STAMP(42);
    drain_bar();
    if (tid < 16) flags_store(ws, 2, i2 * 16 + tid, jw, tag);
    if (b == 0 && tid == 0) __hip_atomic_store((GAS u64*)ws, tag, RLX_AGENT);
    STAMP(7);

    {
        const char* s1 = ws + WS_W1R + (size_t)(Tp0 * 20 + w) * 1024;
        const unsigned l1 = lb + L_A + w * 1024;
        sfor<0, 25>([&](auto U) { glds16<U.v * 4096>(vlan16, s1, l1); s1 += 4096; OPAQUE_S(s1); });
    }
    STAMP(43);
    await<0>(ws, 2, i2 * 16 + j2, tag, w, lane);
    STAMP(8);
    {
        s16x8 H[20][2];
        const int mt0 = 8 * i2 + 2 * w;
        s16x8 stv[2];
#pragma unroll
        for (int m = 0; m < 2; ++m) ld16_sc1(stv[m], (unsigned)(((mt0 + m) * 16 + n) * 64 + g * 16), ws + WS_STAT2);
        unsigned hrun = (unsigned)(mt0 * 20 * 1024 + lane * 16);
#define ISSUE_H(ks) do { ld16_sc1(H[ks][0], hrun, ws + WS_H); ld16_sc1(H[ks][1], hrun + 20 * 1024, ws + WS_H); hrun += 1024; asm volatile("" : "+v"(hrun)); } while (0)
        unsigned vF0 = lb + vlan16, vF1 = lb + 61440 + vlan16, vF2 = lb + 122880 + vlan16;
        asm volatile("" : "+v"(vF0), "+v"(vF1), "+v"(vF2));
        auto wread = [&](auto TT, auto KS) -> s16x8 {
            constexpr int t = TT.v, ks = KS.v;
            return *(volatile LAS s16x8*)((t < 3 ? vF0 : t < 6 ? vF1 : vF2) + (t % 3) * 20480 + ks * 1024);
        };
        {
            f32x4 c0 = z4, c1 = z4;
            const unsigned ta = lb + vlan16 + w * 20480, tb = lb + vlan16 + (w < 3 ? w + 4 : w) * 20480;
            s16x8 Ca[4], Cb[4];
            sfor<0, 3>([&](auto K) { Ca[K.v] = *(volatile LAS s16x8*)(ta + K.v * 1024); Cb[K.v] = *(volatile LAS s16x8*)(tb + K.v * 1024); });
            SB();
            sfor<0, 20>([&](auto K) {
                constexpr int k = K.v;
                c0 = mfma(ones, Ca[k & 3], c0);
                if constexpr (k + 3 < 20) Ca[(k + 3) & 3] = *(volatile LAS s16x8*)(ta + (k + 3) * 1024);
                SB();
                c1 = mfma(ones, Cb[k & 3], c1);
                if constexpr (k + 3 < 20) Cb[(k + 3) & 3] = *(volatile LAS s16x8*)(tb + (k + 3) * 1024);
                if constexpr ((k & 1) == 0 && k < 16) ISSUE_H(k >> 1);
                SB();
            });
            auto split = [&](float c) { const unsigned hi = pk2(c, c); const float lo = c - bflo(hi); u32x2 r = {hi, pk2(lo, lo)}; return r; };
            if (g == 0) {
                *(LAS u32x2*)(lb + L_CS1 + w * 128 + n * 8) = split(c0[0]);
                if (w < 3) *(LAS u32x2*)(lb + L_CS1 + (w + 4) * 128 + n * 8) = split(c1[0]);
            }
            if (tid < 2) *(LAS u64*)(lb + L_CS1 + 896 + tid * 8) = 0;
        }
        STAMP(19);
        asm volatile("s_waitcnt vmcnt(16)" : "+v"(stv[0]), "+v"(stv[1])::"memory");
        s16x8 Hm[2];
#pragma unroll
        for (int m = 0; m < 2; ++m) {
            f32x4 s = __builtin_bit_cast(f32x4, stv[m]);
            const float nm = add_xor32(add_xor16(s[0] + s[2])) * (-1.f / 640.f);
            const unsigned hi = pk2(nm, nm);
            const unsigned pr = g == 0 ? ((hi & 0xffffu) | (pk2(nm - bflo(hi), 0.f) << 16)) : 0u;
            Hm[m] = __builtin_bit_cast(s16x8, u32x4{pr, pr, 0u, 0u});
        }
        s16x8 Wf1[3][4];
        sfor<0, 2>([&](auto KS) { sfor<0, 4>([&](auto TT) { Wf1[KS.v][TT.v] = wread(TT, KS); }); });
        __builtin_amdgcn_s_waitcnt(0xC87F);
        BARX();
        const unsigned csa = lb + L_CS1 + (g == 0 ? n * 8 : 896);
        auto csfrag = [&](int t) { const u32x2 c = *(LAS u32x2*)(csa + (g == 0 ? t * 128 : 0)); return __builtin_bit_cast(s16x8, u32x4{c[0], c[1], 0u, 0u}); };
        auto relu2 = [&](unsigned p) { typedef short s2_ __attribute__((ext_vector_type(2))); const s2_ z = {0, 0};
                                       return __builtin_bit_cast(unsigned, __builtin_elementwise_max(__builtin_bit_cast(s2_, p), z)); };
        auto fin8 = [&](f32x4 x0, f32x4 x1) { return u32x4{relu2(pk2(x0[0], x0[1])), relu2(pk2(x0[2], x0[3])), relu2(pk2(x1[0], x1[1])), relu2(pk2(x1[2], x1[3]))}; };
        f32x4 a1[4][2];
        sfor<0, 4>([&](auto TT) { const s16x8 cf = csfrag(TT.v); a1[TT.v][0] = mfma(cf, Hm[0], z4); a1[TT.v][1] = mfma(cf, Hm[1], z4); });
        {
            auto& Wf = Wf1;
            SB();
            sfor<0, 20>([&](auto KS) {
                constexpr int ks = KS.v;
                constexpr int ny = 2 * ((ks + 7 < 19 ? ks + 7 : 19) - ks) + (ks > 12 ? (ks < 17 ? 2 * (ks - 12) : 10) : 0);
                asm volatile("s_waitcnt vmcnt(%2)" : "+v"(H[ks][0]), "+v"(H[ks][1]) : "i"(ny) : "memory");
                SB();
                sfor<0, 8>([&](auto I) {
                    constexpr int i = I.v, t = i >> 1, m = i & 1;
                    a1[t][m] = mfma(Wf[ks % 3][t], H[ks][m], a1[t][m]);
                    if constexpr (ks + 2 < 20 && (i & 1) == 0) Wf[(ks + 2) % 3][t] = wread(IC<t>{}, IC<ks + 2>{});
                    if constexpr (i == 3 && ks + 8 < 20) ISSUE_H(ks + 8);
                    if constexpr (ks >= 12 && ks < 17 && (i == 1 || i == 5)) { constexpr int pv = 10 + 2 * (ks - 12) + (i == 5); ISSUE_V(pv / 5, pv % 5); }
                    SB();
                });
            });
        }
        STAMP(20);
        f32x4 a2[3][2];
        sfor<0, 3>([&](auto TT) { const s16x8 cf = csfrag(4 + TT.v); a2[TT.v][0] = mfma(cf, Hm[0], z4); a2[TT.v][1] = mfma(cf, Hm[1], z4); });
        {
            s16x8 Wf[3][3];
            sfor<0, 2>([&](auto KS) { sfor<0, 3>([&](auto TT) { Wf[KS.v][TT.v] = wread(IC<4 + TT.v>{}, KS); }); });
            u32x2 e0, e1;
            sfor<0, 20>([&](auto KS) {
                constexpr int ks = KS.v;
                sfor<0, 6>([&](auto I) {
                    constexpr int i = I.v, t = i >> 1, m = i & 1;
                    a2[t][m] = mfma(Wf[ks % 3][t], H[ks][m], a2[t][m]);
                    if constexpr (ks + 2 < 20 && (i & 1) == 0) Wf[(ks + 2) % 3][t] = wread(IC<4 + t>{}, IC<ks + 2>{});
                    if constexpr (i == 3 || (i == 5 && (ks & 1) == 0)) { constexpr int pv = 20 + (ks >> 1) * 3 + ((ks & 1) ? 2 : (i == 5 ? 1 : 0)); ISSUE_V(pv / 5, pv % 5); }
                    if constexpr (ks >= 1 && ks < 17 && ((ks - 1) & 3) < 3) {
                        constexpr int u = (ks - 1) >> 2, ph = (ks - 1) & 3, P = u >> 1, mm = u & 1;
                        if constexpr (ph == 0 && i == 1) e0 = u32x2{relu2(pk2(a1[2 * P][mm][0], a1[2 * P][mm][1])), relu2(pk2(a1[2 * P][mm][2], a1[2 * P][mm][3]))};
                        if constexpr (ph == 1 && i == 1) e1 = u32x2{relu2(pk2(a1[2 * P + 1][mm][0], a1[2 * P + 1][mm][1])), relu2(pk2(a1[2 * P + 1][mm][2], a1[2 * P + 1][mm][3]))};
                        if constexpr (ph == 2 && i == 1)
                            st16_sc1((unsigned)(((mt0 + mm) * 54 + (Tp0 >> 1) + P) * 1024 + lane * 16), ws + WS_H1, u32x4{e0[0], e0[1], e1[0], e1[1]});
                    }
                    SB();
                });
            });
        }
        STAMP(22);
#pragma unroll
        for (int m = 0; m < 2; ++m) {
            st16_sc1((unsigned)(((mt0 + m) * 54 + (Tp0 >> 1) + 2) * 1024 + lane * 16), ws + WS_H1, fin8(a2[0][m], a2[1][m]));
            if (j2 < 12) st8_sc1((unsigned)(((mt0 + m) * 54 + (Tsg >> 1)) * 1024 + lane * 16 + (Tsg & 1) * 8), ws + WS_H1,
                                 u32x2{relu2(pk2(a2[2][m][0], a2[2][m][1])), relu2(pk2(a2[2][m][2], a2[2][m][3]))});
        }
        STAMP(24);
        drain_bar();
        if (tid < 16) flags_store(ws, 3, i2 * 16 + tid, j2, tag);
        STAMP(9);

        sfor<50, 70>([&](auto U) { ISSUE_V(U.v / 5, U.v % 5); });
        STAMP(44);
        await<63>(ws, 3, i2 * 16 + jw, tag, w, lane);
        STAMP(10);
        unsigned arun = (unsigned)(((8 * i2 + 4 * half) * 54 + ksb) * 1024 + lane * 16);
        const unsigned astep13 = nks == 14 ? 1024u : 0u;
        s16x8 A[4][4];
        unsigned aoff[4];
#pragma unroll
        for (int m = 0; m < 4; ++m) aoff[m] = ((w + m) & 3) * 54 * 1024;
        s16x8 st2v;
        ld16_sc1(st2v, (unsigned)((mtW * 16 + n) * 64 + g * 16), ws + WS_STAT2);
#define ISSUE_A(it, m) do { ld16_sc1(A[(it) & 3][m], arun + aoff[m], ws + WS_H1); \
            if ((m) == 3) { arun += ((it) == 12 ? astep13 : 1024u); asm volatile("" : "+v"(arun)); } } while (0)
        sfor<0, 3>([&](auto IT) { sfor<0, 4>([&](auto M) { ISSUE_A(IT.v, M.v); }); });
        f32x4 a[4][5];
#pragma unroll
        for (int m = 0; m < 4; ++m)
#pragma unroll
            for (int t = 0; t < 5; ++t) a[m][t] = z4;
        sfor<0, 14>([&](auto IT) {
            constexpr int it = IT.v;
            constexpr int ny = 4 * ((it + 2 < 13 ? it + 2 : 13) - it);
            if constexpr (it == 1) STAMP(25);
            if constexpr (it == 5) STAMP(26);
            if constexpr (it == 9) STAMP(27);
            if constexpr (it < 8)
                asm volatile("s_waitcnt vmcnt(%9)" : "+v"(A[it & 3][0]), "+v"(A[it & 3][1]), "+v"(A[it & 3][2]), "+v"(A[it & 3][3]),
                             "+a"(V[it][0]), "+a"(V[it][1]), "+a"(V[it][2]), "+a"(V[it][3]), "+a"(V[it][4]) : "i"(ny) : "memory");
            else
                asm volatile("s_waitcnt vmcnt(%9)" : "+v"(A[it & 3][0]), "+v"(A[it & 3][1]), "+v"(A[it & 3][2]), "+v"(A[it & 3][3]),
                             "+v"(V[it][0]), "+v"(V[it][1]), "+v"(V[it][2]), "+v"(V[it][3]), "+v"(V[it][4]) : "i"(ny) : "memory");
            SB();
            if (it < 13 || nks == 14) {
                sfor<0, 20>([&](auto I) {
                    constexpr int i = I.v, t = i >> 2, m = i & 3;
                    a[m][t] = mfma(V[it][t], A[it & 3][m], a[m][t]);
                    if constexpr ((i & 3) == 1 && i < 16 && it + 3 < 14) ISSUE_A(it + 3, i >> 2);
                    SB();
                });
            }
        });
        STAMP(11);
        asm volatile("" : "+v"(st2v));
        const f32x4 sv = __builtin_bit_cast(f32x4, st2v);
        const float smean = add_xor32(add_xor16(sv[0] + sv[2])) * (1.f / 640.f);
        const float rstd2 = __builtin_amdgcn_rsqf(fmaxf(add_xor32(add_xor16(sv[1] + sv[3])) * (1.f / 640.f) - smean * smean, 0.f) + 1e-5f);
#pragma unroll
        for (int m = 1; m < 4; ++m)
#pragma unroll
            for (int t = 0; t < 5; ++t) *(LAS f32x4*)(lb + ((((w + m) & 3) * 3 + (m - 1)) * 5 + t) * 1024 + lane * 16) = a[m][t];
        BAR();
        STAMP(46);
        f32x4 fin[5];
#pragma unroll
        for (int t = 0; t < 5; ++t) {
            const LAS char* p = (const LAS char*)(lb + (w * 15 + t) * 1024 + lane * 16);
            fin[t] = ((a[0][t] + *(const LAS f32x4*)p) + (*(const LAS f32x4*)(p + 5120) + *(const LAS f32x4*)(p + 10240))) * rstd2;
        }
        STAMP(47);
        LAS char* ot = (LAS char*)(lb + L_B + w * 2560);
#pragma unroll
        for (int t = 0; t < 5; ++t) {
            const u32x2 hp = *(LAS u32x2*)(lb + L_XRES + (16 * w + n) * 160 + 32 * t + 8 * g);
            const f32x4 ov = f32x4{bflo(hp[0]), bfhi(hp[0]), bflo(hp[1]), bfhi(hp[1])} + fin[t];
            *(LAS u32x2*)(ot + n * 160 + 32 * t + 8 * g) = pack4(ov);
        }
        asm volatile("s_waitcnt lgkmcnt(0)" ::: "memory");
        int lane3 = lane; asm volatile("" : "+v"(lane3));
#pragma unroll
        for (int u = 0; u < 3; ++u) {
            const int q = 64 * u + lane3, row = q / 10, sl = q - row * 10;
            if (q < 160) *(GAS u32x4*)(out + (size_t)(mtW * 16 + row) * 1280 + 160 * c8 + 16 * sl) = *(LAS u32x4*)(ot + q * 16);
        }
    }
    STAMP(12); STAMPRT(63);
}

extern "C" void kernel_launch(void* const* d_in, const int* in_sizes, int n_in, void* d_out, int out_size, void* d_ws, size_t ws_size,
                              hipStream_t stream) {
    (void)in_sizes; (void)n_in; (void)out_size; (void)ws_size;
    prep<<<1728, 256, 0, stream>>>((const char*)d_in[0], (const char*)d_in[1], (const char*)d_in[2], (const char*)d_in[4], (const char*)d_in[5], (char*)d_ws);
    fused_block<<<256, 256, 0, stream>>>((const char*)d_in[0], (const char*)d_in[1], (const char*)d_in[2], (const char*)d_in[4],
                                         (const char*)d_in[5], (char*)d_out, (char*)d_ws);
}
```

```cpp
#include <hip/hip_runtime.h>
#include <stdint.h>
#pragma clang diagnostic ignored "-Wint-to-pointer-cast"

typedef short s16x4 __attribute__((ext_vector_type(4)));
typedef short s16x8 __attribute__((ext_vector_type(8)));
typedef float f32x4 __attribute__((ext_vector_type(4)));
typedef unsigned u32x4 __attribute__((ext_vector_type(4)));
typedef unsigned u32x2 __attribute__((ext_vector_type(2)));
typedef unsigned long long u64;
#define LAS __attribute__((address_space(3)))
#define GAS __attribute__((address_space(1)))
#define DI __device__ __forceinline__
#define RLX_AGENT __ATOMIC_RELAXED, __HIP_MEMORY_SCOPE_AGENT
#define SB() __builtin_amdgcn_sched_barrier(0)

constexpr size_t WS_FLAG = 4096;
constexpr size_t WS_KVP = 262144;
constexpr size_t WS_STAT2 = 0x120000;
constexpr size_t WS_O = 2u << 20;
constexpr size_t WS_H = 5u << 20;
constexpr size_t WS_H1 = 8u << 20;
constexpr size_t WS_W1R = 16u << 20;
constexpr size_t WS_W2R = 19u << 20;
constexpr size_t WS_STAMP = 24u << 20;
constexpr size_t WS_XN = 28u << 20;
constexpr size_t WS_WQR = 32u << 20;
constexpr size_t WS_W0R = 36u << 20;

constexpr int L_A = 0;
constexpr int L_B = 102400;
constexpr int L_XRES = L_B + 40960;
constexpr int L_M = 155648;
constexpr int L_MEAN = L_M;
constexpr int L_RSTD = L_M + 512;
constexpr int L_CSKV = L_M + 1024;
constexpr int L_CS4 = L_M + 1280;
constexpr int L_KVT = L_M + 1536;
constexpr int L_CS1 = L_M + 3328;
constexpr int L_JUNK = L_M + 5632;
constexpr int L_ZERO = L_M + 5888;
constexpr int L_CSQ = L_M + 5904 + 16;
constexpr int L_EPOCH = L_M + 7424;
constexpr int L_TRASH = L_M + 6400;
constexpr int LDS_BYTES = 163840;
constexpr int CODE_LINES = 288;

#define STAMP(i) do {} while (0)
#define STAMPRT(i) do {} while (0)

template <int I> struct IC { static constexpr int v = I; };
template <int B, int E, class F> DI void sfor(F&& f) { if constexpr (B < E) { f(IC<B>{}); sfor<B + 1, E>(f); } }
#define OPAQUE_S(p) asm volatile("" : "+s"(p))

DI f32x4 mfma(s16x8 a, s16x8 b, f32x4 c) { return __builtin_amdgcn_mfma_f32_16x16x32_bf16(a, b, c, 0, 0, 0); }
DI s16x4 trread(unsigned addr) { return __builtin_bit_cast(s16x4, __builtin_amdgcn_ds_read_tr16_b64_v4i16((LAS s16x4*)addr)); }
DI s16x8 cat(s16x4 a, s16x4 b) { return __builtin_shufflevector(a, b, 0, 1, 2, 3, 4, 5, 6, 7); }
DI unsigned pk2(float lo, float hi) {
    typedef __bf16 bf2 __attribute__((ext_vector_type(2)));
    typedef float f2 __attribute__((ext_vector_type(2)));
    f2 v = {lo, hi};
    return __builtin_bit_cast(unsigned, __builtin_convertvector(v, bf2));
}
DI s16x8 pack8(f32x4 a, f32x4 b) {
    u32x4 r = {pk2(a[0], a[1]), pk2(a[2], a[3]), pk2(b[0], b[1]), pk2(b[2], b[3])};
    return __builtin_bit_cast(s16x8, r);
}
DI u32x2 pack4(f32x4 a) { u32x2 r = {pk2(a[0], a[1]), pk2(a[2], a[3])}; return r; }
DI float bflo(unsigned u) { return __uint_as_float(u << 16); }
DI float bfhi(unsigned u) { return __uint_as_float(u & 0xffff0000u); }
DI float phi(float t) { return (fmaxf(t, 0.f) + 1e-6f) + __builtin_amdgcn_fmed3f(__builtin_amdgcn_exp2f(t * 1.44269504088896f), 0.f, 1.f); }
DI float add_xor16(float v) { const unsigned u = __float_as_uint(v); auto r = __builtin_amdgcn_permlane16_swap(u, u, false, false); return __uint_as_float(r[0]) + __uint_as_float(r[1]); }
DI float add_xor32(float v) { const unsigned u = __float_as_uint(v); auto r = __builtin_amdgcn_permlane32_swap(u, u, false, false); return __uint_as_float(r[0]) + __uint_as_float(r[1]); }
constexpr int wq_pos(int grp) { return grp < 3 ? 16384 + 5120 * grp : grp < 7 ? 32768 + 5120 * (grp - 3) : 5120 * (grp - 7); }

template <int LOFF> DI void glds16(unsigned voff, const void* sbase, unsigned lds) {
    asm volatile("s_add_u32 m0, %2, %3\n\ts_nop 0\n\tglobal_load_lds_dwordx4 %0, %1" ::"v"(voff), "s"(sbase), "s"(lds), "i"(LOFF) : "memory", "scc");
}
template <int LOFF, int GOFF> DI void glds16i(unsigned voff, const void* sbase, unsigned lds) {
    asm volatile("s_add_u32 m0, %2, %3\n\ts_nop 0\n\tglobal_load_lds_dwordx4 %0, %1 offset:%4" ::"v"(voff), "s"(sbase), "s"(lds), "i"(LOFF - GOFF), "i"(GOFF) : "memory", "scc");
}
template <int LOFF> DI void glds16c(unsigned voff, const void* sbase, unsigned lds) {
    asm volatile("s_add_u32 m0, %2, %3\n\ts_nop 0\n\tglobal_load_lds_dwordx4 %0, %1 sc1" ::"v"(voff), "s"(sbase), "s"(lds), "i"(LOFF) : "memory", "scc");
}
DI void glds4(unsigned voff, const void* sbase, unsigned lds) {
    asm volatile("s_mov_b32 m0, %2\n\ts_nop 0\n\tglobal_load_lds_dword %0, %1" ::"v"(voff), "s"(sbase), "s"(lds) : "memory");
}
#define WAITV(n) asm volatile("s_waitcnt vmcnt(%0)" ::"i"(n) : "memory")
#define BARX() do { __builtin_amdgcn_s_barrier(); asm volatile("" ::: "memory"); } while (0)
#define BAR() do { asm volatile("s_waitcnt lgkmcnt(0)" ::: "memory"); __builtin_amdgcn_s_barrier(); asm volatile("" ::: "memory"); } while (0)

DI void ld16_sc1(s16x8& d, unsigned voff, const void* sbase) {
    asm volatile("global_load_dwordx4 %0, %1, %2 sc1" : "=v"(d) : "v"(voff), "s"(sbase) : "memory");
}
DI void ld16(s16x8& d, unsigned voff, const void* sbase) {
    asm volatile("global_load_dwordx4 %0, %1, %2" : "=v"(d) : "v"(voff), "s"(sbase) : "memory");
}
DI void ld16a(s16x8& d, unsigned voff, const void* sbase) {
    asm volatile("global_load_dwordx4 %0, %1, %2" : "=a"(d) : "v"(voff), "s"(sbase) : "memory");
}
DI void ld8_sc1(u64& d, unsigned voff, const void* sbase) { asm volatile("global_load_dwordx2 %0, %1, %2 sc1" : "=v"(d) : "v"(voff), "s"(sbase) : "memory"); }
DI void st16_sc1(unsigned voff, void* sbase, u32x4 v) {
    asm volatile("global_store_dwordx4 %0, %1, %2 sc1\n\ts_nop 1" ::"v"(voff), "v"(v), "s"(sbase) : "memory");
}
DI void st8_sc1(unsigned voff, void* sbase, u32x2 v) {
    asm volatile("global_store_dwordx2 %0, %1, %2 sc1" ::"v"(voff), "v"(v), "s"(sbase) : "memory");
}
DI void drain_bar() { WAITV(0); BAR(); }
DI void flags_store(char* ws, int arr, int line, int slot, u64 tag) {
    __hip_atomic_store((GAS u64*)(ws + WS_FLAG + (size_t)arr * 32768 + (size_t)line * 128) + slot, tag, RLX_AGENT);
}
template <int NV> DI void await(char* ws, int arr, int line, u64 tag, int w, int lane) {
    if (w == 0) {
        GAS u64* fl = (GAS u64*)(ws + WS_FLAG + (size_t)arr * 32768 + (size_t)line * 128) + (lane & 15);
        for (unsigned spins = 0; spins < (1u << 20); ++spins) {
            u64 f = __hip_atomic_load(fl, RLX_AGENT);
            if (__all(f == tag)) break;
        }
    }
    WAITV(NV);
    BAR();
}

__global__ void __launch_bounds__(256)
prep(const char* __restrict__ x, const char* __restrict__ wqkv, const char* __restrict__ wo, const char* __restrict__ w1,
     const char* __restrict__ w2, char* __restrict__ ws) {
    __shared__ __attribute__((aligned(16))) char smem[20992];
    const unsigned lb = (unsigned)(uintptr_t)(LAS char*)smem;
    const int tid = threadIdx.x, lane = tid & 63, w = __builtin_amdgcn_readfirstlane(tid >> 6);
    const int g = lane >> 4, n = lane & 15, q4 = n >> 2, p4 = lane & 3;
    const int u = blockIdx.x;
    if (u < 128) {
        const int row = tid >> 4, c = tid & 15;
        u32x4 d[5];
#pragma unroll
        for (int i = 0; i < 5; ++i) d[i] = *(const GAS u32x4*)(x + (size_t)(u * 16 + row) * 1280 + (c + 16 * i) * 16);
        float v[40], su = 0.f;
#pragma unroll
        for (int i = 0; i < 5; ++i)
#pragma unroll
            for (int j = 0; j < 4; ++j) { v[8 * i + 2 * j] = bflo(d[i][j]); v[8 * i + 2 * j + 1] = bfhi(d[i][j]); su += v[8 * i + 2 * j] + v[8 * i + 2 * j + 1]; }
#pragma unroll
        for (int m = 1; m < 16; m <<= 1) su += __shfl_xor(su, m);
        const float mean = su * (1.f / 640.f);
        float s2 = 0.f;
#pragma unroll
        for (int i = 0; i < 40; ++i) { v[i] -= mean; s2 += v[i] * v[i]; }
#pragma unroll
        for (int m = 1; m < 16; m <<= 1) s2 += __shfl_xor(s2, m);
        const float rstd = __builtin_amdgcn_rsqf(s2 * (1.f / 640.f) + 1e-5f);
#pragma unroll
        for (int i = 0; i < 5; ++i)
            *(LAS u32x4*)(lb + row * 1296 + (c + 16 * i) * 16) = u32x4{pk2(v[8 * i] * rstd, v[8 * i + 1] * rstd), pk2(v[8 * i + 2] * rstd, v[8 * i + 3] * rstd),
                                                                      pk2(v[8 * i + 4] * rstd, v[8 * i + 5] * rstd), pk2(v[8 * i + 6] * rstd, v[8 * i + 7] * rstd)};
        __syncthreads();
#pragma unroll
        for (int k = 0; k < 5; ++k) {
            const int ks = 5 * w + k;
            const u32x2 lo = *(LAS u32x2*)(lb + n * 1296 + 64 * ks + 8 * g), hi = *(LAS u32x2*)(lb + n * 1296 + 64 * ks + 32 + 8 * g);
            *(GAS u32x4*)(ws + WS_XN + (size_t)(u * 20 + ks) * 1024 + lane * 16) = u32x4{lo[0], lo[1], hi[0], hi[1]};
        }
    } else if (u < 448) {
        const int v_ = u - 128, j = v_ / 20, ks = v_ - 20 * j;
#pragma unroll
        for (int r = 0; r < 2; ++r) {
            const int idx = tid + 256 * r;
            if (idx < 480) {
                const int row = idx / 15, sl = idx - 15 * row, sec = sl / 5;
                *(LAS u32x4*)(lb + row * 272 + sl * 16) = *(const GAS u32x4*)(wqkv + (size_t)(32 * ks + row) * 3840 + sec * 1280 + 80 * j + (sl - 5 * sec) * 16);
            }
        }
        __syncthreads();
#pragma unroll
        for (int k = 0; k < 2; ++k) {
            const int t = 2 * w + k;
            const int off = t == 0 ? 80 + 8 * p4 : t == 1 ? 120 + 8 * p4 : t == 2 ? 160 + 8 * p4 : t == 3 ? 200 + 8 * p4
                          : t == 4 ? (p4 == 0 ? 112 : p4 == 1 ? 152 : p4 == 2 ? 192 : 232) : t == 5 ? 8 * p4 : t == 6 ? 40 + 8 * p4 : ((p4 & 1) ? 72 : 32);
            const unsigned a_ = lb + (4 * g + q4) * 272 + off;
            s16x8 f = cat(trread(a_), trread(a_ + 16 * 272));
            if (t == 7 && n >= 8) f = s16x8{0, 0, 0, 0, 0, 0, 0, 0};
            *(GAS s16x8*)(ws + WS_WQR + (size_t)((j * 20 + ks) * 8 + t) * 1024 + lane * 16) = f;
        }
    } else {
        const int row = tid >> 3, sl = tid & 7;
        u32x4 d[5];
        char* dst[5];
#pragma unroll
        for (int r = 0; r < 5; ++r) {
            const int id = u - 448 + 256 * r;
            const char* src;
            if (id < 200) {
                const int ks = id / 10, cl = id - 10 * ks;
                const int srow = ks < 16 ? 40 * ks + (row < 16 ? row : row + 4) : 20 * (4 * (2 * (ks - 16) + (row >> 4)) + ((row & 15) >> 2)) + 16 + (row & 3);
                src = wo + (size_t)srow * 1280 + cl * 128;
                const int T = 4 * cl + w, c8r = T / 5;
                dst[r] = ws + WS_W0R + (size_t)((c8r * 20 + ks) * 5 + (T - 5 * c8r)) * 1024;
            } else if (id < 740) {
                const int v_ = id - 200, ks = v_ / 27, cl = v_ - 27 * ks;
                src = w1 + (size_t)(32 * ks + row) * 3456 + cl * 128;
                dst[r] = ws + WS_W1R + (size_t)((4 * cl + w) * 20 + ks) * 1024;
            } else {
                const int v_ = id - 740, ks = v_ / 10, cl = v_ - 10 * ks;
                src = w2 + (size_t)(32 * ks + row) * 1280 + cl * 128;
                const int T = 4 * cl + w, c8r = T / 5;
                dst[r] = ws + WS_W2R + (size_t)((c8r * 54 + ks) * 5 + (T - 5 * c8r)) * 1024;
            }
            d[r] = *(const GAS u32x4*)(src + sl * 16);
        }
#pragma unroll
        for (int r = 0; r < 5; ++r) *(LAS u32x4*)(lb + r * 4096 + row * 128 + 16 * (sl ^ (((row >> 1) & 3) << 1))) = d[r];
        __syncthreads();
        const int rw = 4 * g + q4;
        const unsigned a_ = lb + rw * 128 + 16 * ((2 * w + (p4 >> 1)) ^ (((rw >> 1) & 3) << 1)) + 8 * (p4 & 1);
#pragma unroll
        for (int r = 0; r < 5; ++r) *(GAS s16x8*)(dst[r] + lane * 16) = cat(trread(a_ + r * 4096), trread(a_ + r * 4096 + 2048));
    }
}

__global__ void __launch_bounds__(256, 1)
fused_block(const char* __restrict__ x, const char* __restrict__ wqkv, const char* __restrict__ wo, const char* __restrict__ w1,
            const char* __restrict__ w2, char* __restrict__ out, char* __restrict__ ws) {
    __shared__ __attribute__((aligned(16))) char smem[LDS_BYTES];
    const unsigned lb = (unsigned)(uintptr_t)(LAS char*)smem;
    const int tid = threadIdx.x, lane = tid & 63;
    const int w = __builtin_amdgcn_readfirstlane(tid >> 6);
    const int b = blockIdx.x, x8 = b & 7, rr = b >> 3;
    const int i1 = 4 * (x8 >> 1) + (rr >> 3), j1 = 8 * (x8 & 1) + (rr & 7);
    const int i2 = i1, j2 = j1;
    const int half = (rr >> 2) & 1, c8 = 4 * (x8 & 1) + (rr & 3), jw = 8 * half + c8;
    const int g = lane >> 4, n = lane & 15, q4 = n >> 2, p4 = lane & 3;
    const s16x8 ones = {0x3F80, 0x3F80, 0x3F80, 0x3F80, 0x3F80, 0x3F80, 0x3F80, 0x3F80};
    const f32x4 z4 = {0, 0, 0, 0};
    STAMP(0); STAMPRT(62);

    {
        u64 pc;
        asm volatile("s_getpc_b64 %0" : "=s"(pc));
        const unsigned cl_ = (rr * 4 + w) * 3 + ((lane & 3) < 3 ? (lane & 3) : 0);
        glds4((cl_ < CODE_LINES ? cl_ : CODE_LINES - 1) * 128, (const void*)(pc & ~(u64)127), lb + L_JUNK);
    }
    const unsigned vlan16 = lane * 16;

    f32x4 acc[8][2];
    {
        s16x8 X[20][2];
        const char* wsrc = ws + WS_WQR + (size_t)(j1 * 20 * 8 + 2 * w) * 1024;
        unsigned xrun = (unsigned)((8 * i1 + 2 * w) * 20 * 1024) + vlan16;
        const unsigned ldsW = lb + w * 2048;
        auto issue = [&](auto KS) {
            constexpr int ks = KS.v;
            glds16<(ks % 18) * 8192>(vlan16, wsrc, ldsW);
            glds16<(ks % 18) * 8192 + 1024>(vlan16 + 1024, wsrc, ldsW);
            wsrc += 8192; OPAQUE_S(wsrc);
            ld16(X[ks][0], xrun, ws + WS_XN);
            ld16(X[ks][1], xrun + 20 * 1024, ws + WS_XN);
            xrun += 1024; asm volatile("" : "+v"(xrun));
        };
        sfor<0, 8>([&](auto KS) { issue(KS); });
        asm volatile("s_mov_b32 m0, %2\n\ts_nop 0\n\tglobal_load_lds_dword %0, %1 sc1" ::"v"((lane & 1) * 4), "s"(ws), "s"(lb + L_EPOCH) : "memory");
        unsigned vW0 = lb + vlan16, vW1 = lb + 65536 + vlan16, vW2 = lb + 131072 + vlan16;
        asm volatile("" : "+v"(vW0), "+v"(vW1), "+v"(vW2));
        auto wread = [&](auto KS, auto TT) -> s16x8 {
            constexpr int o = (KS.v % 18) * 8192 + TT.v * 1024;
            return *(volatile LAS s16x8*)((o < 65536 ? vW0 : o < 131072 ? vW1 : vW2) + (o & 65535));
        };
#pragma unroll
        for (int t = 0; t < 8; ++t) acc[t][0] = acc[t][1] = z4;
        s16x8 Wf[2][8];
        WAITV(4 * 6 + 1);
        BARX();
        sfor<0, 8>([&](auto TT) { Wf[0][TT.v] = wread(IC<0>{}, TT); });
        SB();
        sfor<0, 20>([&](auto KS) {
            constexpr int ks = KS.v;
            if constexpr ((ks & 1) == 0) {
                constexpr int issued = 4 * ((ks + 7 < 19 ? ks + 7 : 19) + 1), need = 4 * (ks + 3 < 20 ? ks + 3 : 20);
                WAITV(issued - need + (ks + 2 < 8 ? 1 : 0));
                BARX();
                SB();
            }
            asm volatile("" : "+v"(X[ks][0]), "+v"(X[ks][1]));
            sfor<0, 16>([&](auto I) {
                constexpr int i = I.v, t = i >> 1, m = i & 1;
                if constexpr (t < 5) acc[t][m] = mfma(X[ks][m], Wf[ks & 1][t], acc[t][m]);
                else acc[t][m] = mfma(Wf[ks & 1][t], X[ks][m], acc[t][m]);
                if constexpr (ks < 19 && (i & 1) == 0) Wf[(ks + 1) & 1][t] = wread(IC<ks + 1>{}, IC<t>{});
                if constexpr (i == 5 && ks + 8 < 20) issue(IC<(ks + 8 < 20 ? ks + 8 : 19)>{});
                SB();
            });
        });
    }
    const u64 tag = *(volatile LAS u64*)(lb + L_EPOCH) + 1;
    STAMP(1);

    {
        const unsigned red = lb + 16384 + w * 3360, trash = lb + L_TRASH + vlan16;
        const unsigned r0 = red + (n * 20 + 4 * g) * 4;
        const unsigned r1 = (n == 0 || (n >= 8 && n < 12)) ? red + ((n == 0 ? 20 : 8 + n) * 20 + 4 * g) * 4 : trash;
        const unsigned r2 = (n == 0 || n >= 12) ? red + (420 + (n == 0 ? 20 : 4 + n) * 20 + 4 * g) * 4 : trash;
        const unsigned r3 = g < 2 ? red + (420 * g + n * 20 + 16) * 4 : trash;
        const unsigned r4 = (g < 2 && (n == 0 || (g == 0 ? (n >= 8 && n < 12) : n >= 12))) ? red + (420 * g + (n == 0 ? 20 : (g == 0 ? 8 + n : 4 + n)) * 20 + 16) * 4 : trash;
        s16x8 F[4], MA, MB;
        sfor<0, 5>([&](auto TT) {
            constexpr int t = TT.v;
            f32x4 v[2], vb_[2];
#pragma unroll
            for (int m = 0; m < 2; ++m)
#pragma unroll
                for (int r = 0; r < 4; ++r) {
                    const float y = acc[t][m][r];
                    if (t < 2) v[m][r] = phi(y);
                    else if (t < 4) v[m][r] = y;
                    else { v[m][r] = n < 8 ? phi(y) : y; vb_[m][r] = n < 8 ? 1.f : y; }
                }
            if constexpr (t < 4) F[t] = pack8(v[0], v[1]);
            else { MA = pack8(v[0], v[1]); MB = pack8(vb_[0], vb_[1]); }
        });
        STAMP(33);
        f32x4 P1 = mfma(F[0], F[2], z4), P2 = mfma(F[0], MB, z4), P3 = mfma(MA, F[2], z4), P4 = mfma(MA, MB, z4);
        f32x4 P5 = mfma(F[1], F[3], z4), P6 = mfma(F[1], MB, z4), P7 = mfma(MA, F[3], z4);
        *(LAS f32x4*)(r0) = P1;
        *(LAS f32x4*)(r0 + 1680) = P5;
        *(LAS f32x4*)(r1) = P2;
        *(LAS f32x4*)(r2) = P6;
        *(LAS f32x4*)(r3) = (g == 0 ? P3 : P7);
        *(LAS f32x4*)(r4) = P4;
        if (tid == 0) { *(LAS u64*)(lb + L_ZERO) = 0; *(LAS u64*)(lb + L_ZERO + 8) = 0; }
    }
    STAMP(34);
    BAR();
    if (tid < 210) {
        LAS char* red = (LAS char*)(lb + 16384 + tid * 16);
        f32x4 s = (*(LAS f32x4*)(red) + *(LAS f32x4*)(red + 3360)) + (*(LAS f32x4*)(red + 6720) + *(LAS f32x4*)(red + 10080));
        st16_sc1((unsigned)(tid * 16), ws + WS_KVP + (size_t)(j1 * 16 + i1) * 3360, __builtin_bit_cast(u32x4, s));
    }
    STAMP(36);
    drain_bar();
    if (tid < 16) flags_store(ws, 0, tid * 16 + j1, i1, tag);
    STAMP(2);

    {
        const char* s0 = ws + WS_W0R + (size_t)(c8 * 100 + w) * 1024;
        sfor<0, 25>([&](auto U) { glds16<U.v * 4096>(vlan16, s0, lb + w * 1024); s0 += 4096; OPAQUE_S(s0); });
        if (w < 2) {
            int lane2 = lane; asm volatile("" : "+v"(lane2));
            sfor<0, 5>([&](auto U) { const int q = 64 * U.v + lane2, row = q / 10, sl = q - row * 10;
                glds16<U.v * 1024>((unsigned)(row * 1280 + 160 * c8 + 16 * sl), x + (size_t)(8 * i2 + 4 * half + 2 * w) * 16 * 1280, lb + L_XRES + w * 5120); });
        }
    }
    s16x8 Fa[2], Fb[2];
#pragma unroll
    for (int m = 0; m < 2; ++m) {
        f32x4 v[3];
#pragma unroll
        for (int t = 0; t < 3; ++t)
#pragma unroll
            for (int r = 0; r < 4; ++r) v[t][r] = phi(acc[5 + t][m][r]);
        u32x4 u2 = __builtin_bit_cast(u32x4, pack8(v[0], v[2])), u3 = __builtin_bit_cast(u32x4, pack8(v[1], v[2]));
        if (g >= 2) { u2[2] = u2[3] = 0u; u3[2] = u3[3] = 0u; }
        Fa[m] = __builtin_bit_cast(s16x8, u2); Fb[m] = __builtin_bit_cast(s16x8, u3);
    }
    unsigned oa[12];
    {
        const unsigned kt = lb + L_KVT, zz = lb + L_ZERO;
        auto ka = [&](int head, int e, int c) { return kt + (head * 420 + e * 20 + 4 * c) * 2; };
        oa[0] = ka(0, n, g);  oa[1] = g == 0 ? ka(0, n, 4) : zz;
        oa[2] = ka(1, n, g);  oa[3] = g == 1 ? ka(1, n, 4) : zz;
        oa[4] = ka(0, 20, g); oa[5] = g == 0 ? ka(0, 20, 4) : zz;
        oa[6] = ka(1, 20, g); oa[7] = g == 1 ? ka(1, 20, 4) : zz;
        oa[8] = n < 4 ? ka(0, 16 + n, g) : zz;                oa[9] = (n < 4 && g == 0) ? ka(0, 16 + n, 4) : zz;
        oa[10] = (n >= 4 && n < 8) ? ka(1, 12 + n, g) : zz;   oa[11] = (n >= 4 && n < 8 && g == 1) ? ka(1, 12 + n, 4) : zz;
    }
    asm volatile("" : "+v"(Fa[0]), "+v"(Fa[1]), "+v"(Fb[0]), "+v"(Fb[1]), "+v"(oa[0]), "+v"(oa[1]), "+v"(oa[2]), "+v"(oa[3]), "+v"(oa[4]), "+v"(oa[5]),
                 "+v"(oa[6]), "+v"(oa[7]), "+v"(oa[8]), "+v"(oa[9]), "+v"(oa[10]), "+v"(oa[11]));
    STAMP(3);

    await<63>(ws, 0, i1 * 16 + j1, tag, w, lane);
    STAMP(4);
    {
        s16x8 kp[16];
        const char* kb = ws + WS_KVP + (size_t)j1 * 16 * 3360;
        const unsigned kvo = (tid < 210 ? tid : 209) * 16;
#pragma unroll
        for (int i = 0; i < 16; ++i) ld16_sc1(kp[i], kvo + i * 3360, kb);
        asm volatile("s_waitcnt vmcnt(0)" : "+v"(kp[0]), "+v"(kp[1]), "+v"(kp[2]), "+v"(kp[3]), "+v"(kp[4]), "+v"(kp[5]), "+v"(kp[6]), "+v"(kp[7]),
                     "+v"(kp[8]), "+v"(kp[9]), "+v"(kp[10]), "+v"(kp[11]), "+v"(kp[12]), "+v"(kp[13]), "+v"(kp[14]), "+v"(kp[15])::"memory");
        f32x4 s0 = __builtin_bit_cast(f32x4, kp[0]), s1 = __builtin_bit_cast(f32x4, kp[1]);
#pragma unroll
        for (int i = 2; i < 16; i += 2) { s0 += __builtin_bit_cast(f32x4, kp[i]); s1 += __builtin_bit_cast(f32x4, kp[i + 1]); }
        if (tid < 210) *(LAS u32x2*)(lb + L_KVT + tid * 8) = pack4(s0 + s1);
    }
    STAMP(38);
    BAR();
    STAMP(39);
    {
        auto rd2 = [&](unsigned a0, unsigned a1) { const u32x2 lo = *(LAS u32x2*)a0, hi = *(LAS u32x2*)a1; return __builtin_bit_cast(s16x8, u32x4{lo[0], lo[1], hi[0], hi[1]}); };
        const s16x8 Ama = rd2(oa[0], oa[1]), Amb = rd2(oa[2], oa[3]), Aza = rd2(oa[4], oa[5]), Azb = rd2(oa[6], oa[7]), Ata = rd2(oa[8], oa[9]), Atb = rd2(oa[10], oa[11]);
#pragma unroll
        for (int m = 0; m < 2; ++m) {
            f32x4 Da = mfma(Ama, Fa[m], z4), Db = mfma(Amb, Fb[m], z4);
            f32x4 Dt = mfma(Atb, Fb[m], mfma(Ata, Fa[m], z4));
            f32x4 Dza = mfma(Aza, Fa[m], z4), Dzb = mfma(Azb, Fb[m], z4);
            const float ra = __builtin_amdgcn_rcpf(Dza[0] + 1e-6f), rb = __builtin_amdgcn_rcpf(Dzb[0] + 1e-6f);
            const int mt = 8 * i1 + 2 * w + m;
            s16x8 of = pack8(Da * ra, Db * rb);
            st16_sc1((unsigned)((mt * 20 + j1) * 1024 + lane * 16), ws + WS_O, __builtin_bit_cast(u32x4, of));
            u32x2 tl = pack4(Dt * (g == 0 ? ra : rb));
            const int t = j1 >> 1;
            if (g < 2) st8_sc1((unsigned)((mt * 20 + 16 + (t >> 1)) * 1024 + (n + 16 * (2 * (j1 & 1) + g)) * 16 + (t & 1) * 8), ws + WS_O, tl);
        }
    }
    STAMP(40);
    drain_bar();
    if (tid < 16) flags_store(ws, 1, i1 * 16 + tid, j1, tag);
    STAMP(5);

    const int T0 = j2 < 12 ? 7 * j2 : 84 + 6 * (j2 - 12);
    const int odd = (j2 < 12) ? (j2 & 1) : 0;
    const int Tp0 = T0 + odd;
    const int Tsg = j2 < 12 ? (odd ? T0 : T0 + 6) : Tp0;
    const int ksb = w < 2 ? 14 * w : 28 + 13 * (w - 2);
    const int nks = w < 2 ? 14 : 13;
    s16x8 V[14][5];
    const char* w2run = ws + WS_W2R + (size_t)(c8 * 54 + ksb) * 5120;
    const int step13 = nks == 14 ? 5120 : 0;
#define ISSUE_V(it, t) do { if constexpr ((it) < 8) ld16a(V[it][t], vlan16 + (t) * 1024, w2run); else ld16(V[it][t], vlan16 + (t) * 1024, w2run); \
            if ((t) == 4) { w2run += ((it) == 12 ? step13 : 5120); OPAQUE_S(w2run); } } while (0)
    sfor<0, 10>([&](auto P) {
        constexpr int bg = P.v;
        const int Tg = bg < 5 ? Tp0 + 5 : Tsg;
        glds16<(5 + bg / 5) * 20480 + (bg % 5) * 4096>(vlan16, ws + WS_W1R + (size_t)(Tg * 20 + (bg % 5) * 4 + w) * 1024, lb + w * 1024);
    });
    sfor<0, 10>([&](auto P) { ISSUE_V(P.v / 5, P.v % 5); });
    const int mtW = 8 * i2 + 4 * half + w;
    STAMP(45);
    await<63>(ws, 1, i2 * 16 + jw, tag, w, lane);
    STAMP(6);
    {
        s16x8 O[20];
        unsigned orun = (unsigned)(mtW * 20 * 1024 + lane * 16);
#define ISSUE_O(i) do { ld16_sc1(O[i], orun, ws + WS_O); orun += 1024u; asm volatile("" : "+v"(orun)); } while (0)
        sfor<0, 8>([&](auto I) { ISSUE_O(I.v); });
        unsigned vA = lb + L_A + vlan16, vA2 = vA + 61440;
        asm volatile("" : "+v"(vA), "+v"(vA2));
        f32x4 a[5];
#pragma unroll
        for (int t = 0; t < 5; ++t) a[t] = z4;
        s16x8 Wf[3][5];
        auto wrd = [&](auto I, auto T) -> s16x8 { constexpr int o = (5 * I.v + T.v) * 1024; return *(volatile LAS s16x8*)((o < 61440 ? vA : vA2) + (o < 61440 ? o : o - 61440)); };
        sfor<0, 2>([&](auto I) { sfor<0, 5>([&](auto T) { Wf[I.v][T.v] = wrd(I, T); }); });
        SB();
        sfor<0, 20>([&](auto I) {
            constexpr int i = I.v;
            asm volatile("s_waitcnt vmcnt(%1)" : "+v"(O[i]) : "i"((i + 7 < 19 ? i + 7 : 19) - i) : "memory");
            SB();
            sfor<0, 5>([&](auto T) {
                constexpr int t = T.v;
                a[t] = mfma(Wf[i % 3][t], O[i], a[t]);
                if constexpr (i + 2 < 20) Wf[(i + 2) % 3][t] = wrd(IC<i + 2>{}, T);
                if constexpr (t == 1 && i + 8 < 20) ISSUE_O(i + 8);
                SB();
            });
        });
        STAMP(28);
        float su = 0.f, s2 = 0.f;
        u32x2 hpk[5];
#pragma unroll
        for (int t = 0; t < 5; ++t) {
            const u32x2 xr = *(LAS u32x2*)(lb + L_XRES + (16 * w + n) * 160 + 32 * t + 8 * g);
            const f32x4 hv = f32x4{bflo(xr[0]), bfhi(xr[0]), bflo(xr[1]), bfhi(xr[1])} + a[t];
            hpk[t] = pack4(hv);
            *(LAS u32x2*)(lb + L_XRES + (16 * w + n) * 160 + 32 * t + 8 * g) = hpk[t];
#pragma unroll
            for (int r = 0; r < 4; ++r) { su += hv[r]; s2 += hv[r] * hv[r]; }
        }
        const unsigned hb = (unsigned)((mtW * 20 + ((5 * c8) >> 1)) * 1024 + lane * 16);
        const int od = c8 & 1;
        st8_sc1(hb + (od ? 8 : 2048), ws + WS_H, od ? hpk[0] : hpk[4]);
        { const u32x2 p0 = od ? hpk[1] : hpk[0], p1 = od ? hpk[2] : hpk[1]; st16_sc1(hb + (od ? 1024 : 0), ws + WS_H, u32x4{p0[0], p0[1], p1[0], p1[1]}); }
        { const u32x2 p0 = od ? hpk[3] : hpk[2], p1 = od ? hpk[4] : hpk[3]; st16_sc1(hb + (od ? 2048 : 1024), ws + WS_H, u32x4{p0[0], p0[1], p1[0], p1[1]}); }
        su = add_xor32(add_xor16(su)); s2 = add_xor32(add_xor16(s2));
        if (g == 0) { u32x2 st = {__float_as_uint(su), __float_as_uint(s2)}; st8_sc1((unsigned)((mtW * 16 + n) * 64 + c8 * 8), ws + WS_STAT2, st); }
    }
    STAMP(42);
    drain_bar();
    if (tid < 16) flags_store(ws, 2, i2 * 16 + tid, jw, tag);
    if (b == 0 && tid == 0) __hip_atomic_store((GAS u64*)ws, tag, RLX_AGENT);
    STAMP(7);

    {
        const char* s1 = ws + WS_W1R + (size_t)(Tp0 * 20 + w) * 1024;
        const unsigned l1 = lb + L_A + w * 1024;
        sfor<0, 25>([&](auto U) { glds16<U.v * 4096>(vlan16, s1, l1); s1 += 4096; OPAQUE_S(s1); });
    }
    STAMP(43);
    await<0>(ws, 2, i2 * 16 + j2, tag, w, lane);
    STAMP(8);
    {
        s16x8 H[20][2];
        const int mt0 = 8 * i2 + 2 * w;
        s16x8 stv[2];
#pragma unroll
        for (int m = 0; m < 2; ++m) ld16_sc1(stv[m], (unsigned)(((mt0 + m) * 16 + n) * 64 + g * 16), ws + WS_STAT2);
        unsigned hrun = (unsigned)(mt0 * 20 * 1024 + lane * 16);
#define ISSUE_H(ks) do { ld16_sc1(H[ks][0], hrun, ws + WS_H); ld16_sc1(H[ks][1], hrun + 20 * 1024, ws + WS_H); hrun += 1024; asm volatile("" : "+v"(hrun)); } while (0)
        unsigned vF0 = lb + vlan16, vF1 = lb + 61440 + vlan16, vF2 = lb + 122880 + vlan16;
        asm volatile("" : "+v"(vF0), "+v"(vF1), "+v"(vF2));
        auto wread = [&](auto TT, auto KS) -> s16x8 {
            constexpr int t = TT.v, ks = KS.v;
            return *(volatile LAS s16x8*)((t < 3 ? vF0 : t < 6 ? vF1 : vF2) + (t % 3) * 20480 + ks * 1024);
        };
        {
            f32x4 c0 = z4, c1 = z4;
            const unsigned ta = lb + vlan16 + w * 20480, tb = lb + vlan16 + (w < 3 ? w + 4 : w) * 20480;
            s16x8 Ca[4], Cb[4];
            sfor<0, 3>([&](auto K) { Ca[K.v] = *(volatile LAS s16x8*)(ta + K.v * 1024); Cb[K.v] = *(volatile LAS s16x8*)(tb + K.v * 1024); });
            SB();
            sfor<0, 20>([&](auto K) {
                constexpr int k = K.v;
                c0 = mfma(ones, Ca[k & 3], c0);
                if constexpr (k + 3 < 20) Ca[(k + 3) & 3] = *(volatile LAS s16x8*)(ta + (k + 3) * 1024);
                SB();
                c1 = mfma(ones, Cb[k & 3], c1);
                if constexpr (k + 3 < 20) Cb[(k + 3) & 3] = *(volatile LAS s16x8*)(tb + (k + 3) * 1024);
                if constexpr ((k & 1) == 0 && k < 16) ISSUE_H(k >> 1);
                SB();
            });
            auto split = [&](float c) { const unsigned hi = pk2(c, c); const float lo = c - bflo(hi); u32x2 r = {hi, pk2(lo, lo)}; return r; };
            if (g == 0) {
                *(LAS u32x2*)(lb + L_CS1 + w * 128 + n * 8) = split(c0[0]);
                if (w < 3) *(LAS u32x2*)(lb + L_CS1 + (w + 4) * 128 + n * 8) = split(c1[0]);
            }
            if (tid < 2) *(LAS u64*)(lb + L_CS1 + 896 + tid * 8) = 0;
        }
        STAMP(19);
        asm volatile("s_waitcnt vmcnt(16)" : "+v"(stv[0]), "+v"(stv[1])::"memory");
        s16x8 Hm[2];
#pragma unroll
        for (int m = 0; m < 2; ++m) {
            f32x4 s = __builtin_bit_cast(f32x4, stv[m]);
            const float nm = add_xor32(add_xor16(s[0] + s[2])) * (-1.f / 640.f);
            const unsigned hi = pk2(nm, nm);
            const unsigned pr = g == 0 ? ((hi & 0xffffu) | (pk2(nm - bflo(hi), 0.f) << 16)) : 0u;
            Hm[m] = __builtin_bit_cast(s16x8, u32x4{pr, pr, 0u, 0u});
        }
        s16x8 Wf1[3][4];
        sfor<0, 2>([&](auto KS) { sfor<0, 4>([&](auto TT) { Wf1[KS.v][TT.v] = wread(TT, KS); }); });
        __builtin_amdgcn_s_waitcnt(0xC87F);
        BARX();
        const unsigned csa = lb + L_CS1 + (g == 0 ? n * 8 : 896);
        auto csfrag = [&](int t) { const u32x2 c = *(LAS u32x2*)(csa + (g == 0 ? t * 128 : 0)); return __builtin_bit_cast(s16x8, u32x4{c[0], c[1], 0u, 0u}); };
        auto relu2 = [&](unsigned p) { typedef short s2_ __attribute__((ext_vector_type(2))); const s2_ z = {0, 0};
                                       return __builtin_bit_cast(unsigned, __builtin_elementwise_max(__builtin_bit_cast(s2_, p), z)); };
        auto fin8 = [&](f32x4 x0, f32x4 x1) { return u32x4{relu2(pk2(x0[0], x0[1])), relu2(pk2(x0[2], x0[3])), relu2(pk2(x1[0], x1[1])), relu2(pk2(x1[2], x1[3]))}; };
        f32x4 a1[4][2];
        sfor<0, 4>([&](auto TT) { const s16x8 cf = csfrag(TT.v); a1[TT.v][0] = mfma(cf, Hm[0], z4); a1[TT.v][1] = mfma(cf, Hm[1], z4); });
        {
            auto& Wf = Wf1;
            SB();
            sfor<0, 20>([&](auto KS) {
                constexpr int ks = KS.v;
                constexpr int ny = 2 * ((ks + 7 < 19 ? ks + 7 : 19) - ks) + (ks > 12 ? (ks < 17 ? 2 * (ks - 12) : 10) : 0);
                asm volatile("s_waitcnt vmcnt(%2)" : "+v"(H[ks][0]), "+v"(H[ks][1]) : "i"(ny) : "memory");
                SB();
                sfor<0, 8>([&](auto I) {
                    constexpr int i = I.v, t = i >> 1, m = i & 1;
                    a1[t][m] = mfma(Wf[ks % 3][t], H[ks][m], a1[t][m]);
                    if constexpr (ks + 2 < 20 && (i & 1) == 0) Wf[(ks + 2) % 3][t] = wread(IC<t>{}, IC<ks + 2>{});
                    if constexpr (i == 3 && ks + 8 < 20) ISSUE_H(ks + 8);
                    if constexpr (ks >= 12 && ks < 17 && (i == 1 || i == 5)) { constexpr int pv = 10 + 2 * (ks - 12) + (i == 5); ISSUE_V(pv / 5, pv % 5); }
                    SB();
                });
            });
        }
        STAMP(20);
        f32x4 a2[3][2];
        sfor<0, 3>([&](auto TT) { const s16x8 cf = csfrag(4 + TT.v); a2[TT.v][0] = mfma(cf, Hm[0], z4); a2[TT.v][1] = mfma(cf, Hm[1], z4); });
        {
            s16x8 Wf[3][3];
            sfor<0, 2>([&](auto KS) { sfor<0, 3>([&](auto TT) { Wf[KS.v][TT.v] = wread(IC<4 + TT.v>{}, KS); }); });
            u32x2 e0, e1;
            sfor<0, 20>([&](auto KS) {
                constexpr int ks = KS.v;
                sfor<0, 6>([&](auto I) {
                    constexpr int i = I.v, t = i >> 1, m = i & 1;
                    a2[t][m] = mfma(Wf[ks % 3][t], H[ks][m], a2[t][m]);
                    if constexpr (ks + 2 < 20 && (i & 1) == 0) Wf[(ks + 2) % 3][t] = wread(IC<4 + t>{}, IC<ks + 2>{});
                    if constexpr (i == 3 || (i == 5 && (ks & 1) == 0)) { constexpr int pv = 20 + (ks >> 1) * 3 + ((ks & 1) ? 2 : (i == 5 ? 1 : 0)); ISSUE_V(pv / 5, pv % 5); }
                    if constexpr (ks >= 1 && ks < 17 && ((ks - 1) & 3) < 3) {
                        constexpr int u = (ks - 1) >> 2, ph = (ks - 1) & 3, P = u >> 1, mm = u & 1;
                        if constexpr (ph == 0 && i == 1) e0 = u32x2{relu2(pk2(a1[2 * P][mm][0], a1[2 * P][mm][1])), relu2(pk2(a1[2 * P][mm][2], a1[2 * P][mm][3]))};
                        if constexpr (ph == 1 && i == 1) e1 = u32x2{relu2(pk2(a1[2 * P + 1][mm][0], a1[2 * P + 1][mm][1])), relu2(pk2(a1[2 * P + 1][mm][2], a1[2 * P + 1][mm][3]))};
                        if constexpr (ph == 2 && i == 1)
                            st16_sc1((unsigned)(((mt0 + mm) * 54 + (Tp0 >> 1) + P) * 1024 + lane * 16), ws + WS_H1, u32x4{e0[0], e0[1], e1[0], e1[1]});
                    }
                    SB();
                });
            });
        }
        STAMP(22);
#pragma unroll
        for (int m = 0; m < 2; ++m) {
            st16_sc1((unsigned)(((mt0 + m) * 54 + (Tp0 >> 1) + 2) * 1024 + lane * 16), ws + WS_H1, fin8(a2[0][m], a2[1][m]));
            if (j2 < 12) st8_sc1((unsigned)(((mt0 + m) * 54 + (Tsg >> 1)) * 1024 + lane * 16 + (Tsg & 1) * 8), ws + WS_H1,
                                 u32x2{relu2(pk2(a2[2][m][0], a2[2][m][1])), relu2(pk2(a2[2][m][2], a2[2][m][3]))});
        }
        STAMP(24);
        drain_bar();
        if (tid < 16) flags_store(ws, 3, i2 * 16 + tid, j2, tag);
        STAMP(9);

        sfor<50, 70>([&](auto U) { ISSUE_V(U.v / 5, U.v % 5); });
        STAMP(44);
        await<63>(ws, 3, i2 * 16 + jw, tag, w, lane);
        STAMP(10);
        unsigned arun = (unsigned)(((8 * i2 + 4 * half) * 54 + ksb) * 1024 + lane * 16);
        const unsigned astep13 = nks == 14 ? 1024u : 0u;
        s16x8 A[4][4];
        unsigned aoff[4];
#pragma unroll
        for (int m = 0; m < 4; ++m) aoff[m] = ((w + m) & 3) * 54 * 1024;
        s16x8 st2v;
        ld16_sc1(st2v, (unsigned)((mtW * 16 + n) * 64 + g * 16), ws + WS_STAT2);
#define ISSUE_A(it, m) do { ld16_sc1(A[(it) & 3][m], arun + aoff[m], ws + WS_H1); \
            if ((m) == 3) { arun += ((it) == 12 ? astep13 : 1024u); asm volatile("" : "+v"(arun)); } } while (0)
        sfor<0, 3>([&](auto IT) { sfor<0, 4>([&](auto M) { ISSUE_A(IT.v, M.v); }); });
        f32x4 a[4][5];
#pragma unroll
        for (int m = 0; m < 4; ++m)
#pragma unroll
            for (int t = 0; t < 5; ++t) a[m][t] = z4;
        sfor<0, 14>([&](auto IT) {
            constexpr int it = IT.v;
            constexpr int ny = 4 * ((it + 2 < 13 ? it + 2 : 13) - it);
            if constexpr (it == 1) STAMP(25);
            if constexpr (it == 5) STAMP(26);
            if constexpr (it == 9) STAMP(27);
            if constexpr (it < 8)
                asm volatile("s_waitcnt vmcnt(%9)" : "+v"(A[it & 3][0]), "+v"(A[it & 3][1]), "+v"(A[it & 3][2]), "+v"(A[it & 3][3]),
                             "+a"(V[it][0]), "+a"(V[it][1]), "+a"(V[it][2]), "+a"(V[it][3]), "+a"(V[it][4]) : "i"(ny) : "memory");
            else
                asm volatile("s_waitcnt vmcnt(%9)" : "+v"(A[it & 3][0]), "+v"(A[it & 3][1]), "+v"(A[it & 3][2]), "+v"(A[it & 3][3]),
                             "+v"(V[it][0]), "+v"(V[it][1]), "+v"(V[it][2]), "+v"(V[it][3]), "+v"(V[it][4]) : "i"(ny) : "memory");
            SB();
            if (it < 13 || nks == 14) {
                sfor<0, 20>([&](auto I) {
                    constexpr int i = I.v, t = i >> 2, m = i & 3;
                    a[m][t] = mfma(V[it][t], A[it & 3][m], a[m][t]);
                    if constexpr ((i & 3) == 1 && i < 16 && it + 3 < 14) ISSUE_A(it + 3, i >> 2);
                    SB();
                });
            }
        });
        STAMP(11);
        asm volatile("" : "+v"(st2v));
        const f32x4 sv = __builtin_bit_cast(f32x4, st2v);
        const float smean = add_xor32(add_xor16(sv[0] + sv[2])) * (1.f / 640.f);
        const float rstd2 = __builtin_amdgcn_rsqf(fmaxf(add_xor32(add_xor16(sv[1] + sv[3])) * (1.f / 640.f) - smean * smean, 0.f) + 1e-5f);
#pragma unroll
        for (int m = 1; m < 4; ++m)
#pragma unroll
            for (int t = 0; t < 5; ++t) *(LAS f32x4*)(lb + ((((w + m) & 3) * 3 + (m - 1)) * 5 + t) * 1024 + lane * 16) = a[m][t];
        BAR();
        STAMP(46);
        f32x4 fin[5];
#pragma unroll
        for (int t = 0; t < 5; ++t) {
            const LAS char* p = (const LAS char*)(lb + (w * 15 + t) * 1024 + lane * 16);
            fin[t] = ((a[0][t] + *(const LAS f32x4*)p) + (*(const LAS f32x4*)(p + 5120) + *(const LAS f32x4*)(p + 10240))) * rstd2;
        }
        STAMP(47);
        LAS char* ot = (LAS char*)(lb + L_B + w * 2560);
#pragma unroll
        for (int t = 0; t < 5; ++t) {
            const u32x2 hp = *(LAS u32x2*)(lb + L_XRES + (16 * w + n) * 160 + 32 * t + 8 * g);
            const f32x4 ov = f32x4{bflo(hp[0]), bfhi(hp[0]), bflo(hp[1]), bfhi(hp[1])} + fin[t];
            *(LAS u32x2*)(ot + n * 160 + 32 * t + 8 * g) = pack4(ov);
        }
        asm volatile("s_waitcnt lgkmcnt(0)" ::: "memory");
        int lane3 = lane; asm volatile("" : "+v"(lane3));
#pragma unroll
        for (int u = 0; u < 3; ++u) {
            const int q = 64 * u + lane3, row = q / 10, sl = q - row * 10;
            if (q < 160) *(GAS u32x4*)(out + (size_t)(mtW * 16 + row) * 1280 + 160 * c8 + 16 * sl) = *(LAS u32x4*)(ot + q * 16);
        }
    }
    STAMP(12); STAMPRT(63);
}

extern "C" void kernel_launch(void* const* d_in, const int* in_sizes, int n_in, void* d_out, int out_size, void* d_ws, size_t ws_size,
                              hipStream_t stream) {
    (void)in_sizes; (void)n_in; (void)out_size; (void)ws_size;
    prep<<<704, 256, 0, stream>>>((const char*)d_in[0], (const char*)d_in[1], (const char*)d_in[2], (const char*)d_in[4], (const char*)d_in[5], (char*)d_ws);
    fused_block<<<256, 256, 0, stream>>>((const char*)d_in[0], (const char*)d_in[1], (const char*)d_in[2], (const char*)d_in[4],
                                         (const char*)d_in[5], (char*)d_out, (char*)d_ws);
}
```

```cpp
#include <hip/hip_runtime.h>
#include <stdint.h>
#pragma clang diagnostic ignored "-Wint-to-pointer-cast"

typedef short s16x4 __attribute__((ext_vector_type(4)));
typedef short s16x8 __attribute__((ext_vector_type(8)));
typedef float f32x4 __attribute__((ext_vector_type(4)));
typedef unsigned u32x4 __attribute__((ext_vector_type(4)));
typedef unsigned u32x2 __attribute__((ext_vector_type(2)));
typedef unsigned long long u64;
#define LAS __attribute__((address_space(3)))
#define GAS __attribute__((address_space(1)))
#define DI __device__ __forceinline__
#define RLX_AGENT __ATOMIC_RELAXED, __HIP_MEMORY_SCOPE_AGENT
#define SB() __builtin_amdgcn_sched_barrier(0)

constexpr size_t WS_FLAG = 4096;
constexpr size_t WS_KVP = 262144;
constexpr size_t WS_STAT2 = 0x120000;
constexpr size_t WS_O = 2u << 20;
constexpr size_t WS_H = 5u << 20;
constexpr size_t WS_H1 = 8u << 20;
constexpr size_t WS_W1R = 16u << 20;
constexpr size_t WS_W2R = 19u << 20;
constexpr size_t WS_STAMP = 24u << 20;
constexpr size_t WS_XN = 28u << 20;
constexpr size_t WS_WQR = 32u << 20;
constexpr size_t WS_W0R = 36u << 20;

constexpr int L_A = 0;
constexpr int L_B = 102400;
constexpr int L_XRES = L_B + 40960;
constexpr int L_M = 155648;
constexpr int L_MEAN = L_M;
constexpr int L_RSTD = L_M + 512;
constexpr int L_CSKV = L_M + 1024;
constexpr int L_CS4 = L_M + 1280;
constexpr int L_KVT = L_M + 1536;
constexpr int L_CS1 = L_M + 3328;
constexpr int L_JUNK = L_M + 5632;
constexpr int L_ZERO = L_M + 5888;
constexpr int L_CSQ = L_M + 5904 + 16;
constexpr int L_EPOCH = L_M + 7424;
constexpr int L_TRASH = L_M + 6400;
constexpr int LDS_BYTES = 163840;
constexpr int CODE_LINES = 288;

#define STAMP(i) do {} while (0)
#define STAMPRT(i) do {} while (0)

template <int I> struct IC { static constexpr int v = I; };
template <int B, int E, class F> DI void sfor(F&& f) { if constexpr (B < E) { f(IC<B>{}); sfor<B + 1, E>(f); } }
#define OPAQUE_S(p) asm volatile("" : "+s"(p))

DI f32x4 mfma(s16x8 a, s16x8 b, f32x4 c) { return __builtin_amdgcn_mfma_f32_16x16x32_bf16(a, b, c, 0, 0, 0); }
DI s16x4 trread(unsigned addr) { return __builtin_bit_cast(s16x4, __builtin_amdgcn_ds_read_tr16_b64_v4i16((LAS s16x4*)addr)); }
DI s16x8 cat(s16x4 a, s16x4 b) { return __builtin_shufflevector(a, b, 0, 1, 2, 3, 4, 5, 6, 7); }
DI unsigned pk2(float lo, float hi) {
    typedef __bf16 bf2 __attribute__((ext_vector_type(2)));
    typedef float f2 __attribute__((ext_vector_type(2)));
    f2 v = {lo, hi};
    return __builtin_bit_cast(unsigned, __builtin_convertvector(v, bf2));
}
DI s16x8 pack8(f32x4 a, f32x4 b) {
    u32x4 r = {pk2(a[0], a[1]), pk2(a[2], a[3]), pk2(b[0], b[1]), pk2(b[2], b[3])};
    return __builtin_bit_cast(s16x8, r);
}
DI u32x2 pack4(f32x4 a) { u32x2 r = {pk2(a[0], a[1]), pk2(a[2], a[3])}; return r; }
DI float bflo(unsigned u) { return __uint_as_float(u << 16); }
DI float bfhi(unsigned u) { return __uint_as_float(u & 0xffff0000u); }
DI float phi(float t) { return (fmaxf(t, 0.f) + 1e-6f) + __builtin_amdgcn_fmed3f(__builtin_amdgcn_exp2f(t * 1.44269504088896f), 0.f, 1.f); }
DI float add_xor16(float v) { const unsigned u = __float_as_uint(v); auto r = __builtin_amdgcn_permlane16_swap(u, u, false, false); return __uint_as_float(r[0]) + __uint_as_float(r[1]); }
DI float add_xor32(float v) { const unsigned u = __float_as_uint(v); auto r = __builtin_amdgcn_permlane32_swap(u, u, false, false); return __uint_as_float(r[0]) + __uint_as_float(r[1]); }
constexpr int wq_pos(int grp) { return grp < 3 ? 16384 + 5120 * grp : grp < 7 ? 32768 + 5120 * (grp - 3) : 5120 * (grp - 7); }

template <int LOFF> DI void glds16(unsigned voff, const void* sbase, unsigned lds) {
    asm volatile("s_add_u32 m0, %2, %3\n\ts_nop 0\n\tglobal_load_lds_dwordx4 %0, %1" ::"v"(voff), "s"(sbase), "s"(lds), "i"(LOFF) : "memory", "scc");
}
template <int LOFF, int GOFF> DI void glds16i(unsigned voff, const void* sbase, unsigned lds) {
    asm volatile("s_add_u32 m0, %2, %3\n\ts_nop 0\n\tglobal_load_lds_dwordx4 %0, %1 offset:%4" ::"v"(voff), "s"(sbase), "s"(lds), "i"(LOFF - GOFF), "i"(GOFF) : "memory", "scc");
}
template <int LOFF> DI void glds16c(unsigned voff, const void* sbase, unsigned lds) {
    asm volatile("s_add_u32 m0, %2, %3\n\ts_nop 0\n\tglobal_load_lds_dwordx4 %0, %1 sc1" ::"v"(voff), "s"(sbase), "s"(lds), "i"(LOFF) : "memory", "scc");
}
DI void glds4(unsigned voff, const void* sbase, unsigned lds) {
    asm volatile("s_mov_b32 m0, %2\n\ts_nop 0\n\tglobal_load_lds_dword %0, %1" ::"v"(voff), "s"(sbase), "s"(lds) : "memory");
}
#define WAITV(n) asm volatile("s_waitcnt vmcnt(%0)" ::"i"(n) : "memory")
#define BARX() do { __builtin_amdgcn_s_barrier(); asm volatile("" ::: "memory"); } while (0)
#define BAR() do { asm volatile("s_waitcnt lgkmcnt(0)" ::: "memory"); __builtin_amdgcn_s_barrier(); asm volatile("" ::: "memory"); } while (0)

DI void ld16_sc1(s16x8& d, unsigned voff, const void* sbase) {
    asm volatile("global_load_dwordx4 %0, %1, %2 sc1" : "=v"(d) : "v"(voff), "s"(sbase) : "memory");
}
DI void ld16(s16x8& d, unsigned voff, const void* sbase) {
    asm volatile("global_load_dwordx4 %0, %1, %2" : "=v"(d) : "v"(voff), "s"(sbase) : "memory");
}
DI void ld16a(s16x8& d, unsigned voff, const void* sbase) {
    asm volatile("global_load_dwordx4 %0, %1, %2" : "=a"(d) : "v"(voff), "s"(sbase) : "memory");
}
DI void ld8_sc1(u64& d, unsigned voff, const void* sbase) { asm volatile("global_load_dwordx2 %0, %1, %2 sc1" : "=v"(d) : "v"(voff), "s"(sbase) : "memory"); }
DI void st16_sc1(unsigned voff, void* sbase, u32x4 v) {
    asm volatile("global_store_dwordx4 %0, %1, %2 sc1\n\ts_nop 1" ::"v"(voff), "v"(v), "s"(sbase) : "memory");
}
DI void st8_sc1(unsigned voff, void* sbase, u32x2 v) {
    asm volatile("global_store_dwordx2 %0, %1, %2 sc1" ::"v"(voff), "v"(v), "s"(sbase) : "memory");
}
DI void drain_bar() { WAITV(0); BAR(); }
DI void flags_store(char* ws, int arr, int line, int slot, u64 tag) {
    __hip_atomic_store((GAS u64*)(ws + WS_FLAG + (size_t)arr * 32768 + (size_t)line * 128) + slot, tag, RLX_AGENT);
}
template <int NV> DI void await(char* ws, int arr, int line, u64 tag, int w, int lane) {
    if (w == 0) {
        GAS u64* fl = (GAS u64*)(ws + WS_FLAG + (size_t)arr * 32768 + (size_t)line * 128) + (lane & 15);
        for (unsigned spins = 0; spins < (1u << 20); ++spins) {
            u64 f = __hip_atomic_load(fl, RLX_AGENT);
            if (__all(f == tag)) break;
        }
    }
    WAITV(NV);
    BAR();
}

__global__ void __launch_bounds__(256)
prep(const char* __restrict__ x, const char* __restrict__ wqkv, const char* __restrict__ wo, const char* __restrict__ w1,
     const char* __restrict__ w2, char* __restrict__ ws) {
    __shared__ __attribute__((aligned(16))) char smem[20992];
    const unsigned lb = (unsigned)(uintptr_t)(LAS char*)smem;
    const int tid = threadIdx.x, lane = tid & 63, w = __builtin_amdgcn_readfirstlane(tid >> 6);
    const int g = lane >> 4, n = lane & 15, q4 = n >> 2, p4 = lane & 3;
    const int u = blockIdx.x;
    if (u < 128) {
        const int row = tid >> 4, c = tid & 15;
        u32x4 d[5];
#pragma unroll
        for (int i = 0; i < 5; ++i) d[i] = *(const GAS u32x4*)(x + (size_t)(u * 16 + row) * 1280 + (c + 16 * i) * 16);
        float v[40], su = 0.f;
#pragma unroll
        for (int i = 0; i < 5; ++i)
#pragma unroll
            for (int j = 0; j < 4; ++j) { v[8 * i + 2 * j] = bflo(d[i][j]); v[8 * i + 2 * j + 1] = bfhi(d[i][j]); su += v[8 * i + 2 * j] + v[8 * i + 2 * j + 1]; }
#pragma unroll
        for (int m = 1; m < 16; m <<= 1) su += __shfl_xor(su, m);
        const float mean = su * (1.f / 640.f);
        float s2 = 0.f;
#pragma unroll
        for (int i = 0; i < 40; ++i) { v[i] -= mean; s2 += v[i] * v[i]; }
#pragma unroll
        for (int m = 1; m < 16; m <<= 1) s2 += __shfl_xor(s2, m);
        const float rstd = __builtin_amdgcn_rsqf(s2 * (1.f / 640.f) + 1e-5f);
#pragma unroll
        for (int i = 0; i < 5; ++i)
            *(LAS u32x4*)(lb + row * 1296 + (c + 16 * i) * 16) = u32x4{pk2(v[8 * i] * rstd, v[8 * i + 1] * rstd), pk2(v[8 * i + 2] * rstd, v[8 * i + 3] * rstd),
                                                                      pk2(v[8 * i + 4] * rstd, v[8 * i + 5] * rstd), pk2(v[8 * i + 6] * rstd, v[8 * i + 7] * rstd)};
        __syncthreads();
#pragma unroll
        for (int k = 0; k < 5; ++k) {
            const int ks = 5 * w + k;
            const u32x2 lo = *(LAS u32x2*)(lb + n * 1296 + 64 * ks + 8 * g), hi = *(LAS u32x2*)(lb + n * 1296 + 64 * ks + 32 + 8 * g);
            st16_sc1((unsigned)((u * 20 + ks) * 1024 + lane * 16), ws + WS_XN, u32x4{lo[0], lo[1], hi[0], hi[1]});
        }
    } else if (u < 448) {
        const int v_ = u - 128, j = v_ / 20, ks = v_ - 20 * j;
#pragma unroll
        for (int r = 0; r < 2; ++r) {
            const int idx = tid + 256 * r;
            if (idx < 480) {
                const int row = idx / 15, sl = idx - 15 * row, sec = sl / 5;
                *(LAS u32x4*)(lb + row * 272 + sl * 16) = *(const GAS u32x4*)(wqkv + (size_t)(32 * ks + row) * 3840 + sec * 1280 + 80 * j + (sl - 5 * sec) * 16);
            }
        }
        __syncthreads();
#pragma unroll
        for (int k = 0; k < 2; ++k) {
            const int t = 2 * w + k;
            const int off = t == 0 ? 80 + 8 * p4 : t == 1 ? 120 + 8 * p4 : t == 2 ? 160 + 8 * p4 : t == 3 ? 200 + 8 * p4
                          : t == 4 ? (p4 == 0 ? 112 : p4 == 1 ? 152 : p4 == 2 ? 192 : 232) : t == 5 ? 8 * p4 : t == 6 ? 40 + 8 * p4 : ((p4 & 1) ? 72 : 32);
            const unsigned a_ = lb + (4 * g + q4) * 272 + off;
            s16x8 f = cat(trread(a_), trread(a_ + 16 * 272));
            if (t == 7 && n >= 8) f = s16x8{0, 0, 0, 0, 0, 0, 0, 0};
            st16_sc1((unsigned)(((j * 20 + ks) * 8 + t) * 1024 + lane * 16), ws + WS_WQR, __builtin_bit_cast(u32x4, f));
        }
    } else {
        const int row = tid >> 3, sl = tid & 7;
        u32x4 d[5];
        unsigned dst[5];
#pragma unroll
        for (int r = 0; r < 5; ++r) {
            const int id = u - 448 + 256 * r;
            const char* src;
            if (id < 200) {
                const int ks = id / 10, cl = id - 10 * ks;
                const int srow = ks < 16 ? 40 * ks + (row < 16 ? row : row + 4) : 20 * (4 * (2 * (ks - 16) + (row >> 4)) + ((row & 15) >> 2)) + 16 + (row & 3);
                src = wo + (size_t)srow * 1280 + cl * 128;
                const int T = 4 * cl + w, c8r = T / 5;
                dst[r] = (unsigned)WS_W0R + ((c8r * 20 + ks) * 5 + (T - 5 * c8r)) * 1024;
            } else if (id < 740) {
                const int v_ = id - 200, ks = v_ / 27, cl = v_ - 27 * ks;
                src = w1 + (size_t)(32 * ks + row) * 3456 + cl * 128;
                dst[r] = (unsigned)WS_W1R + ((4 * cl + w) * 20 + ks) * 1024;
            } else {
                const int v_ = id - 740, ks = v_ / 10, cl = v_ - 10 * ks;
                src = w2 + (size_t)(32 * ks + row) * 1280 + cl * 128;
                const int T = 4 * cl + w, c8r = T / 5;
                dst[r] = (unsigned)WS_W2R + ((c8r * 54 + ks) * 5 + (T - 5 * c8r)) * 1024;
            }
            d[r] = *(const GAS u32x4*)(src + sl * 16);
        }
#pragma unroll
        for (int r = 0; r < 5; ++r) *(LAS u32x4*)(lb + r * 4096 + row * 128 + 16 * (sl ^ (((row >> 1) & 3) << 1))) = d[r];
        __syncthreads();
        const int rw = 4 * g + q4;
        const unsigned a_ = lb + rw * 128 + 16 * ((2 * w + (p4 >> 1)) ^ (((rw >> 1) & 3) << 1)) + 8 * (p4 & 1);
#pragma unroll
        for (int r = 0; r < 5; ++r) st16_sc1(dst[r] + lane * 16, ws, __builtin_bit_cast(u32x4, cat(trread(a_ + r * 4096), trread(a_ + r * 4096 + 2048))));
    }
}

__global__ void __launch_bounds__(256, 1)
fused_block(const char* __restrict__ x, const char* __restrict__ wqkv, const char* __restrict__ wo, const char* __restrict__ w1,
            const char* __restrict__ w2, char* __restrict__ out, char* __restrict__ ws) {
    __shared__ __attribute__((aligned(16))) char smem[LDS_BYTES];
    const unsigned lb = (unsigned)(uintptr_t)(LAS char*)smem;
    const int tid = threadIdx.x, lane = tid & 63;
    const int w = __builtin_amdgcn_readfirstlane(tid >> 6);
    const int b = blockIdx.x, x8 = b & 7, rr = b >> 3;
    const int i1 = 4 * (x8 >> 1) + (rr >> 3), j1 = 8 * (x8 & 1) + (rr & 7);
    const int i2 = i1, j2 = j1;
    const int half = (rr >> 2) & 1, c8 = 4 * (x8 & 1) + (rr & 3), jw = 8 * half + c8;
    const int g = lane >> 4, n = lane & 15, q4 = n >> 2, p4 = lane & 3;
    const s16x8 ones = {0x3F80, 0x3F80, 0x3F80, 0x3F80, 0x3F80, 0x3F80, 0x3F80, 0x3F80};
    const f32x4 z4 = {0, 0, 0, 0};
    STAMP(0); STAMPRT(62);

    {
        u64 pc;
        asm volatile("s_getpc_b64 %0" : "=s"(pc));
        const unsigned cl_ = (rr * 4 + w) * 3 + ((lane & 3) < 3 ? (lane & 3) : 0);
        glds4((cl_ < CODE_LINES ? cl_ : CODE_LINES - 1) * 128, (const void*)(pc & ~(u64)127), lb + L_JUNK);
    }
    const unsigned vlan16 = lane * 16;

    f32x4 acc[8][2];
    {
        s16x8 X[20][2];
        const char* wsrc = ws + WS_WQR + (size_t)(j1 * 20 * 8 + 2 * w) * 1024;
        unsigned xrun = (unsigned)((8 * i1 + 2 * w) * 20 * 1024) + vlan16;
        const unsigned ldsW = lb + w * 2048;
        auto issue = [&](auto KS) {
            constexpr int ks = KS.v;
            glds16<(ks % 18) * 8192>(vlan16, wsrc, ldsW);
            glds16<(ks % 18) * 8192 + 1024>(vlan16 + 1024, wsrc, ldsW);
            wsrc += 8192; OPAQUE_S(wsrc);
            ld16(X[ks][0], xrun, ws + WS_XN);
            ld16(X[ks][1], xrun + 20 * 1024, ws + WS_XN);
            xrun += 1024; asm volatile("" : "+v"(xrun));
        };
        sfor<0, 8>([&](auto KS) { issue(KS); });
        asm volatile("s_mov_b32 m0, %2\n\ts_nop 0\n\tglobal_load_lds_dword %0, %1 sc1" ::"v"((lane & 1) * 4), "s"(ws), "s"(lb + L_EPOCH) : "memory");
        unsigned vW0 = lb + vlan16, vW1 = lb + 65536 + vlan16, vW2 = lb + 131072 + vlan16;
        asm volatile("" : "+v"(vW0), "+v"(vW1), "+v"(vW2));
        auto wread = [&](auto KS, auto TT) -> s16x8 {
            constexpr int o = (KS.v % 18) * 8192 + TT.v * 1024;
            return *(volatile LAS s16x8*)((o < 65536 ? vW0 : o < 131072 ? vW1 : vW2) + (o & 65535));
        };
#pragma unroll
        for (int t = 0; t < 8; ++t) acc[t][0] = acc[t][1] = z4;
        s16x8 Wf[2][8];
        WAITV(4 * 6 + 1);
        BARX();
        sfor<0, 8>([&](auto TT) { Wf[0][TT.v] = wread(IC<0>{}, TT); });
        SB();
        sfor<0, 20>([&](auto KS) {
            constexpr int ks = KS.v;
            if constexpr ((ks & 1) == 0) {
                constexpr int issued = 4 * ((ks + 7 < 19 ? ks + 7 : 19) + 1), need = 4 * (ks + 3 < 20 ? ks + 3 : 20);
                WAITV(issued - need + (ks + 2 < 8 ? 1 : 0));
                BARX();
                SB();
            }
            asm volatile("" : "+v"(X[ks][0]), "+v"(X[ks][1]));
            sfor<0, 16>([&](auto I) {
                constexpr int i = I.v, t = i >> 1, m = i & 1;
                if constexpr (t < 5) acc[t][m] = mfma(X[ks][m], Wf[ks & 1][t], acc[t][m]);
                else acc[t][m] = mfma(Wf[ks & 1][t], X[ks][m], acc[t][m]);
                if constexpr (ks < 19 && (i & 1) == 0) Wf[(ks + 1) & 1][t] = wread(IC<ks + 1>{}, IC<t>{});
                if constexpr (i == 5 && ks + 8 < 20) issue(IC<(ks + 8 < 20 ? ks + 8 : 19)>{});
                SB();
            });
        });
    }
    const u64 tag = *(volatile LAS u64*)(lb + L_EPOCH) + 1;
    STAMP(1);

    {
        const unsigned red = lb + 16384 + w * 3360, trash = lb + L_TRASH + vlan16;
        const unsigned r0 = red + (n * 20 + 4 * g) * 4;
        const unsigned r1 = (n == 0 || (n >= 8 && n < 12)) ? red + ((n == 0 ? 20 : 8 + n) * 20 + 4 * g) * 4 : trash;
        const unsigned r2 = (n == 0 || n >= 12) ? red + (420 + (n == 0 ? 20 : 4 + n) * 20 + 4 * g) * 4 : trash;
        const unsigned r3 = g < 2 ? red + (420 * g + n * 20 + 16) * 4 : trash;
        const unsigned r4 = (g < 2 && (n == 0 || (g == 0 ? (n >= 8 && n < 12) : n >= 12))) ? red + (420 * g + (n == 0 ? 20 : (g == 0 ? 8 + n : 4 + n)) * 20 + 16) * 4 : trash;
        s16x8 F[4], MA, MB;
        sfor<0, 5>([&](auto TT) {
            constexpr int t = TT.v;
            f32x4 v[2], vb_[2];
#pragma unroll
            for (int m = 0; m < 2; ++m)
#pragma unroll
                for (int r = 0; r < 4; ++r) {
                    const float y = acc[t][m][r];
                    if (t < 2) v[m][r] = phi(y);
                    else if (t < 4) v[m][r] = y;
                    else { v[m][r] = n < 8 ? phi(y) : y; vb_[m][r] = n < 8 ? 1.f : y; }
                }
            if constexpr (t < 4) F[t] = pack8(v[0], v[1]);
            else { MA = pack8(v[0], v[1]); MB = pack8(vb_[0], vb_[1]); }
        });
        STAMP(33);
        f32x4 P1 = mfma(F[0], F[2], z4), P2 = mfma(F[0], MB, z4), P3 = mfma(MA, F[2], z4), P4 = mfma(MA, MB, z4);
        f32x4 P5 = mfma(F[1], F[3], z4), P6 = mfma(F[1], MB, z4), P7 = mfma(MA, F[3], z4);
        *(LAS f32x4*)(r0) = P1;
        *(LAS f32x4*)(r0 + 1680) = P5;
        *(LAS f32x4*)(r1) = P2;
        *(LAS f32x4*)(r2) = P6;
        *(LAS f32x4*)(r3) = (g == 0 ? P3 : P7);
        *(LAS f32x4*)(r4) = P4;
        if (tid == 0) { *(LAS u64*)(lb + L_ZERO) = 0; *(LAS u64*)(lb + L_ZERO + 8) = 0; }
    }
    STAMP(34);
    BAR();
    if (tid < 210) {
        LAS char* red = (LAS char*)(lb + 16384 + tid * 16);
        f32x4 s = (*(LAS f32x4*)(red) + *(LAS f32x4*)(red + 3360)) + (*(LAS f32x4*)(red + 6720) + *(LAS f32x4*)(red + 10080));
        st16_sc1((unsigned)(tid * 16), ws + WS_KVP + (size_t)(j1 * 16 + i1) * 3360, __builtin_bit_cast(u32x4, s));
    }
    STAMP(36);
    drain_bar();
    if (tid < 16) flags_store(ws, 0, tid * 16 + j1, i1, tag);
    STAMP(2);

    {
        const char* s0 = ws + WS_W0R + (size_t)(c8 * 100 + w) * 1024;
        sfor<0, 25>([&](auto U) { glds16<U.v * 4096>(vlan16, s0, lb + w * 1024); s0 += 4096; OPAQUE_S(s0); });
        if (w < 2) {
            int lane2 = lane; asm volatile("" : "+v"(lane2));
            sfor<0, 5>([&](auto U) { const int q = 64 * U.v + lane2, row = q / 10, sl = q - row * 10;
                glds16<U.v * 1024>((unsigned)(row * 1280 + 160 * c8 + 16 * sl), x + (size_t)(8 * i2 + 4 * half + 2 * w) * 16 * 1280, lb + L_XRES + w * 5120); });
        }
    }
    s16x8 Fa[2], Fb[2];
#pragma unroll
    for (int m = 0; m < 2; ++m) {
        f32x4 v[3];
#pragma unroll
        for (int t = 0; t < 3; ++t)
#pragma unroll
            for (int r = 0; r < 4; ++r) v[t][r] = phi(acc[5 + t][m][r]);
        u32x4 u2 = __builtin_bit_cast(u32x4, pack8(v[0], v[2])), u3 = __builtin_bit_cast(u32x4, pack8(v[1], v[2]));
        if (g >= 2) { u2[2] = u2[3] = 0u; u3[2] = u3[3] = 0u; }
        Fa[m] = __builtin_bit_cast(s16x8, u2); Fb[m] = __builtin_bit_cast(s16x8, u3);
    }
    unsigned oa[12];
    {
        const unsigned kt = lb + L_KVT, zz = lb + L_ZERO;
        auto ka = [&](int head, int e, int c) { return kt + (head * 420 + e * 20 + 4 * c) * 2; };
        oa[0] = ka(0, n, g);  oa[1] = g == 0 ? ka(0, n, 4) : zz;
        oa[2] = ka(1, n, g);  oa[3] = g == 1 ? ka(1, n, 4) : zz;
        oa[4] = ka(0, 20, g); oa[5] = g == 0 ? ka(0, 20, 4) : zz;
        oa[6] = ka(1, 20, g); oa[7] = g == 1 ? ka(1, 20, 4) : zz;
        oa[8] = n < 4 ? ka(0, 16 + n, g) : zz;                oa[9] = (n < 4 && g == 0) ? ka(0, 16 + n, 4) : zz;
        oa[10] = (n >= 4 && n < 8) ? ka(1, 12 + n, g) : zz;   oa[11] = (n >= 4 && n < 8 && g == 1) ? ka(1, 12 + n, 4) : zz;
    }
    asm volatile("" : "+v"(Fa[0]), "+v"(Fa[1]), "+v"(Fb[0]), "+v"(Fb[1]), "+v"(oa[0]), "+v"(oa[1]), "+v"(oa[2]), "+v"(oa[3]), "+v"(oa[4]), "+v"(oa[5]),
                 "+v"(oa[6]), "+v"(oa[7]), "+v"(oa[8]), "+v"(oa[9]), "+v"(oa[10]), "+v"(oa[11]));
    STAMP(3);

    await<63>(ws, 0, i1 * 16 + j1, tag, w, lane);
    STAMP(4);
    {
        s16x8 kp[16];
        const char* kb = ws + WS_KVP + (size_t)j1 * 16 * 3360;
        const unsigned kvo = (tid < 210 ? tid : 209) * 16;
#pragma unroll
        for (int i = 0; i < 16; ++i) ld16_sc1(kp[i], kvo + i * 3360, kb);
        asm volatile("s_waitcnt vmcnt(0)" : "+v"(kp[0]), "+v"(kp[1]), "+v"(kp[2]), "+v"(kp[3]), "+v"(kp[4]), "+v"(kp[5]), "+v"(kp[6]), "+v"(kp[7]),
                     "+v"(kp[8]), "+v"(kp[9]), "+v"(kp[10]), "+v"(kp[11]), "+v"(kp[12]), "+v"(kp[13]), "+v"(kp[14]), "+v"(kp[15])::"memory");
        f32x4 s0 = __builtin_bit_cast(f32x4, kp[0]), s1 = __builtin_bit_cast(f32x4, kp[1]);
#pragma unroll
        for (int i = 2; i < 16; i += 2) { s0 += __builtin_bit_cast(f32x4, kp[i]); s1 += __builtin_bit_cast(f32x4, kp[i + 1]); }
        if (tid < 210) *(LAS u32x2*)(lb + L_KVT + tid * 8) = pack4(s0 + s1);
    }
    STAMP(38);
    BAR();
    STAMP(39);
    {
        auto rd2 = [&](unsigned a0, unsigned a1) { const u32x2 lo = *(LAS u32x2*)a0, hi = *(LAS u32x2*)a1; return __builtin_bit_cast(s16x8, u32x4{lo[0], lo[1], hi[0], hi[1]}); };
        const s16x8 Ama = rd2(oa[0], oa[1]), Amb = rd2(oa[2], oa[3]), Aza = rd2(oa[4], oa[5]), Azb = rd2(oa[6], oa[7]), Ata = rd2(oa[8], oa[9]), Atb = rd2(oa[10], oa[11]);
#pragma unroll
        for (int m = 0; m < 2; ++m) {
            f32x4 Da = mfma(Ama, Fa[m], z4), Db = mfma(Amb, Fb[m], z4);
            f32x4 Dt = mfma(Atb, Fb[m], mfma(Ata, Fa[m], z4));
            f32x4 Dza = mfma(Aza, Fa[m], z4), Dzb = mfma(Azb, Fb[m], z4);
            const float ra = __builtin_amdgcn_rcpf(Dza[0] + 1e-6f), rb = __builtin_amdgcn_rcpf(Dzb[0] + 1e-6f);
            const int mt = 8 * i1 + 2 * w + m;
            s16x8 of = pack8(Da * ra, Db * rb);
            st16_sc1((unsigned)((mt * 20 + j1) * 1024 + lane * 16), ws + WS_O, __builtin_bit_cast(u32x4, of));
            u32x2 tl = pack4(Dt * (g == 0 ? ra : rb));
            const int t = j1 >> 1;
            if (g < 2) st8_sc1((unsigned)((mt * 20 + 16 + (t >> 1)) * 1024 + (n + 16 * (2 * (j1 & 1) + g)) * 16 + (t & 1) * 8), ws + WS_O, tl);
        }
    }
    STAMP(40);
    drain_bar();
    if (tid < 16) flags_store(ws, 1, i1 * 16 + tid, j1, tag);
    STAMP(5);

    const int T0 = j2 < 12 ? 7 * j2 : 84 + 6 * (j2 - 12);
    const int odd = (j2 < 12) ? (j2 & 1) : 0;
    const int Tp0 = T0 + odd;
    const int Tsg = j2 < 12 ? (odd ? T0 : T0 + 6) : Tp0;
    const int ksb = w < 2 ? 14 * w : 28 + 13 * (w - 2);
    const int nks = w < 2 ? 14 : 13;
    s16x8 V[14][5];
    const char* w2run = ws + WS_W2R + (size_t)(c8 * 54 + ksb) * 5120;
    const int step13 = nks == 14 ? 5120 : 0;
#define ISSUE_V(it, t) do { if constexpr ((it) < 8) ld16a(V[it][t], vlan16 + (t) * 1024, w2run); else ld16(V[it][t], vlan16 + (t) * 1024, w2run); \
            if ((t) == 4) { w2run += ((it) == 12 ? step13 : 5120); OPAQUE_S(w2run); } } while (0)
    sfor<0, 10>([&](auto P) {
        constexpr int bg = P.v;
        const int Tg = bg < 5 ? Tp0 + 5 : Tsg;
        glds16<(5 + bg / 5) * 20480 + (bg % 5) * 4096>(vlan16, ws + WS_W1R + (size_t)(Tg * 20 + (bg % 5) * 4 + w) * 1024, lb + w * 1024);
    });
    sfor<0, 10>([&](auto P) { ISSUE_V(P.v / 5, P.v % 5); });
    const int mtW = 8 * i2 + 4 * half + w;
    STAMP(45);
    await<63>(ws, 1, i2 * 16 + jw, tag, w, lane);
    STAMP(6);
    {
        s16x8 O[20];
        unsigned orun = (unsigned)(mtW * 20 * 1024 + lane * 16);
#define ISSUE_O(i) do { ld16_sc1(O[i], orun, ws + WS_O); orun += 1024u; asm volatile("" : "+v"(orun)); } while (0)
        sfor<0, 8>([&](auto I) { ISSUE_O(I.v); });
        unsigned vA = lb + L_A + vlan16, vA2 = vA + 61440;
        asm volatile("" : "+v"(vA), "+v"(vA2));
        f32x4 a[5];
#pragma unroll
        for (int t = 0; t < 5; ++t) a[t] = z4;
        s16x8 Wf[3][5];
        auto wrd = [&](auto I, auto T) -> s16x8 { constexpr int o = (5 * I.v + T.v) * 1024; return *(volatile LAS s16x8*)((o < 61440 ? vA : vA2) + (o < 61440 ? o : o - 61440)); };
        sfor<0, 2>([&](auto I) { sfor<0, 5>([&](auto T) { Wf[I.v][T.v] = wrd(I, T); }); });
        SB();
        sfor<0, 20>([&](auto I) {
            constexpr int i = I.v;
            asm volatile("s_waitcnt vmcnt(%1)" : "+v"(O[i]) : "i"((i + 7 < 19 ? i + 7 : 19) - i) : "memory");
            SB();
            sfor<0, 5>([&](auto T) {
                constexpr int t = T.v;
                a[t] = mfma(Wf[i % 3][t], O[i], a[t]);
                if constexpr (i + 2 < 20) Wf[(i + 2) % 3][t] = wrd(IC<i + 2>{}, T);
                if constexpr (t == 1 && i + 8 < 20) ISSUE_O(i + 8);
                SB();
            });
        });
        STAMP(28);
        float su = 0.f, s2 = 0.f;
        u32x2 hpk[5];
#pragma unroll
        for (int t = 0; t < 5; ++t) {
            const u32x2 xr = *(LAS u32x2*)(lb + L_XRES + (16 * w + n) * 160 + 32 * t + 8 * g);
            const f32x4 hv = f32x4{bflo(xr[0]), bfhi(xr[0]), bflo(xr[1]), bfhi(xr[1])} + a[t];
            hpk[t] = pack4(hv);
            *(LAS u32x2*)(lb + L_XRES + (16 * w + n) * 160 + 32 * t + 8 * g) = hpk[t];
#pragma unroll
            for (int r = 0; r < 4; ++r) { su += hv[r]; s2 += hv[r] * hv[r]; }
        }
        const unsigned hb = (unsigned)((mtW * 20 + ((5 * c8) >> 1)) * 1024 + lane * 16);
        const int od = c8 & 1;
        st8_sc1(hb + (od ? 8 : 2048), ws + WS_H, od ? hpk[0] : hpk[4]);
        { const u32x2 p0 = od ? hpk[1] : hpk[0], p1 = od ? hpk[2] : hpk[1]; st16_sc1(hb + (od ? 1024 : 0), ws + WS_H, u32x4{p0[0], p0[1], p1[0], p1[1]}); }
        { const u32x2 p0 = od ? hpk[3] : hpk[2], p1 = od ? hpk[4] : hpk[3]; st16_sc1(hb + (od ? 2048 : 1024), ws + WS_H, u32x4{p0[0], p0[1], p1[0], p1[1]}); }
        su = add_xor32(add_xor16(su)); s2 = add_xor32(add_xor16(s2));
        if (g == 0) { u32x2 st = {__float_as_uint(su), __float_as_uint(s2)}; st8_sc1((unsigned)((mtW * 16 + n) * 64 + c8 * 8), ws + WS_STAT2, st); }
    }
    STAMP(42);
    drain_bar();
    if (tid < 16) flags_store(ws, 2, i2 * 16 + tid, jw, tag);
    if (b == 0 && tid == 0) __hip_atomic_store((GAS u64*)ws, tag, RLX_AGENT);
    STAMP(7);

    {
        const char* s1 = ws + WS_W1R + (size_t)(Tp0 * 20 + w) * 1024;
        const unsigned l1 = lb + L_A + w * 1024;
        sfor<0, 25>([&](auto U) { glds16<U.v * 4096>(vlan16, s1, l1); s1 += 4096; OPAQUE_S(s1); });
    }
    STAMP(43);
    await<0>(ws, 2, i2 * 16 + j2, tag, w, lane);
    STAMP(8);
    {
        s16x8 H[20][2];
        const int mt0 = 8 * i2 + 2 * w;
        s16x8 stv[2];
#pragma unroll
        for (int m = 0; m < 2; ++m) ld16_sc1(stv[m], (unsigned)(((mt0 + m) * 16 + n) * 64 + g * 16), ws + WS_STAT2);
        unsigned hrun = (unsigned)(mt0 * 20 * 1024 + lane * 16);
#define ISSUE_H(ks) do { ld16_sc1(H[ks][0], hrun, ws + WS_H); ld16_sc1(H[ks][1], hrun + 20 * 1024, ws + WS_H); hrun += 1024; asm volatile("" : "+v"(hrun)); } while (0)
        unsigned vF0 = lb + vlan16, vF1 = lb + 61440 + vlan16, vF2 = lb + 122880 + vlan16;
        asm volatile("" : "+v"(vF0), "+v"(vF1), "+v"(vF2));
        auto wread = [&](auto TT, auto KS) -> s16x8 {
            constexpr int t = TT.v, ks = KS.v;
            return *(volatile LAS s16x8*)((t < 3 ? vF0 : t < 6 ? vF1 : vF2) + (t % 3) * 20480 + ks * 1024);
        };
        {
            f32x4 c0 = z4, c1 = z4;
            const unsigned ta = lb + vlan16 + w * 20480, tb = lb + vlan16 + (w < 3 ? w + 4 : w) * 20480;
            s16x8 Ca[4], Cb[4];
            sfor<0, 3>([&](auto K) { Ca[K.v] = *(volatile LAS s16x8*)(ta + K.v * 1024); Cb[K.v] = *(volatile LAS s16x8*)(tb + K.v * 1024); });
            SB();
            sfor<0, 20>([&](auto K) {
                constexpr int k = K.v;
                c0 = mfma(ones, Ca[k & 3], c0);
                if constexpr (k + 3 < 20) Ca[(k + 3) & 3] = *(volatile LAS s16x8*)(ta + (k + 3) * 1024);
                SB();
                c1 = mfma(ones, Cb[k & 3], c1);
                if constexpr (k + 3 < 20) Cb[(k + 3) & 3] = *(volatile LAS s16x8*)(tb + (k + 3) * 1024);
                if constexpr ((k & 1) == 0 && k < 16) ISSUE_H(k >> 1);
                SB();
            });
            auto split = [&](float c) { const unsigned hi = pk2(c, c); const float lo = c - bflo(hi); u32x2 r = {hi, pk2(lo, lo)}; return r; };
            if (g == 0) {
                *(LAS u32x2*)(lb + L_CS1 + w * 128 + n * 8) = split(c0[0]);
                if (w < 3) *(LAS u32x2*)(lb + L_CS1 + (w + 4) * 128 + n * 8) = split(c1[0]);
            }
            if (tid < 2) *(LAS u64*)(lb + L_CS1 + 896 + tid * 8) = 0;
        }
        STAMP(19);
        asm volatile("s_waitcnt vmcnt(16)" : "+v"(stv[0]), "+v"(stv[1])::"memory");
        s16x8 Hm[2];
#pragma unroll
        for (int m = 0; m < 2; ++m) {
            f32x4 s = __builtin_bit_cast(f32x4, stv[m]);
            const float nm = add_xor32(add_xor16(s[0] + s[2])) * (-1.f / 640.f);
            const unsigned hi = pk2(nm, nm);
            const unsigned pr = g == 0 ? ((hi & 0xffffu) | (pk2(nm - bflo(hi), 0.f) << 16)) : 0u;
            Hm[m] = __builtin_bit_cast(s16x8, u32x4{pr, pr, 0u, 0u});
        }
        s16x8 Wf1[3][4];
        sfor<0, 2>([&](auto KS) { sfor<0, 4>([&](auto TT) { Wf1[KS.v][TT.v] = wread(TT, KS); }); });
        __builtin_amdgcn_s_waitcnt(0xC87F);
        BARX();
        const unsigned csa = lb + L_CS1 + (g == 0 ? n * 8 : 896);
        auto csfrag = [&](int t) { const u32x2 c = *(LAS u32x2*)(csa + (g == 0 ? t * 128 : 0)); return __builtin_bit_cast(s16x8, u32x4{c[0], c[1], 0u, 0u}); };
        auto relu2 = [&](unsigned p) { typedef short s2_ __attribute__((ext_vector_type(2))); const s2_ z = {0, 0};
                                       return __builtin_bit_cast(unsigned, __builtin_elementwise_max(__builtin_bit_cast(s2_, p), z)); };
        auto fin8 = [&](f32x4 x0, f32x4 x1) { return u32x4{relu2(pk2(x0[0], x0[1])), relu2(pk2(x0[2], x0[3])), relu2(pk2(x1[0], x1[1])), relu2(pk2(x1[2], x1[3]))}; };
        f32x4 a1[4][2];
        sfor<0, 4>([&](auto TT) { const s16x8 cf = csfrag(TT.v); a1[TT.v][0] = mfma(cf, Hm[0], z4); a1[TT.v][1] = mfma(cf, Hm[1], z4); });
        {
            auto& Wf = Wf1;
            SB();
            sfor<0, 20>([&](auto KS) {
                constexpr int ks = KS.v;
                constexpr int ny = 2 * ((ks + 7 < 19 ? ks + 7 : 19) - ks) + (ks > 12 ? (ks < 17 ? 2 * (ks - 12) : 10) : 0);
                asm volatile("s_waitcnt vmcnt(%2)" : "+v"(H[ks][0]), "+v"(H[ks][1]) : "i"(ny) : "memory");
                SB();
                sfor<0, 8>([&](auto I) {
                    constexpr int i = I.v, t = i >> 1, m = i & 1;
                    a1[t][m] = mfma(Wf[ks % 3][t], H[ks][m], a1[t][m]);
                    if constexpr (ks + 2 < 20 && (i & 1) == 0) Wf[(ks + 2) % 3][t] = wread(IC<t>{}, IC<ks + 2>{});
                    if constexpr (i == 3 && ks + 8 < 20) ISSUE_H(ks + 8);
                    if constexpr (ks >= 12 && ks < 17 && (i == 1 || i == 5)) { constexpr int pv = 10 + 2 * (ks - 12) + (i == 5); ISSUE_V(pv / 5, pv % 5); }
                    SB();
                });
            });
        }
        STAMP(20);
        f32x4 a2[3][2];
        sfor<0, 3>([&](auto TT) { const s16x8 cf = csfrag(4 + TT.v); a2[TT.v][0] = mfma(cf, Hm[0], z4); a2[TT.v][1] = mfma(cf, Hm[1], z4); });
        {
            s16x8 Wf[3][3];
            sfor<0, 2>([&](auto KS) { sfor<0, 3>([&](auto TT) { Wf[KS.v][TT.v] = wread(IC<4 + TT.v>{}, KS); }); });
            u32x2 e0, e1;
            sfor<0, 20>([&](auto KS) {
                constexpr int ks = KS.v;
                sfor<0, 6>([&](auto I) {
                    constexpr int i = I.v, t = i >> 1, m = i & 1;
                    a2[t][m] = mfma(Wf[ks % 3][t], H[ks][m], a2[t][m]);
                    if constexpr (ks + 2 < 20 && (i & 1) == 0) Wf[(ks + 2) % 3][t] = wread(IC<4 + t>{}, IC<ks + 2>{});
                    if constexpr (i == 3 || (i == 5 && (ks & 1) == 0)) { constexpr int pv = 20 + (ks >> 1) * 3 + ((ks & 1) ? 2 : (i == 5 ? 1 : 0)); ISSUE_V(pv / 5, pv % 5); }
                    if constexpr (ks >= 1 && ks < 17 && ((ks - 1) & 3) < 3) {
                        constexpr int u = (ks - 1) >> 2, ph = (ks - 1) & 3, P = u >> 1, mm = u & 1;
                        if constexpr (ph == 0 && i == 1) e0 = u32x2{relu2(pk2(a1[2 * P][mm][0], a1[2 * P][mm][1])), relu2(pk2(a1[2 * P][mm][2], a1[2 * P][mm][3]))};
                        if constexpr (ph == 1 && i == 1) e1 = u32x2{relu2(pk2(a1[2 * P + 1][mm][0], a1[2 * P + 1][mm][1])), relu2(pk2(a1[2 * P + 1][mm][2], a1[2 * P + 1][mm][3]))};
                        if constexpr (ph == 2 && i == 1)
                            st16_sc1((unsigned)(((mt0 + mm) * 54 + (Tp0 >> 1) + P) * 1024 + lane * 16), ws + WS_H1, u32x4{e0[0], e0[1], e1[0], e1[1]});
                    }
                    SB();
                });
            });
        }
        STAMP(22);
#pragma unroll
        for (int m = 0; m < 2; ++m) {
            st16_sc1((unsigned)(((mt0 + m) * 54 + (Tp0 >> 1) + 2) * 1024 + lane * 16), ws + WS_H1, fin8(a2[0][m], a2[1][m]));
            if (j2 < 12) st8_sc1((unsigned)(((mt0 + m) * 54 + (Tsg >> 1)) * 1024 + lane * 16 + (Tsg & 1) * 8), ws + WS_H1,
                                 u32x2{relu2(pk2(a2[2][m][0], a2[2][m][1])), relu2(pk2(a2[2][m][2], a2[2][m][3]))});
        }
        STAMP(24);
        drain_bar();
        if (tid < 16) flags_store(ws, 3, i2 * 16 + tid, j2, tag);
        STAMP(9);

        sfor<50, 70>([&](auto U) { ISSUE_V(U.v / 5, U.v % 5); });
        STAMP(44);
        await<63>(ws, 3, i2 * 16 + jw, tag, w, lane);
        STAMP(10);
        unsigned arun = (unsigned)(((8 * i2 + 4 * half) * 54 + ksb) * 1024 + lane * 16);
        const unsigned astep13 = nks == 14 ? 1024u : 0u;
        s16x8 A[4][4];
        unsigned aoff[4];
#pragma unroll
        for (int m = 0; m < 4; ++m) aoff[m] = ((w + m) & 3) * 54 * 1024;
        s16x8 st2v;
        ld16_sc1(st2v, (unsigned)((mtW * 16 + n) * 64 + g * 16), ws + WS_STAT2);
#define ISSUE_A(it, m) do { ld16_sc1(A[(it) & 3][m], arun + aoff[m], ws + WS_H1); \
            if ((m) == 3) { arun += ((it) == 12 ? astep13 : 1024u); asm volatile("" : "+v"(arun)); } } while (0)
        sfor<0, 3>([&](auto IT) { sfor<0, 4>([&](auto M) { ISSUE_A(IT.v, M.v); }); });
        f32x4 a[4][5];
#pragma unroll
        for (int m = 0; m < 4; ++m)
#pragma unroll
            for (int t = 0; t < 5; ++t) a[m][t] = z4;
        sfor<0, 14>([&](auto IT) {
            constexpr int it = IT.v;
            constexpr int ny = 4 * ((it + 2 < 13 ? it + 2 : 13) - it);
            if constexpr (it == 1) STAMP(25);
            if constexpr (it == 5) STAMP(26);
            if constexpr (it == 9) STAMP(27);
            if constexpr (it < 8)
                asm volatile("s_waitcnt vmcnt(%9)" : "+v"(A[it & 3][0]), "+v"(A[it & 3][1]), "+v"(A[it & 3][2]), "+v"(A[it & 3][3]),
                             "+a"(V[it][0]), "+a"(V[it][1]), "+a"(V[it][2]), "+a"(V[it][3]), "+a"(V[it][4]) : "i"(ny) : "memory");
            else
                asm volatile("s_waitcnt vmcnt(%9)" : "+v"(A[it & 3][0]), "+v"(A[it & 3][1]), "+v"(A[it & 3][2]), "+v"(A[it & 3][3]),
                             "+v"(V[it][0]), "+v"(V[it][1]), "+v"(V[it][2]), "+v"(V[it][3]), "+v"(V[it][4]) : "i"(ny) : "memory");
            SB();
            if (it < 13 || nks == 14) {
                sfor<0, 20>([&](auto I) {
                    constexpr int i = I.v, t = i >> 2, m = i & 3;
                    a[m][t] = mfma(V[it][t], A[it & 3][m], a[m][t]);
                    if constexpr ((i & 3) == 1 && i < 16 && it + 3 < 14) ISSUE_A(it + 3, i >> 2);
                    SB();
                });
            }
        });
        STAMP(11);
        asm volatile("" : "+v"(st2v));
        const f32x4 sv = __builtin_bit_cast(f32x4, st2v);
        const float smean = add_xor32(add_xor16(sv[0] + sv[2])) * (1.f / 640.f);
        const float rstd2 = __builtin_amdgcn_rsqf(fmaxf(add_xor32(add_xor16(sv[1] + sv[3])) * (1.f / 640.f) - smean * smean, 0.f) + 1e-5f);
#pragma unroll
        for (int m = 1; m < 4; ++m)
#pragma unroll
            for (int t = 0; t < 5; ++t) *(LAS f32x4*)(lb + ((((w + m) & 3) * 3 + (m - 1)) * 5 + t) * 1024 + lane * 16) = a[m][t];
        BAR();
        STAMP(46);
        f32x4 fin[5];
#pragma unroll
        for (int t = 0; t < 5; ++t) {
            const LAS char* p = (const LAS char*)(lb + (w * 15 + t) * 1024 + lane * 16);
            fin[t] = ((a[0][t] + *(const LAS f32x4*)p) + (*(const LAS f32x4*)(p + 5120) + *(const LAS f32x4*)(p + 10240))) * rstd2;
        }
        STAMP(47);
        LAS char* ot = (LAS char*)(lb + L_B + w * 2560);
#pragma unroll
        for (int t = 0; t < 5; ++t) {
            const u32x2 hp = *(LAS u32x2*)(lb + L_XRES + (16 * w + n) * 160 + 32 * t + 8 * g);
            const f32x4 ov = f32x4{bflo(hp[0]), bfhi(hp[0]), bflo(hp[1]), bfhi(hp[1])} + fin[t];
            *(LAS u32x2*)(ot + n * 160 + 32 * t + 8 * g) = pack4(ov);
        }
        asm volatile("s_waitcnt lgkmcnt(0)" ::: "memory");
        int lane3 = lane; asm volatile("" : "+v"(lane3));
#pragma unroll
        for (int u = 0; u < 3; ++u) {
            const int q = 64 * u + lane3, row = q / 10, sl = q - row * 10;
            if (q < 160) *(GAS u32x4*)(out + (size_t)(mtW * 16 + row) * 1280 + 160 * c8 + 16 * sl) = *(LAS u32x4*)(ot + q * 16);
        }
    }
    STAMP(12); STAMPRT(63);
}

extern "C" void kernel_launch(void* const* d_in, const int* in_sizes, int n_in, void* d_out, int out_size, void* d_ws, size_t ws_size,
                              hipStream_t stream) {
    (void)in_sizes; (void)n_in; (void)out_size; (void)ws_size;
    prep<<<704, 256, 0, stream>>>((const char*)d_in[0], (const char*)d_in[1], (const char*)d_in[2], (const char*)d_in[4], (const char*)d_in[5], (char*)d_ws);
    fused_block<<<256, 256, 0, stream>>>((const char*)d_in[0], (const char*)d_in[1], (const char*)d_in[2], (const char*)d_in[4],
                                         (const char*)d_in[5], (char*)d_out, (char*)d_ws);
}
```

```cpp
#include <hip/hip_runtime.h>
#include <stdint.h>
#pragma clang diagnostic ignored "-Wint-to-pointer-cast"

typedef short s16x4 __attribute__((ext_vector_type(4)));
typedef short s16x8 __attribute__((ext_vector_type(8)));
typedef float f32x4 __attribute__((ext_vector_type(4)));
typedef unsigned u32x4 __attribute__((ext_vector_type(4)));
typedef unsigned u32x2 __attribute__((ext_vector_type(2)));
typedef unsigned long long u64;
#define LAS __attribute__((address_space(3)))
#define GAS __attribute__((address_space(1)))
#define DI __device__ __forceinline__
#define RLX_AGENT __ATOMIC_RELAXED, __HIP_MEMORY_SCOPE_AGENT
#define SB() __builtin_amdgcn_sched_barrier(0)

constexpr size_t WS_FLAG = 4096;
constexpr size_t WS_GFLAG = 2048;
constexpr size_t WS_KVP = 262144;
constexpr size_t WS_STAT2 = 0x120000;
constexpr size_t WS_O = 2u << 20;
constexpr size_t WS_H = 5u << 20;
constexpr size_t WS_H1 = 8u << 20;
constexpr size_t WS_W1R = 16u << 20;
constexpr size_t WS_W2R = 19u << 20;
constexpr size_t WS_STAMP = 24u << 20;

constexpr int L_A = 0;
constexpr int L_B = 102400;
constexpr int L_XRES = L_B + 40960;
constexpr int L_M = 155648;
constexpr int L_MEAN = L_M;
constexpr int L_RSTD = L_M + 512;
constexpr int L_CSKV = L_M + 1024;
constexpr int L_CS4 = L_M + 1280;
constexpr int L_KVT = L_M + 1536;
constexpr int L_CS1 = L_M + 3328;
constexpr int L_JUNK = L_M + 5632;
constexpr int L_ZERO = L_M + 5888;
constexpr int L_CSQ = L_M + 5904 + 16;
constexpr int L_EPOCH = L_M + 7424;
constexpr int L_TRASH = L_M + 6400;
constexpr int LDS_BYTES = 163840;
constexpr int CODE_LINES = 368;

enum { OP_X, OP_W, OP_R, OP_O, OP_Q, OP_T, OP_S, OP_U };
struct P1Op { int kind, a, b, ks, slot; };
constexpr P1Op P1SEQ[] = {
    {OP_O, 0, 0, -1, 0},
    {OP_U, 0, 0, -1, 0}, {OP_U, 1, 0, -1, 0},
    {OP_X, 0, 0, -1, 0}, {OP_X, 0, 1, -1, 0}, {OP_X, 0, 2, -1, 0}, {OP_X, 0, 3, -1, 0},
    {OP_W, 0, 0, -1, 0}, {OP_W, 0, 1, -1, 0}, {OP_W, 0, 2, -1, 0}, {OP_W, 0, 3, -1, 0}, {OP_W, 0, 4, -1, 0},
    {OP_U, 2, 0, -1, 0}, {OP_U, 3, 0, -1, 0},
    {OP_X, 1, 0, -1, 0}, {OP_X, 1, 1, -1, 0}, {OP_X, 1, 2, -1, 0}, {OP_X, 1, 3, -1, 0},
    {OP_O, 1, 0, -1, 0},
    {OP_W, 1, 0, -1, 0}, {OP_W, 1, 1, -1, 0}, {OP_W, 1, 2, -1, 0}, {OP_W, 1, 3, -1, 0}, {OP_W, 1, 4, -1, 0},
    {OP_X, 2, 0, -1, 0}, {OP_X, 2, 1, -1, 0}, {OP_X, 2, 2, -1, 0}, {OP_X, 2, 3, -1, 0},
    {OP_W, 2, 0, 0, 3}, {OP_W, 2, 1, 0, 8}, {OP_W, 2, 2, 0, 13},
    {OP_X, 3, 0, 1, 2}, {OP_X, 3, 1, 1, 6}, {OP_X, 3, 2, 1, 10}, {OP_X, 3, 3, 1, 14},
    {OP_W, 2, 3, 2, 3}, {OP_W, 2, 4, 2, 8}, {OP_W, 3, 0, 2, 13},
    {OP_X, 4, 0, 3, 2}, {OP_X, 4, 1, 3, 6}, {OP_X, 4, 2, 3, 10}, {OP_X, 4, 3, 3, 14},
    {OP_W, 3, 1, 4, 3}, {OP_U, 4, 0, 4, 5}, {OP_W, 3, 2, 4, 8}, {OP_W, 3, 3, 4, 13},
    {OP_X, 5, 0, 5, 2}, {OP_X, 5, 1, 5, 6}, {OP_X, 5, 2, 5, 10}, {OP_X, 5, 3, 5, 14},
    {OP_W, 3, 4, 6, 3}, {OP_W, 4, 0, 6, 8}, {OP_W, 4, 1, 6, 13},
    {OP_X, 6, 0, 7, 2}, {OP_X, 6, 1, 7, 6}, {OP_X, 6, 2, 7, 10}, {OP_X, 6, 3, 7, 14},
    {OP_W, 4, 2, 8, 3}, {OP_W, 4, 3, 8, 8}, {OP_W, 4, 4, 8, 13},
    {OP_X, 7, 0, 9, 2}, {OP_X, 7, 1, 9, 6}, {OP_X, 7, 2, 9, 10}, {OP_X, 7, 3, 9, 14},
    {OP_R, 0, 0, 10, 3}, {OP_R, 1, 0, 10, 8}, {OP_R, 2, 0, 10, 13},
    {OP_X, 8, 0, 11, 2}, {OP_X, 8, 1, 11, 6}, {OP_X, 8, 2, 11, 10}, {OP_X, 8, 3, 11, 14},
    {OP_R, 3, 0, 12, 3}, {OP_R, 4, 0, 12, 8}, {OP_U, 5, 0, 12, 13},
    {OP_X, 9, 0, 13, 2}, {OP_X, 9, 1, 13, 6}, {OP_X, 9, 2, 13, 10}, {OP_X, 9, 3, 13, 14},
    {OP_Q, 0, 0, 15, 3}, {OP_Q, 0, 1, 15, 8}, {OP_Q, 0, 2, 15, 13}, {OP_Q, 0, 3, 16, 3}, {OP_Q, 0, 4, 16, 8},
    {OP_T, 0, 0, 17, 1}, {OP_T, 1, 0, 17, 2}, {OP_Q, 1, 0, 17, 3}, {OP_T, 2, 0, 17, 5}, {OP_T, 3, 0, 17, 6}, {OP_T, 4, 0, 17, 7},
    {OP_Q, 1, 1, 17, 8}, {OP_Q, 1, 2, 17, 13},
    {OP_S, 0, 0, 18, 1}, {OP_Q, 1, 3, 18, 3}, {OP_S, 1, 0, 18, 6}, {OP_Q, 1, 4, 18, 8}, {OP_S, 2, 0, 18, 11}, {OP_S, 3, 0, 19, 2}, {OP_S, 4, 0, 19, 7},
};
constexpr int P1N = sizeof(P1SEQ) / sizeof(P1SEQ[0]);
constexpr int p1_wait(int s) {
    int issued = 0, need = 0;
    for (int i = 0; i < P1N; ++i) {
        if (P1SEQ[i].ks < 2 * s - 1 || P1SEQ[i].ks < 0) issued = i + 1;
        if ((P1SEQ[i].kind == OP_X && P1SEQ[i].a == s) || (P1SEQ[i].kind == OP_W && P1SEQ[i].a <= (s >> 1))) need = need > i + 1 ? need : i + 1;
    }
    return issued - need;
}
constexpr bool p1_ok() { for (int s = 0; s < 10; ++s) if (p1_wait(s) < 0 || p1_wait(s) > 63) return false; return true; }
static_assert(p1_ok(), "P1a schedule: an op is waited for before it is issued");

#define STAMP(i) do {} while (0)
#define STAMPRT(i) do {} while (0)

template <int I> struct IC { static constexpr int v = I; };
template <int B, int E, class F> DI void sfor(F&& f) { if constexpr (B < E) { f(IC<B>{}); sfor<B + 1, E>(f); } }
#define OPAQUE_S(p) asm volatile("" : "+s"(p))

DI f32x4 mfma(s16x8 a, s16x8 b, f32x4 c) { return __builtin_amdgcn_mfma_f32_16x16x32_bf16(a, b, c, 0, 0, 0); }
DI s16x4 trread(unsigned addr) { return __builtin_bit_cast(s16x4, __builtin_amdgcn_ds_read_tr16_b64_v4i16((LAS s16x4*)addr)); }
DI s16x8 cat(s16x4 a, s16x4 b) { return __builtin_shufflevector(a, b, 0, 1, 2, 3, 4, 5, 6, 7); }
DI unsigned pk2(float lo, float hi) {
    typedef __bf16 bf2 __attribute__((ext_vector_type(2)));
    typedef float f2 __attribute__((ext_vector_type(2)));
    f2 v = {lo, hi};
    return __builtin_bit_cast(unsigned, __builtin_convertvector(v, bf2));
}
DI s16x8 pack8(f32x4 a, f32x4 b) {
    u32x4 r = {pk2(a[0], a[1]), pk2(a[2], a[3]), pk2(b[0], b[1]), pk2(b[2], b[3])};
    return __builtin_bit_cast(s16x8, r);
}
DI u32x2 pack4(f32x4 a) { u32x2 r = {pk2(a[0], a[1]), pk2(a[2], a[3])}; return r; }
DI float bflo(unsigned u) { return __uint_as_float(u << 16); }
DI float bfhi(unsigned u) { return __uint_as_float(u & 0xffff0000u); }
DI float phi(float t) { return (fmaxf(t, 0.f) + 1e-6f) + __builtin_amdgcn_fmed3f(__builtin_amdgcn_exp2f(t * 1.44269504088896f), 0.f, 1.f); }
DI float add_xor16(float v) { const unsigned u = __float_as_uint(v); auto r = __builtin_amdgcn_permlane16_swap(u, u, false, false); return __uint_as_float(r[0]) + __uint_as_float(r[1]); }
DI float add_xor32(float v) { const unsigned u = __float_as_uint(v); auto r = __builtin_amdgcn_permlane32_swap(u, u, false, false); return __uint_as_float(r[0]) + __uint_as_float(r[1]); }
constexpr int wq_pos(int grp) { return grp < 3 ? 16384 + 5120 * grp : grp < 7 ? 32768 + 5120 * (grp - 3) : 5120 * (grp - 7); }

template <int LOFF> DI void glds16(unsigned voff, const void* sbase, unsigned lds) {
    asm volatile("s_add_u32 m0, %2, %3\n\ts_nop 0\n\tglobal_load_lds_dwordx4 %0, %1" ::"v"(voff), "s"(sbase), "s"(lds), "i"(LOFF) : "memory", "scc");
}
template <int LOFF, int GOFF> DI void glds16i(unsigned voff, const void* sbase, unsigned lds) {
    asm volatile("s_add_u32 m0, %2, %3\n\ts_nop 0\n\tglobal_load_lds_dwordx4 %0, %1 offset:%4" ::"v"(voff), "s"(sbase), "s"(lds), "i"(LOFF - GOFF), "i"(GOFF) : "memory", "scc");
}
template <int LOFF> DI void glds16c(unsigned voff, const void* sbase, unsigned lds) {
    asm volatile("s_add_u32 m0, %2, %3\n\ts_nop 0\n\tglobal_load_lds_dwordx4 %0, %1 sc1" ::"v"(voff), "s"(sbase), "s"(lds), "i"(LOFF) : "memory", "scc");
}
DI void glds4(unsigned voff, const void* sbase, unsigned lds) {
    asm volatile("s_mov_b32 m0, %2\n\ts_nop 0\n\tglobal_load_lds_dword %0, %1" ::"v"(voff), "s"(sbase), "s"(lds) : "memory");
}
#define WAITV(n) asm volatile("s_waitcnt vmcnt(%0)" ::"i"(n) : "memory")
#define BARX() do { __builtin_amdgcn_s_barrier(); asm volatile("" ::: "memory"); } while (0)
#define BAR() do { asm volatile("s_waitcnt lgkmcnt(0)" ::: "memory"); __builtin_amdgcn_s_barrier(); asm volatile("" ::: "memory"); } while (0)

DI void ld16_sc1(s16x8& d, unsigned voff, const void* sbase) {
    asm volatile("global_load_dwordx4 %0, %1, %2 sc1" : "=v"(d) : "v"(voff), "s"(sbase) : "memory");
}
DI void ld16a_sc1(s16x8& d, unsigned voff, const void* sbase) {
    asm volatile("global_load_dwordx4 %0, %1, %2 sc1" : "=a"(d) : "v"(voff), "s"(sbase) : "memory");
}
DI void ld8_sc1(u64& d, unsigned voff, const void* sbase) { asm volatile("global_load_dwordx2 %0, %1, %2 sc1" : "=v"(d) : "v"(voff), "s"(sbase) : "memory"); }
DI void st16_sc1(unsigned voff, void* sbase, u32x4 v) {
    asm volatile("global_store_dwordx4 %0, %1, %2 sc1\n\ts_nop 1" ::"v"(voff), "v"(v), "s"(sbase) : "memory");
}
DI void st8_sc1(unsigned voff, void* sbase, u32x2 v) {
    asm volatile("global_store_dwordx2 %0, %1, %2 sc1" ::"v"(voff), "v"(v), "s"(sbase) : "memory");
}
DI void drain_bar() { WAITV(0); BAR(); }
DI void flags_store(char* ws, int arr, int line, int slot, u64 tag) {
    __hip_atomic_store((GAS u64*)(ws + WS_FLAG + (size_t)arr * 32768 + (size_t)line * 128) + slot, tag, RLX_AGENT);
}
template <int NV> DI void await(char* ws, int arr, int line, u64 tag, int w, int lane) {
    if (w == 0) {
        GAS u64* fl = (GAS u64*)(ws + WS_FLAG + (size_t)arr * 32768 + (size_t)line * 128) + (lane & 15);
        for (unsigned spins = 0; spins < (1u << 20); ++spins) {
            u64 f = __hip_atomic_load(fl, RLX_AGENT);
            if (__all(f == tag)) break;
        }
    }
    WAITV(NV);
    BAR();
}

__global__ void __launch_bounds__(256, 1)
fused_block(const char* __restrict__ x, const char* __restrict__ wqkv, const char* __restrict__ wo, const char* __restrict__ w1,
            const char* __restrict__ w2, char* __restrict__ out, char* __restrict__ ws) {
    __shared__ __attribute__((aligned(16))) char smem[LDS_BYTES];
    const unsigned lb = (unsigned)(uintptr_t)(LAS char*)smem;
    const int tid = threadIdx.x, lane = tid & 63;
    const int w = __builtin_amdgcn_readfirstlane(tid >> 6);
    const int b = blockIdx.x, x8 = b & 7, rr = b >> 3;
    const int i1 = 4 * (x8 >> 1) + (rr >> 3), j1 = 8 * (x8 & 1) + (rr & 7);
    const int i2 = i1, j2 = j1;
    const int half = (rr >> 2) & 1, c8 = 4 * (x8 & 1) + (rr & 3), jw = 8 * half + c8;
    const int g = lane >> 4, n = lane & 15, q4 = n >> 2, p4 = lane & 3;
    const s16x8 ones = {0x3F80, 0x3F80, 0x3F80, 0x3F80, 0x3F80, 0x3F80, 0x3F80, 0x3F80};
    const f32x4 z4 = {0, 0, 0, 0};
    STAMP(0); STAMPRT(62);

    s16x8 X[20][2];
    f32x4 acc[5][2], sq[2], rs[2], csA, csB;
    unsigned voffW[5], voffX[4];
    const char* xb = x + (size_t)i1 * 128 * 1280;
    const unsigned ldsXw = lb + L_B + w * 4096, ldsWw = lb + L_A + w * 5120;
#pragma unroll
    for (int u = 0; u < 4; ++u)
        voffX[u] = (32 * w + 8 * u + (lane >> 3)) * 1280 + 16 * ((lane & 7) ^ ((lane >> 4) + 4 * (u & 1)));
#define ISSUE_X(s, u) glds16i<((s) % 3) * 16384 + (u) * 1024, (s) * 128>(voffX[u], xb, ldsXw)
#define ISSUE_XS(s) do { ISSUE_X(s, 0); ISSUE_X(s, 1); ISSUE_X(s, 2); ISSUE_X(s, 3); } while (0)
#pragma unroll
    for (int u = 0; u < 5; ++u) {
        int q = 64 * u + lane, row = q / 10, s = q - row * 10;
        row ^= ((row >> 3) & 1) << 2;
        voffW[u] = row * 3840 + (s < 5 ? (640 + 40 * j1) * 2 + 16 * s : (1280 + 40 * j1) * 2 + 16 * (s - 5));
    }
    const char* wkb = wqkv + (size_t)w * (32 * 3840);
    const char* wrun = wkb;
#define ISSUE_W(t, u) do { glds16<(t) * 20480 + (u) * 1024>(voffW[u], wrun, ldsWw); if ((u) == 4) { wrun += 128 * 3840; OPAQUE_S(wrun); } } while (0)
    unsigned voffQ[5];
    auto make_voffQ = [&] {
        int l2 = lane; asm volatile("" : "+v"(l2));
#pragma unroll
        for (int u = 0; u < 5; ++u) {
            int q = 64 * u + l2, row = q / 5, s = q - row * 5;
            row ^= ((row >> 3) & 1) << 2;
            voffQ[u] = row * 3840 + 80 * j1 + 16 * s;
        }
        asm volatile("" : "+v"(voffQ[0]), "+v"(voffQ[1]), "+v"(voffQ[2]), "+v"(voffQ[3]), "+v"(voffQ[4]));
    };
    auto issueQ = [&](auto A, auto U) {
        constexpr int a = A.v, u = U.v;
        const int grp = a == 0 ? (w < 2 ? w : 2) : a == 1 ? 3 + w : 7 + (w < 2 ? w : 2);
        const int pos = a == 0 ? 16384 + 5120 * grp : a == 1 ? 32768 + 5120 * w : 5120 * (grp - 7);
        glds16<u * 1024>(voffQ[u], wqkv + (size_t)grp * (64 * 3840), lb + L_B + pos);
    };
    const unsigned raR = lb + L_A + (4 * g + q4) * 128 + 16 * ((2 * w + (p4 >> 1)) ^ ((((4 * g + q4) >> 1) & 3) << 1)) + 8 * (p4 & 1);
    s16x8 Rf[5];
    auto runit = [&](auto R, unsigned& isw1, unsigned& ksr, unsigned& cl) {
        constexpr int r = R.v;
        const unsigned ub = (unsigned)b;
        unsigned v;
        if constexpr (r < 2) { isw1 = 1; v = ub + 256 * r; }
        else if constexpr (r == 2) { isw1 = ub < 28; v = isw1 ? ub + 512 : ub - 28; }
        else if constexpr (r == 3) { isw1 = 0; v = ub + 228; }
        else { isw1 = 0; v = ub < 56 ? ub + 484 : ub + 228; }
        ksr = isw1 ? (v * 2428u) >> 16 : (v * 6554u) >> 16;
        cl = v - ksr * (isw1 ? 27u : 10u);
    };
    auto p1issue = [&](auto N) {
        constexpr P1Op op = P1SEQ[N.v];
        if constexpr (op.kind == OP_X) ISSUE_X(op.a, op.b);
        else if constexpr (op.kind == OP_W) ISSUE_W(op.a, op.b);
        else if constexpr (op.kind == OP_R) {
            unsigned isw1, ksr, cl;
            runit(IC<op.a>{}, isw1, ksr, cl);
            const unsigned pitch = isw1 ? 3456u : 1280u;
            const unsigned rrow = 8 * w + (lane >> 3);
            glds16<op.a * 4096>(rrow * pitch + 16 * ((lane & 7) ^ (((rrow >> 1) & 3) << 1)), (isw1 ? w1 : w2) + (ksr * 32 * pitch + cl * 128), lb + L_A + w * 1024);
        } else if constexpr (op.kind == OP_U) {
            const unsigned wv = rr * 4 + w;
            if constexpr (op.a == 0 || op.a == 2) {
                const unsigned l = lane < 20 ? lane : 0, rw = (l * 52u) >> 8;
                glds4((4 * wv + rw) * 1280 + (l - 5 * rw + (op.a == 2 ? 5 : 0)) * 128, x + (size_t)(x8 >> 1) * 512 * 1280, lb + L_JUNK);
            } else if constexpr (op.a == 1 || op.a == 3) {
                const unsigned li = wv * 25 + (lane < 25 ? lane : 0), rw = (li * 6554u) >> 16, rem = li - rw * 10;
                glds4((rw + (op.a == 3 ? 320 : 0)) * 3840 + 1280 + (rem >= 5 ? 640 : 0) + (x8 & 1) * 640 + rem * 128, wqkv, lb + L_JUNK);
            } else if constexpr (op.a == 4) {
                const unsigned li = wv * 25 + (lane < 25 ? lane : 0), rw = (li * 13108u) >> 16, rem = li - rw * 5;
                glds4(rw * 3840 + (x8 & 1) * 640 + rem * 128, wqkv, lb + L_JUNK);
            } else {
                const unsigned li = wv * 25 + (lane < 25 ? lane : 0), rw = (li * 13108u) >> 16, rem = li - rw * 5;
                glds4(rw * 1280 + (x8 & 1) * 640 + rem * 128, wo, lb + L_JUNK);
            }
        } else if constexpr (op.kind == OP_Q) issueQ(IC<op.a>{}, IC<op.b>{});
        else if constexpr (op.kind == OP_T) {
            Rf[op.a] = cat(trread(raR + op.a * 4096), trread(raR + op.a * 4096 + 2048));
        } else if constexpr (op.kind == OP_S) {
            unsigned isw1, ksr, cl;
            runit(IC<op.a>{}, isw1, ksr, cl);
            const unsigned T = 4 * cl + w, c8r = (T * 52u) >> 8;
            const unsigned dst = isw1 ? (unsigned)WS_W1R + (T * 20 + ksr) * 1024 : (unsigned)WS_W2R + ((c8r * 54 + ksr) * 5 + (T - 5 * c8r)) * 1024;
            st16_sc1((unsigned)(lane * 16), ws + dst, __builtin_bit_cast(u32x4, Rf[op.a]));
        } else if constexpr (op.a == 0) {
            u64 pc;
            asm volatile("s_getpc_b64 %0" : "=s"(pc));
            pc &= ~(u64)127;
            const unsigned cl_ = (rr * 4 + w) * 3 + ((lane & 3) < 3 ? (lane & 3) : 0);
            glds4((cl_ < CODE_LINES ? cl_ : CODE_LINES - 1) * 128, (const void*)pc, lb + L_JUNK);
        } else asm volatile("s_mov_b32 m0, %2\n\ts_nop 0\n\tglobal_load_lds_dword %0, %1 sc1" ::"v"((lane & 1) * 4), "s"(ws), "s"(lb + L_EPOCH) : "memory");
    };
    sfor<0, P1N>([&](auto N) { if constexpr (P1SEQ[N.v].ks < 0) p1issue(N); });
    unsigned rda[5];
    auto make_rda = [&] {
        int l2 = lane; asm volatile("" : "+v"(l2));
        const int g2 = l2 >> 4, n2 = l2 & 15;
        const unsigned red = lb + L_A + 20480 + w * 3360, trash = lb + L_TRASH + l2 * 16;
        rda[0] = red + (n2 * 20 + 4 * g2) * 4;
        rda[1] = (n2 == 0 || (n2 >= 8 && n2 < 12)) ? red + ((n2 == 0 ? 20 : 8 + n2) * 20 + 4 * g2) * 4 : trash;
        rda[2] = (n2 == 0 || n2 >= 12) ? red + (420 + (n2 == 0 ? 20 : 4 + n2) * 20 + 4 * g2) * 4 : trash;
        rda[3] = g2 < 2 ? red + (420 * g2 + n2 * 20 + 16) * 4 : trash;
        rda[4] = (g2 < 2 && (n2 == 0 || (g2 == 0 ? (n2 >= 8 && n2 < 12) : n2 >= 12))) ? red + (420 * g2 + (n2 == 0 ? 20 : (g2 == 0 ? 8 + n2 : 4 + n2)) * 20 + 16) * 4 : trash;
        asm volatile("" : "+v"(rda[0]), "+v"(rda[1]), "+v"(rda[2]), "+v"(rda[3]), "+v"(rda[4]));
        if (tid == 0) { *(LAS u64*)(lb + L_ZERO) = 0; *(LAS u64*)(lb + L_ZERO + 8) = 0; }
    };
    const int ro1 = 8 * g + q4 + 4 * (g & 1), ro2 = 8 * g + q4 + 4 * (1 - (g & 1));
    {
        unsigned vW1[2], vW2[2], vM1[2], vM2[2], vC1[2], vC2[2];
        const int mo = (p4 == 0 ? 32 : p4 == 1 ? 72 : p4 == 2 ? 112 : 152);
#pragma unroll
        for (int h = 0; h < 2; ++h) {
            vW1[h] = lb + L_A + h * 51200 + ro1 * 160 + 8 * p4; vW2[h] = lb + L_A + h * 51200 + ro2 * 160 + 8 * p4;
            vM1[h] = lb + L_A + h * 51200 + ro1 * 160 + mo; vM2[h] = lb + L_A + h * 51200 + ro2 * 160 + mo;
            vC1[h] = vW1[h] + 40 * w; vC2[h] = vW2[h] + 40 * w;
            asm volatile("" : "+v"(vW1[h]), "+v"(vW2[h]), "+v"(vM1[h]), "+v"(vM2[h]), "+v"(vC1[h]), "+v"(vC2[h]));
        }
        unsigned vD1 = vM1[0] + w * 5120, vD2 = vM2[0] + w * 5120;
        unsigned vX[2];
#pragma unroll
        for (int kl = 0; kl < 2; ++kl) vX[kl] = lb + L_B + w * 4096 + n * 128 + 16 * ((4 * kl + g) ^ (n >> 1));
#pragma unroll
        for (int t = 0; t < 5; ++t) acc[t][0] = acc[t][1] = z4;
        sq[0] = sq[1] = rs[0] = rs[1] = csA = csB = z4;
        s16x4 Wl[2][7], Wh[2][7];
        auto side = [&](auto KS, auto I) {
            constexpr int ks = KS.v, i = I.v, r = ks & 1, s = ks >> 1, kl = ks & 1, h = ks >= 10;
            constexpr unsigned ko = (ks - 10 * h) * 5120;
            if constexpr (i < 2) X[ks][i] = *(volatile LAS s16x8*)(vX[kl] + (s % 3) * 16384 + i * 2048);
            else if constexpr (i < 10) { constexpr int t = (i - 2) >> 1; if constexpr (i & 1) Wh[r][t] = trread(vW2[h] + ko + 40 * t); else Wl[r][t] = trread(vW1[h] + ko + 40 * t); }
            else if constexpr (i < 12) { if constexpr (i & 1) Wh[r][4] = trread(vM2[h] + ko); else Wl[r][4] = trread(vM1[h] + ko); }
            else if constexpr (i < 14) { if constexpr (i & 1) Wh[r][5] = trread(vC2[h] + ko); else Wl[r][5] = trread(vC1[h] + ko); }
            else {
                constexpr int u = ks >> 2;
                if constexpr (i & 1) { Wh[r][6] = trread(vD2 + (u % 3) * 20480); if constexpr (u == 2) { vD2 += 61440; asm volatile("" : "+v"(vD2)); } }
                else { Wl[r][6] = trread(vD1 + (u % 3) * 20480); if constexpr (u == 2) { vD1 += 61440; asm volatile("" : "+v"(vD1)); } }
            }
        };
        STAMP(48);
        WAITV(p1_wait(0));
        BARX();
        STAMP(49);
        sfor<0, 14>([&](auto I) { side(IC<0>{}, I); });
        SB();
        sfor<0, 20>([&](auto KS) {
            constexpr int ks = KS.v;
            if constexpr ((ks & 1) && ks < 19) {
                constexpr int s = (ks + 1) >> 1;
                WAITV(p1_wait(s));
                if constexpr (s >= 8) BAR();
                else if constexpr ((s & 1) == 0) BARX();
                SB();
            }
            if constexpr (ks == 4) STAMP(50);
            if constexpr (ks == 8) STAMP(51);
            if constexpr (ks == 12) STAMP(52);
            if constexpr (ks == 16) STAMP(53);
            constexpr int r = ks & 1;
            constexpr int nm = ((ks & 3) == 3) ? 16 : 15;
            constexpr int nside = ks < 19 ? (((ks + 1) & 3) == 3 ? 16 : 14) : 0;
            sfor<0, 16>([&](auto I) {
                constexpr int i = I.v;
                if constexpr (i < nm) {
                    if constexpr (i < 10) { constexpr int t = i >> 1, m = i & 1; acc[t][m] = mfma(X[ks][m], cat(Wl[r][t], Wh[r][t]), acc[t][m]); }
                    else if constexpr (i == 10) csA = mfma(ones, cat(Wl[r][5], Wh[r][5]), csA);
                    else if constexpr (i < 13) sq[i - 11] = mfma(X[ks][i - 11], X[ks][i - 11], sq[i - 11]);
                    else if constexpr (i < 15) rs[i - 13] = mfma(X[ks][i - 13], ones, rs[i - 13]);
                    else csB = mfma(ones, cat(Wl[r][6], Wh[r][6]), csB);
                }
                if constexpr (i < nside) side(IC<ks + 1>{}, I);
                sfor<0, P1N>([&](auto N) { if constexpr (P1SEQ[N.v].ks == ks && P1SEQ[N.v].slot == i) p1issue(N); });
                if constexpr (ks == 3 && i == 12) make_rda();
                if constexpr (ks == 5 && i == 12) make_voffQ();
                SB();
            });
        });
    }
    asm volatile("" : "+v"(csA), "+v"(csB));
    const u64 tag = *(volatile LAS u64*)(lb + L_EPOCH) + 1;
    STAMP(1);

    if (g == 0) { ((LAS float*)(lb + L_CSKV))[16 * w + n] = csA[0]; ((LAS float*)(lb + L_CS4))[16 * w + n] = csB[0]; }
    f32x4 lnA[2], lnB[2];
#pragma unroll
    for (int m = 0; m < 2; ++m) {
        f32x4 mean4, rstd4;
#pragma unroll
        for (int r = 0; r < 4; ++r) {
            const float s2 = __uint_as_float(__builtin_amdgcn_ds_bpermute(4 * (20 * g + r), __float_as_uint(sq[m][r])));
            const float mean = rs[m][r] * (1.f / 640.f);
            const float rstd = __builtin_amdgcn_rsqf(fmaxf(s2 * (1.f / 640.f) - mean * mean, 0.f) + 1e-5f);
            mean4[r] = mean; rstd4[r] = rstd;
            lnA[m][r] = rstd; lnB[m][r] = -mean * rstd;
        }
        *(LAS f32x4*)(lb + L_MEAN + (32 * w + 16 * m + 4 * g) * 4) = mean4;
        *(LAS f32x4*)(lb + L_RSTD + (32 * w + 16 * m + 4 * g) * 4) = rstd4;
    }
    BAR();
    STAMP(31);
    {
        float cs[5];
#pragma unroll
        for (int t = 0; t < 4; ++t) cs[t] = ((LAS float*)(lb + L_CSKV))[16 * t + n];
        {
            LAS float* c4 = (LAS float*)(lb + L_CS4);
            cs[4] = (c4[n] + c4[16 + n]) + (c4[32 + n] + c4[48 + n]);
        }
        s16x8 F[4], MA, MB;
        sfor<0, 5>([&](auto TT) {
            constexpr int t = TT.v;
            issueQ(IC<2>{}, TT);
            f32x4 v[2], vb_[2];
#pragma unroll
            for (int m = 0; m < 2; ++m)
#pragma unroll
                for (int r = 0; r < 4; ++r) {
                    const float y = lnA[m][r] * acc[t][m][r] + lnB[m][r] * cs[t];
                    if (t < 2) v[m][r] = phi(y);
                    else if (t < 4) v[m][r] = y;
                    else { v[m][r] = n < 8 ? phi(y) : y; vb_[m][r] = n < 8 ? 1.f : y; }
                }
            if constexpr (t < 4) F[t] = pack8(v[0], v[1]);
            else { MA = pack8(v[0], v[1]); MB = pack8(vb_[0], vb_[1]); }
            SB();
        });
        STAMP(33);
        f32x4 P1 = mfma(F[0], F[2], z4), P2 = mfma(F[0], MB, z4), P3 = mfma(MA, F[2], z4), P4 = mfma(MA, MB, z4);
        f32x4 P5 = mfma(F[1], F[3], z4), P6 = mfma(F[1], MB, z4), P7 = mfma(MA, F[3], z4);
        *(LAS f32x4*)(rda[0]) = P1;
        *(LAS f32x4*)(rda[0] + 1680) = P5;
        *(LAS f32x4*)(rda[1]) = P2;
        *(LAS f32x4*)(rda[2]) = P6;
        *(LAS f32x4*)(rda[3]) = (g == 0 ? P3 : P7);
        *(LAS f32x4*)(rda[4]) = P4;
    }
    STAMP(34);
    BAR();
    STAMP(35);
    if (tid < 210) {
        LAS char* red = (LAS char*)(lb + L_A + 20480 + tid * 16);
        f32x4 s = (*(LAS f32x4*)(red) + *(LAS f32x4*)(red + 3360)) + (*(LAS f32x4*)(red + 6720) + *(LAS f32x4*)(red + 10080));
        st16_sc1((unsigned)(tid * 16), ws + WS_KVP + (size_t)(j1 * 16 + i1) * 3360, __builtin_bit_cast(u32x4, s));
    }
    STAMP(36);
    drain_bar();
    if (tid < 16) flags_store(ws, 0, tid * 16 + j1, i1, tag);
    if (tid == 16) __hip_atomic_store((GAS u64*)(ws + WS_GFLAG) + b, tag, RLX_AGENT);
    STAMP(2);

    unsigned voff160[5];
    {
        int lane2 = lane; asm volatile("" : "+v"(lane2));
#pragma unroll
        for (int u = 0; u < 5; ++u) { int q = 64 * u + lane2, row = q / 10, s = q - row * 10; voff160[u] = row * 1280 + 160 * c8 + 16 * s; }
    }
    s16x8 kp[16];
    u64 gf[4];
    f32x4 aq[3][2];
    unsigned oa[12];
    const char* worun = wo + (size_t)w * (32 * 1280);
    {
        const unsigned vQ1 = lb + L_B + ro1 * 80 + 8 * p4, vQ2 = lb + L_B + ro2 * 80 + 8 * p4;
        const int to = (p4 == 0 ? 32 : p4 == 1 ? 72 : p4 == 2 ? 32 : 72);
        const unsigned vT1 = lb + L_B + ro1 * 80 + to, vT2 = lb + L_B + ro2 * 80 + to;
        const int to2 = (p4 & 1) ? 72 : 32;
        const unsigned vC1 = w < 2 ? vQ1 + 40 * w : vT1, vC2 = w < 2 ? vQ2 + 40 * w : vT2;
        (void)to2;
        f32x4 cq = z4;
#pragma unroll
        for (int t = 0; t < 3; ++t) aq[t][0] = aq[t][1] = z4;
        s16x4 Wl[2][4], Wh[2][4];
        auto side = [&](auto KS, auto I) {
            constexpr int ks = KS.v, i = I.v, r = ks & 1, t = i >> 1;
            constexpr unsigned ko = wq_pos(ks >> 1) + (ks & 1) * 2560;
            if constexpr (t < 2) { if constexpr (i & 1) Wh[r][t] = trread(vQ2 + ko + 40 * t); else Wl[r][t] = trread(vQ1 + ko + 40 * t); }
            else if constexpr (t == 2) { if constexpr (i & 1) Wh[r][2] = trread(vT2 + ko); else Wl[r][2] = trread(vT1 + ko); }
            else { if constexpr (i & 1) Wh[r][3] = trread(vC2 + ko); else Wl[r][3] = trread(vC1 + ko); }
        };
        sfor<0, 8>([&](auto I) { side(IC<0>{}, I); });
        SB();
        const char* kb = ws + WS_KVP + (size_t)j1 * 16 * 3360;
        const unsigned kvo = (tid < 210 ? tid : 209) * 16;
        {
            const unsigned kt = lb + L_KVT, zz = lb + L_ZERO;
            auto ka = [&](int head, int e, int c) { return kt + (head * 420 + e * 20 + 4 * c) * 2; };
            oa[0] = ka(0, n, g);  oa[1] = g == 0 ? ka(0, n, 4) : zz;
            oa[2] = ka(1, n, g);  oa[3] = g == 1 ? ka(1, n, 4) : zz;
            oa[4] = ka(0, 20, g); oa[5] = g == 0 ? ka(0, 20, 4) : zz;
            oa[6] = ka(1, 20, g); oa[7] = g == 1 ? ka(1, 20, 4) : zz;
            oa[8] = n < 4 ? ka(0, 16 + n, g) : zz;                oa[9] = (n < 4 && g == 0) ? ka(0, 16 + n, 4) : zz;
            oa[10] = (n >= 4 && n < 8) ? ka(1, 12 + n, g) : zz;   oa[11] = (n >= 4 && n < 8 && g == 1) ? ka(1, 12 + n, 4) : zz;
            asm volatile("" : "+v"(oa[0]), "+v"(oa[1]), "+v"(oa[2]), "+v"(oa[3]), "+v"(oa[4]), "+v"(oa[5]), "+v"(oa[6]), "+v"(oa[7]),
                         "+v"(oa[8]), "+v"(oa[9]), "+v"(oa[10]), "+v"(oa[11]));
        }
        sfor<0, 20>([&](auto KS) {
            constexpr int ks = KS.v, r = ks & 1;
            if constexpr (ks == 12) { STAMP(3); await<63>(ws, 0, i1 * 16 + j1, tag, w, lane); STAMP(4); SB(); }
            sfor<0, 8>([&](auto I) {
                constexpr int i = I.v;
                if constexpr (i < 6) { constexpr int t = i >> 1, m = i & 1; aq[t][m] = mfma(cat(Wl[r][t], Wh[r][t]), X[ks][m], aq[t][m]); }
                else if constexpr (i == 6) cq = mfma(ones, cat(Wl[r][3], Wh[r][3]), cq);
                if constexpr (ks < 19) side(IC<ks + 1>{}, I);
                if constexpr ((i == 2 || i == 6) && ks < 9) {
                    constexpr int pc_ = 2 * ks + (i == 6), tt = pc_ / 5, u = pc_ % 5;
                    glds16<tt * 20480 + u * 1024>(voff160[u], worun, ldsWw);
                    if constexpr (u == 4) { worun += 128 * 1280; OPAQUE_S(worun); }
                }
                if constexpr (ks == 18 && i < 4) ld8_sc1(gf[i], (unsigned)(lane * 8 + i * 512), ws + WS_GFLAG);
                if constexpr (ks >= 12 && ks < 18 && (i == 1 || i == 4 || i == 7) && 3 * (ks - 12) + i / 3 < 16) {
                    constexpr int pi = 3 * (ks - 12) + i / 3;
                    ld16_sc1(kp[pi], kvo + pi * 3360, kb);
                }
                SB();
            });
        });
        STAMP(37);
        { const unsigned hi = pk2(cq[0], cq[0]); const float lo = cq[0] - bflo(hi);
          if (g == 0 && w < 3) *(LAS u32x2*)(lb + L_CSQ + w * 128 + n * 8) = u32x2{hi, pk2(lo, lo)}; }
    }

    bool gok;
    {
        asm volatile("s_waitcnt vmcnt(0)" : "+v"(kp[0]), "+v"(kp[1]), "+v"(kp[2]), "+v"(kp[3]), "+v"(kp[4]), "+v"(kp[5]), "+v"(kp[6]), "+v"(kp[7]),
                     "+v"(kp[8]), "+v"(kp[9]), "+v"(kp[10]), "+v"(kp[11]), "+v"(kp[12]), "+v"(kp[13]), "+v"(kp[14]), "+v"(kp[15]),
                     "+v"(gf[0]), "+v"(gf[1]), "+v"(gf[2]), "+v"(gf[3])::"memory");
        gok = __all(gf[0] == tag && gf[1] == tag && gf[2] == tag && gf[3] == tag);
        f32x4 s0 = __builtin_bit_cast(f32x4, kp[0]), s1 = __builtin_bit_cast(f32x4, kp[1]);
#pragma unroll
        for (int i = 2; i < 16; i += 2) { s0 += __builtin_bit_cast(f32x4, kp[i]); s1 += __builtin_bit_cast(f32x4, kp[i + 1]); }
        if (tid < 210) *(LAS u32x2*)(lb + L_KVT + tid * 8) = pack4(s0 + s1);
    }
    STAMP(38);
    BAR();
    STAMP(39);
    if (w < 2) sfor<0, 5>([&](auto U) { glds16<U.v * 1024>(voff160[U.v], x + (size_t)(8 * i2 + 4 * half + 2 * w) * 16 * 1280, lb + L_XRES + w * 5120); });
    {
        auto rd2 = [&](unsigned a0, unsigned a1) { const u32x2 lo = *(LAS u32x2*)a0, hi = *(LAS u32x2*)a1; return __builtin_bit_cast(s16x8, u32x4{lo[0], lo[1], hi[0], hi[1]}); };
        s16x8 Fa[2], Fb[2];
        {
            const unsigned csa = g == 0 ? lb + L_CSQ + n * 8 : lb + L_ZERO;
            s16x8 cf[3];
#pragma unroll
            for (int t = 0; t < 3; ++t) { const u32x2 c = *(LAS u32x2*)(csa + (g == 0 ? t * 128 : 0)); cf[t] = __builtin_bit_cast(s16x8, u32x4{c[0], c[1], 0u, 0u}); }
#pragma unroll
            for (int m = 0; m < 2; ++m) {
                const float nm = -((LAS float*)(lb + L_MEAN))[32 * w + 16 * m + n], rstd = ((LAS float*)(lb + L_RSTD))[32 * w + 16 * m + n];
                const unsigned hi = pk2(nm, nm);
                const unsigned pr = g == 0 ? ((hi & 0xffffu) | (pk2(nm - bflo(hi), 0.f) << 16)) : 0u;
                const s16x8 hm = __builtin_bit_cast(s16x8, u32x4{pr, pr, 0u, 0u});
                f32x4 v[3];
#pragma unroll
                for (int t = 0; t < 3; ++t) {
                    const f32x4 y = mfma(cf[t], hm, aq[t][m]) * rstd;
#pragma unroll
                    for (int r = 0; r < 4; ++r) v[t][r] = phi(y[r]);
                }
                const s16x8 q2 = pack8(v[0], v[2]), q3 = pack8(v[1], v[2]);
                u32x4 u2 = __builtin_bit_cast(u32x4, q2), u3 = __builtin_bit_cast(u32x4, q3);
                if (g >= 2) { u2[2] = u2[3] = 0u; u3[2] = u3[3] = 0u; }
                Fa[m] = __builtin_bit_cast(s16x8, u2); Fb[m] = __builtin_bit_cast(s16x8, u3);
            }
        }
        const s16x8 Ama = rd2(oa[0], oa[1]), Amb = rd2(oa[2], oa[3]), Aza = rd2(oa[4], oa[5]), Azb = rd2(oa[6], oa[7]), Ata = rd2(oa[8], oa[9]), Atb = rd2(oa[10], oa[11]);
#pragma unroll
        for (int m = 0; m < 2; ++m) {
            f32x4 Da = mfma(Ama, Fa[m], z4), Db = mfma(Amb, Fb[m], z4);
            f32x4 Dt = mfma(Atb, Fb[m], mfma(Ata, Fa[m], z4));
            f32x4 Dza = mfma(Aza, Fa[m], z4), Dzb = mfma(Azb, Fb[m], z4);
            const float ra = __builtin_amdgcn_rcpf(Dza[0] + 1e-6f), rb = __builtin_amdgcn_rcpf(Dzb[0] + 1e-6f);
            const int mt = 8 * i1 + 2 * w + m;
            s16x8 of = pack8(Da * ra, Db * rb);
            st16_sc1((unsigned)((mt * 20 + j1) * 1024 + lane * 16), ws + WS_O, __builtin_bit_cast(u32x4, of));
            u32x2 tl = pack4(Dt * (g == 0 ? ra : rb));
            const int t = j1 >> 1;
            if (g < 2) st8_sc1((unsigned)((mt * 20 + 16 + (t >> 1)) * 1024 + (n + 16 * (2 * (j1 & 1) + g)) * 16 + (t & 1) * 8), ws + WS_O, tl);
        }
    }
    STAMP(40);
    drain_bar();
    if (tid < 16) flags_store(ws, 1, i1 * 16 + tid, j1, tag);
    STAMP(5);

    const int T0 = j2 < 12 ? 7 * j2 : 84 + 6 * (j2 - 12);
    const int odd = (j2 < 12) ? (j2 & 1) : 0;
    const int Tp0 = T0 + odd;
    const int Tsg = j2 < 12 ? (odd ? T0 : T0 + 6) : Tp0;
    const unsigned vlan16 = lane * 16;
    const int ksb = w < 2 ? 14 * w : 28 + 13 * (w - 2);
    const int nks = w < 2 ? 14 : 13;
    s16x8 V[14][5];
    const char* w2run = ws + WS_W2R + (size_t)(c8 * 54 + ksb) * 5120;
    const int step13 = nks == 14 ? 5120 : 0;
#define ISSUE_V(it, t) do { if constexpr ((it) < 8) ld16a_sc1(V[it][t], vlan16 + (t) * 1024, w2run); else ld16_sc1(V[it][t], vlan16 + (t) * 1024, w2run); \
            if ((t) == 4) { w2run += ((it) == 12 ? step13 : 5120); OPAQUE_S(w2run); } } while (0)
    if (w == 3 && gok) {
#pragma unroll
        for (int k = 0; k < 5; ++k) {
            int li = k * 64 + lane; li = li < 280 ? li : 279; li += rr * 280; li = li < ((x8 & 1) ? 8320 : 8960) ? li : 8319;
            glds4((unsigned)(li * 128), ws + WS_W1R + (x8 & 1) * 56 * 20480, lb + L_JUNK);
        }
    }
    sfor<18, 25>([&](auto P) {
        constexpr int tt = P.v / 5, u = P.v % 5;
        glds16<tt * 20480 + u * 1024>(voff160[u], worun, ldsWw);
        if constexpr (u == 4) { worun += 128 * 1280; OPAQUE_S(worun); }
    });
    const int mtW = 8 * i2 + 4 * half + w;
    STAMP(45);
    await<0>(ws, 1, i2 * 16 + jw, tag, w, lane);
    STAMP(6);
    {
        s16x8 O[20];
        unsigned orun = (unsigned)(mtW * 20 * 1024 + lane * 16);
#define ISSUE_O(i) do { ld16_sc1(O[i], orun, ws + WS_O); orun += 1024u; asm volatile("" : "+v"(orun)); } while (0)
        sfor<0, 8>([&](auto I) { ISSUE_O(I.v); });
        unsigned vA = lb + L_A + (4 * g + q4) * 160 + 8 * p4, vA2 = vA + 8 * 6400;
        const unsigned vB = lb + L_A + (20 * g + 16 + q4) * 160 + 8 * p4;
        unsigned vB2 = vB + 2 * 25600;
        asm volatile("" : "+v"(vA), "+v"(vA2), "+v"(vB2));
        f32x4 a[5];
#pragma unroll
        for (int t = 0; t < 5; ++t) a[t] = z4;
        s16x4 Wl[3][5], Wh[3][5];
        auto side = [&](auto I, auto J) {
            constexpr int i = I.v, j = J.v, r = i % 3, t = j >> 1;
            if constexpr (i < 16) {
                const unsigned a_ = (i < 8 ? vA : vA2) + (i & 7) * 6400 + 32 * t;
                if constexpr (j & 1) Wh[r][t] = trread(a_ + 3200); else Wl[r][t] = trread(a_);
            } else {
                const unsigned a_ = (i < 18 ? vB : vB2) + (i & 1) * 25600 + 32 * t;
                if constexpr (j & 1) Wh[r][t] = trread(a_ + 12800); else Wl[r][t] = trread(a_);
            }
        };
        sfor<0, 2>([&](auto I) { sfor<0, 10>([&](auto J) { side(I, J); }); });
        SB();
        sfor<0, 20>([&](auto I) {
            constexpr int i = I.v;
            constexpr int ny = ((i + 7 < 19 ? i + 7 : 19) - i) + (i > 12 ? (i < 17 ? 2 * (i - 12) : 10) : 0) + (i == 18 ? 1 : i == 19 ? 2 : 0);
            asm volatile("s_waitcnt vmcnt(%1)" : "+v"(O[i]) : "i"(ny) : "memory");
            SB();
            sfor<0, 5>([&](auto T) {
                constexpr int t = T.v;
                a[t] = mfma(cat(Wl[i % 3][t], Wh[i % 3][t]), O[i], a[t]);
                if constexpr (i + 2 < 20) { side(IC<i + 2>{}, IC<2 * t>{}); side(IC<i + 2>{}, IC<2 * t + 1>{}); }
                if constexpr (t == 1 && i + 8 < 20) ISSUE_O(i + 8);
                if constexpr ((i == 17 || i == 18) && t == 1) {
                    int li = w * 68 + (i == 17 ? lane : 64 + (lane < 4 ? lane : 0)); li = li < 270 ? li : 269;
                    glds4((unsigned)((rr * 270 + li) * 128), ws + WS_W2R + (size_t)(x8 & 1) * 4 * 54 * 5120, lb + L_JUNK);
                }
                if constexpr (i >= 12 && i < 17 && (t == 1 || t == 3)) {
                    constexpr int bg = 2 * (i - 12) + (t == 3);
                    const int Tg = bg < 5 ? Tp0 + 5 : Tsg;
                    glds16c<(5 + bg / 5) * 20480 + (bg % 5) * 4096>(vlan16, ws + WS_W1R + (size_t)(Tg * 20 + (bg % 5) * 4 + w) * 1024, lb + w * 1024);
                }
                SB();
            });
        });
        STAMP(28);
        float su = 0.f, s2 = 0.f;
        u32x2 hpk[5];
#pragma unroll
        for (int t = 0; t < 5; ++t) {
            const u32x2 xr = *(LAS u32x2*)(lb + L_XRES + (16 * w + n) * 160 + 32 * t + 8 * g);
            const f32x4 hv = f32x4{bflo(xr[0]), bfhi(xr[0]), bflo(xr[1]), bfhi(xr[1])} + a[t];
            hpk[t] = pack4(hv);
            *(LAS u32x2*)(lb + L_XRES + (16 * w + n) * 160 + 32 * t + 8 * g) = hpk[t];
#pragma unroll
            for (int r = 0; r < 4; ++r) { su += hv[r]; s2 += hv[r] * hv[r]; }
        }
        const unsigned hb = (unsigned)((mtW * 20 + ((5 * c8) >> 1)) * 1024 + lane * 16);
        const int od = c8 & 1;
        st8_sc1(hb + (od ? 8 : 2048), ws + WS_H, od ? hpk[0] : hpk[4]);
        { const u32x2 p0 = od ? hpk[1] : hpk[0], p1 = od ? hpk[2] : hpk[1]; st16_sc1(hb + (od ? 1024 : 0), ws + WS_H, u32x4{p0[0], p0[1], p1[0], p1[1]}); }
        { const u32x2 p0 = od ? hpk[3] : hpk[2], p1 = od ? hpk[4] : hpk[3]; st16_sc1(hb + (od ? 2048 : 1024), ws + WS_H, u32x4{p0[0], p0[1], p1[0], p1[1]}); }
        su = add_xor32(add_xor16(su)); s2 = add_xor32(add_xor16(s2));
        if (g == 0) { u32x2 st = {__float_as_uint(su), __float_as_uint(s2)}; st8_sc1((unsigned)((mtW * 16 + n) * 64 + c8 * 8), ws + WS_STAT2, st); }
    }
    STAMP(42);
    drain_bar();
    if (tid < 16) flags_store(ws, 2, i2 * 16 + tid, jw, tag);
    if (b == 0 && tid == 0) __hip_atomic_store((GAS u64*)ws, tag, RLX_AGENT);
    STAMP(7);

    {
        const char* s1 = ws + WS_W1R + (size_t)(Tp0 * 20 + w) * 1024;
        const unsigned l1 = lb + L_A + w * 1024;
        sfor<0, 25>([&](auto U) { glds16c<U.v * 4096>(vlan16, s1, l1); s1 += 4096; OPAQUE_S(s1); });
    }
    STAMP(43);
    await<0>(ws, 2, i2 * 16 + j2, tag, w, lane);
    STAMP(8);
    {
        s16x8 H[20][2];
        const int mt0 = 8 * i2 + 2 * w;
        s16x8 stv[2];
#pragma unroll
        for (int m = 0; m < 2; ++m) ld16_sc1(stv[m], (unsigned)(((mt0 + m) * 16 + n) * 64 + g * 16), ws + WS_STAT2);
        unsigned hrun = (unsigned)(mt0 * 20 * 1024 + lane * 16);
#define ISSUE_H(ks) do { ld16_sc1(H[ks][0], hrun, ws + WS_H); ld16_sc1(H[ks][1], hrun + 20 * 1024, ws + WS_H); hrun += 1024; asm volatile("" : "+v"(hrun)); } while (0)
        unsigned vF0 = lb + vlan16, vF1 = lb + 61440 + vlan16, vF2 = lb + 122880 + vlan16;
        asm volatile("" : "+v"(vF0), "+v"(vF1), "+v"(vF2));
        auto wread = [&](auto TT, auto KS) -> s16x8 {
            constexpr int t = TT.v, ks = KS.v;
            return *(volatile LAS s16x8*)((t < 3 ? vF0 : t < 6 ? vF1 : vF2) + (t % 3) * 20480 + ks * 1024);
        };
        {
            f32x4 c0 = z4, c1 = z4;
            const unsigned ta = lb + vlan16 + w * 20480, tb = lb + vlan16 + (w < 3 ? w + 4 : w) * 20480;
            s16x8 Ca[4], Cb[4];
            sfor<0, 3>([&](auto K) { Ca[K.v] = *(volatile LAS s16x8*)(ta + K.v * 1024); Cb[K.v] = *(volatile LAS s16x8*)(tb + K.v * 1024); });
            SB();
            sfor<0, 20>([&](auto K) {
                constexpr int k = K.v;
                c0 = mfma(ones, Ca[k & 3], c0);
                if constexpr (k + 3 < 20) Ca[(k + 3) & 3] = *(volatile LAS s16x8*)(ta + (k + 3) * 1024);
                SB();
                c1 = mfma(ones, Cb[k & 3], c1);
                if constexpr (k + 3 < 20) Cb[(k + 3) & 3] = *(volatile LAS s16x8*)(tb + (k + 3) * 1024);
                if constexpr ((k & 1) == 0 && k < 16) ISSUE_H(k >> 1);
                SB();
            });
            auto split = [&](float c) { const unsigned hi = pk2(c, c); const float lo = c - bflo(hi); u32x2 r = {hi, pk2(lo, lo)}; return r; };
            if (g == 0) {
                *(LAS u32x2*)(lb + L_CS1 + w * 128 + n * 8) = split(c0[0]);
                if (w < 3) *(LAS u32x2*)(lb + L_CS1 + (w + 4) * 128 + n * 8) = split(c1[0]);
            }
            if (tid < 2) *(LAS u64*)(lb + L_CS1 + 896 + tid * 8) = 0;
        }
        STAMP(19);
        asm volatile("s_waitcnt vmcnt(16)" : "+v"(stv[0]), "+v"(stv[1])::"memory");
        s16x8 Hm[2];
#pragma unroll
        for (int m = 0; m < 2; ++m) {
            f32x4 s = __builtin_bit_cast(f32x4, stv[m]);
            const float nm = add_xor32(add_xor16(s[0] + s[2])) * (-1.f / 640.f);
            const unsigned hi = pk2(nm, nm);
            const unsigned pr = g == 0 ? ((hi & 0xffffu) | (pk2(nm - bflo(hi), 0.f) << 16)) : 0u;
            Hm[m] = __builtin_bit_cast(s16x8, u32x4{pr, pr, 0u, 0u});
        }
        s16x8 Wf1[3][4];
        sfor<0, 2>([&](auto KS) { sfor<0, 4>([&](auto TT) { Wf1[KS.v][TT.v] = wread(TT, KS); }); });
        __builtin_amdgcn_s_waitcnt(0xC87F);
        BARX();
        const unsigned csa = lb + L_CS1 + (g == 0 ? n * 8 : 896);
        auto csfrag = [&](int t) { const u32x2 c = *(LAS u32x2*)(csa + (g == 0 ? t * 128 : 0)); return __builtin_bit_cast(s16x8, u32x4{c[0], c[1], 0u, 0u}); };
        auto relu2 = [&](unsigned p) { typedef short s2_ __attribute__((ext_vector_type(2))); const s2_ z = {0, 0};
                                       return __builtin_bit_cast(unsigned, __builtin_elementwise_max(__builtin_bit_cast(s2_, p), z)); };
        auto fin8 = [&](f32x4 x0, f32x4 x1) { return u32x4{relu2(pk2(x0[0], x0[1])), relu2(pk2(x0[2], x0[3])), relu2(pk2(x1[0], x1[1])), relu2(pk2(x1[2], x1[3]))}; };
        f32x4 a1[4][2];
        sfor<0, 4>([&](auto TT) { const s16x8 cf = csfrag(TT.v); a1[TT.v][0] = mfma(cf, Hm[0], z4); a1[TT.v][1] = mfma(cf, Hm[1], z4); });
        {
            auto& Wf = Wf1;
            SB();
            sfor<0, 20>([&](auto KS) {
                constexpr int ks = KS.v;
                constexpr int ny = 2 * ((ks + 7 < 19 ? ks + 7 : 19) - ks) + (ks > 12 ? (ks < 17 ? 2 * (ks - 12) : 10) : 0);
                asm volatile("s_waitcnt vmcnt(%2)" : "+v"(H[ks][0]), "+v"(H[ks][1]) : "i"(ny) : "memory");
                SB();
                sfor<0, 8>([&](auto I) {
                    constexpr int i = I.v, t = i >> 1, m = i & 1;
                    a1[t][m] = mfma(Wf[ks % 3][t], H[ks][m], a1[t][m]);
                    if constexpr (ks + 2 < 20 && (i & 1) == 0) Wf[(ks + 2) % 3][t] = wread(IC<t>{}, IC<ks + 2>{});
                    if constexpr (i == 3 && ks + 8 < 20) ISSUE_H(ks + 8);
                    if constexpr (ks >= 12 && ks < 17 && (i == 1 || i == 5)) { constexpr int pv = 2 * (ks - 12) + (i == 5); ISSUE_V(pv / 5, pv % 5); }
                    SB();
                });
            });
        }
        STAMP(20);
        f32x4 a2[3][2];
        sfor<0, 3>([&](auto TT) { const s16x8 cf = csfrag(4 + TT.v); a2[TT.v][0] = mfma(cf, Hm[0], z4); a2[TT.v][1] = mfma(cf, Hm[1], z4); });
        {
            s16x8 Wf[3][3];
            sfor<0, 2>([&](auto KS) { sfor<0, 3>([&](auto TT) { Wf[KS.v][TT.v] = wread(IC<4 + TT.v>{}, KS); }); });
            u32x2 e0, e1;
            sfor<0, 20>([&](auto KS) {
                constexpr int ks = KS.v;
                sfor<0, 6>([&](auto I) {
                    constexpr int i = I.v, t = i >> 1, m = i & 1;
                    a2[t][m] = mfma(Wf[ks % 3][t], H[ks][m], a2[t][m]);
                    if constexpr (ks + 2 < 20 && (i & 1) == 0) Wf[(ks + 2) % 3][t] = wread(IC<4 + t>{}, IC<ks + 2>{});
                    if constexpr (i == 3 || (i == 5 && (ks & 1) == 0)) { constexpr int pv = 10 + (ks >> 1) * 3 + ((ks & 1) ? 2 : (i == 5 ? 1 : 0)); ISSUE_V(pv / 5, pv % 5); }
                    if constexpr (ks >= 1 && ks < 17 && ((ks - 1) & 3) < 3) {
                        constexpr int u = (ks - 1) >> 2, ph = (ks - 1) & 3, P = u >> 1, mm = u & 1;
                        if constexpr (ph == 0 && i == 1) e0 = u32x2{relu2(pk2(a1[2 * P][mm][0], a1[2 * P][mm][1])), relu2(pk2(a1[2 * P][mm][2], a1[2 * P][mm][3]))};
                        if constexpr (ph == 1 && i == 1) e1 = u32x2{relu2(pk2(a1[2 * P + 1][mm][0], a1[2 * P + 1][mm][1])), relu2(pk2(a1[2 * P + 1][mm][2], a1[2 * P + 1][mm][3]))};
                        if constexpr (ph == 2 && i == 1)
                            st16_sc1((unsigned)(((mt0 + mm) * 54 + (Tp0 >> 1) + P) * 1024 + lane * 16), ws + WS_H1, u32x4{e0[0], e0[1], e1[0], e1[1]});
                    }
                    SB();
                });
            });
        }
        STAMP(22);
#pragma unroll
        for (int m = 0; m < 2; ++m) {
            st16_sc1((unsigned)(((mt0 + m) * 54 + (Tp0 >> 1) + 2) * 1024 + lane * 16), ws + WS_H1, fin8(a2[0][m], a2[1][m]));
            if (j2 < 12) st8_sc1((unsigned)(((mt0 + m) * 54 + (Tsg >> 1)) * 1024 + lane * 16 + (Tsg & 1) * 8), ws + WS_H1,
                                 u32x2{relu2(pk2(a2[2][m][0], a2[2][m][1])), relu2(pk2(a2[2][m][2], a2[2][m][3]))});
        }
        STAMP(24);
        drain_bar();
        if (tid < 16) flags_store(ws, 3, i2 * 16 + tid, j2, tag);
        STAMP(9);

        sfor<40, 60>([&](auto U) { ISSUE_V(U.v / 5, U.v % 5); });
        STAMP(44);
        await<63>(ws, 3, i2 * 16 + jw, tag, w, lane);
        STAMP(10);
        unsigned arun = (unsigned)(((8 * i2 + 4 * half) * 54 + ksb) * 1024 + lane * 16);
        const unsigned astep13 = nks == 14 ? 1024u : 0u;
        s16x8 A[4][4];
        unsigned aoff[4];
#pragma unroll
        for (int m = 0; m < 4; ++m) aoff[m] = ((w + m) & 3) * 54 * 1024;
        s16x8 st2v;
        ld16_sc1(st2v, (unsigned)((mtW * 16 + n) * 64 + g * 16), ws + WS_STAT2);
#define ISSUE_A(it, m) do { ld16_sc1(A[(it) & 3][m], arun + aoff[m], ws + WS_H1); \
            if ((m) == 3) { arun += ((it) == 12 ? astep13 : 1024u); asm volatile("" : "+v"(arun)); } } while (0)
        sfor<0, 3>([&](auto IT) { sfor<0, 4>([&](auto M) { ISSUE_A(IT.v, M.v); }); });
        f32x4 a[4][5];
#pragma unroll
        for (int m = 0; m < 4; ++m)
#pragma unroll
            for (int t = 0; t < 5; ++t) a[m][t] = z4;
        sfor<0, 14>([&](auto IT) {
            constexpr int it = IT.v;
            constexpr int ny = [] {
                int seq[200] = {}, n_ = 0;
                for (int a_ = 0; a_ < 3; ++a_) for (int m = 0; m < 4; ++m) seq[n_++] = 1000 + a_;
                for (int j = 0; j < it; ++j)
                    for (int i = 0; i < 20; ++i) {
                        if ((i & 3) == 1 && i < 16 && j + 3 < 14) seq[n_++] = 1000 + j + 3;
                        if ((i == 3 || i == 11) && j < 5) seq[n_++] = 2000 + (60 + 2 * j + (i == 11)) / 5;
                    }
                int need = 0;
                for (int q = 0; q < n_; ++q) if (seq[q] == 1000 + it || seq[q] == 2000 + it) need = q + 1;
                return n_ - need;
            }();
            if constexpr (it == 1) STAMP(25);
            if constexpr (it == 5) STAMP(26);
            if constexpr (it == 9) STAMP(27);
            if constexpr (it < 8)
                asm volatile("s_waitcnt vmcnt(%9)" : "+v"(A[it & 3][0]), "+v"(A[it & 3][1]), "+v"(A[it & 3][2]), "+v"(A[it & 3][3]),
                             "+a"(V[it][0]), "+a"(V[it][1]), "+a"(V[it][2]), "+a"(V[it][3]), "+a"(V[it][4]) : "i"(ny) : "memory");
            else
                asm volatile("s_waitcnt vmcnt(%9)" : "+v"(A[it & 3][0]), "+v"(A[it & 3][1]), "+v"(A[it & 3][2]), "+v"(A[it & 3][3]),
                             "+v"(V[it][0]), "+v"(V[it][1]), "+v"(V[it][2]), "+v"(V[it][3]), "+v"(V[it][4]) : "i"(ny) : "memory");
            SB();
            if (it < 13 || nks == 14) {
                sfor<0, 20>([&](auto I) {
                    constexpr int i = I.v, t = i >> 2, m = i & 3;
                    a[m][t] = mfma(V[it][t], A[it & 3][m], a[m][t]);
                    if constexpr ((i & 3) == 1 && i < 16 && it + 3 < 14) ISSUE_A(it + 3, i >> 2);
                    if constexpr ((i == 3 || i == 11) && it < 5) { constexpr int pv = 60 + 2 * it + (i == 11); ISSUE_V(pv / 5, pv % 5); }
                    SB();
                });
            }
        });
        STAMP(11);
        asm volatile("" : "+v"(st2v));
        const f32x4 sv = __builtin_bit_cast(f32x4, st2v);
        const float smean = add_xor32(add_xor16(sv[0] + sv[2])) * (1.f / 640.f);
        const float rstd2 = __builtin_amdgcn_rsqf(fmaxf(add_xor32(add_xor16(sv[1] + sv[3])) * (1.f / 640.f) - smean * smean, 0.f) + 1e-5f);
#pragma unroll
        for (int m = 1; m < 4; ++m)
#pragma unroll
            for (int t = 0; t < 5; ++t) *(LAS f32x4*)(lb + ((((w + m) & 3) * 3 + (m - 1)) * 5 + t) * 1024 + lane * 16) = a[m][t];
        BAR();
        STAMP(46);
        f32x4 fin[5];
#pragma unroll
        for (int t = 0; t < 5; ++t) {
            const LAS char* p = (const LAS char*)(lb + (w * 15 + t) * 1024 + lane * 16);
            fin[t] = ((a[0][t] + *(const LAS f32x4*)p) + (*(const LAS f32x4*)(p + 5120) + *(const LAS f32x4*)(p + 10240))) * rstd2;
        }
        STAMP(47);
        LAS char* ot = (LAS char*)(lb + L_B + w * 2560);
#pragma unroll
        for (int t = 0; t < 5; ++t) {
            const u32x2 hp = *(LAS u32x2*)(lb + L_XRES + (16 * w + n) * 160 + 32 * t + 8 * g);
            const f32x4 ov = f32x4{bflo(hp[0]), bfhi(hp[0]), bflo(hp[1]), bfhi(hp[1])} + fin[t];
            *(LAS u32x2*)(ot + n * 160 + 32 * t + 8 * g) = pack4(ov);
        }
        asm volatile("s_waitcnt lgkmcnt(0)" ::: "memory");
        int lane3 = lane; asm volatile("" : "+v"(lane3));
#pragma unroll
        for (int u = 0; u < 3; ++u) {
            const int q = 64 * u + lane3, row = q / 10, sl = q - row * 10;
            if (q < 160) *(GAS u32x4*)(out + (size_t)(mtW * 16 + row) * 1280 + 160 * c8 + 16 * sl) = *(LAS u32x4*)(ot + q * 16);
        }
    }
    STAMP(12); STAMPRT(63);
}

extern "C" void kernel_launch(void* const* d_in, const int* in_sizes, int n_in, void* d_out, int out_size, void* d_ws, size_t ws_size,
                              hipStream_t stream) {
    (void)in_sizes; (void)n_in; (void)out_size; (void)ws_size;
    fused_block<<<256, 256, 0, stream>>>((const char*)d_in[0], (const char*)d_in[1], (const char*)d_in[2], (const char*)d_in[4],
                                         (const char*)d_in[5], (char*)d_out, (char*)d_ws);
}
```

```cpp
#include <hip/hip_runtime.h>
#include <stdint.h>
#pragma clang diagnostic ignored "-Wint-to-pointer-cast"

typedef short s16x4 __attribute__((ext_vector_type(4)));
typedef short s16x8 __attribute__((ext_vector_type(8)));
typedef float f32x4 __attribute__((ext_vector_type(4)));
typedef unsigned u32x4 __attribute__((ext_vector_type(4)));
typedef unsigned u32x2 __attribute__((ext_vector_type(2)));
typedef unsigned long long u64;
#define LAS __attribute__((address_space(3)))
#define GAS __attribute__((address_space(1)))
#define DI __device__ __forceinline__
#define RLX_AGENT __ATOMIC_RELAXED, __HIP_MEMORY_SCOPE_AGENT
#define SB() __builtin_amdgcn_sched_barrier(0)

constexpr size_t WS_FLAG = 4096;
constexpr size_t WS_GFLAG = 2048;
constexpr size_t WS_KVP = 262144;
constexpr size_t WS_STAT2 = 0x120000;
constexpr size_t WS_O = 2u << 20;
constexpr size_t WS_H = 5u << 20;
constexpr size_t WS_H1 = 8u << 20;
constexpr size_t WS_W1R = 16u << 20;
constexpr size_t WS_W2R = 19u << 20;
constexpr size_t WS_STAMP = 24u << 20;

constexpr int L_A = 0;
constexpr int L_B = 102400;
constexpr int L_XRES = L_B + 40960;
constexpr int L_M = 155648;
constexpr int L_MEAN = L_M;
constexpr int L_RSTD = L_M + 512;
constexpr int L_CSKV = L_M + 1024;
constexpr int L_CS4 = L_M + 1280;
constexpr int L_KVT = L_M + 1536;
constexpr int L_CS1 = L_M + 3328;
constexpr int L_JUNK = L_M + 5632;
constexpr int L_ZERO = L_M + 5888;
constexpr int L_CSQ = L_M + 5904 + 16;
constexpr int L_EPOCH = L_M + 7424;
constexpr int L_TRASH = L_M + 6400;
constexpr int LDS_BYTES = 163840;
constexpr int CODE_LINES = 368;

enum { OP_X, OP_W, OP_R, OP_O, OP_Q, OP_T, OP_S, OP_U };
struct P1Op { int kind, a, b, ks, slot; };
constexpr P1Op P1SEQ[] = {
    {OP_O, 0, 0, -1, 0},
    {OP_U, 0, 0, -1, 0}, {OP_U, 1, 0, -1, 0},
    {OP_X, 0, 0, -1, 0}, {OP_X, 0, 1, -1, 0}, {OP_X, 0, 2, -1, 0}, {OP_X, 0, 3, -1, 0},
    {OP_W, 0, 0, -1, 0}, {OP_W, 0, 1, -1, 0}, {OP_W, 0, 2, -1, 0}, {OP_W, 0, 3, -1, 0}, {OP_W, 0, 4, -1, 0},
    {OP_U, 6, 0, -1, 0},
    {OP_U, 2, 0, -1, 0}, {OP_U, 3, 0, -1, 0},
    {OP_X, 1, 0, -1, 0}, {OP_X, 1, 1, -1, 0}, {OP_X, 1, 2, -1, 0}, {OP_X, 1, 3, -1, 0},
    {OP_O, 1, 0, -1, 0},
    {OP_W, 1, 0, -1, 0}, {OP_W, 1, 1, -1, 0}, {OP_W, 1, 2, -1, 0}, {OP_W, 1, 3, -1, 0}, {OP_W, 1, 4, -1, 0},
    {OP_X, 2, 0, -1, 0}, {OP_X, 2, 1, -1, 0}, {OP_X, 2, 2, -1, 0}, {OP_X, 2, 3, -1, 0},
    {OP_W, 2, 0, 0, 3}, {OP_W, 2, 1, 0, 8}, {OP_W, 2, 2, 0, 13},
    {OP_X, 3, 0, 1, 2}, {OP_X, 3, 1, 1, 6}, {OP_X, 3, 2, 1, 10}, {OP_X, 3, 3, 1, 14},
    {OP_W, 2, 3, 2, 3}, {OP_W, 2, 4, 2, 8}, {OP_W, 3, 0, 2, 13},
    {OP_X, 4, 0, 3, 2}, {OP_X, 4, 1, 3, 6}, {OP_X, 4, 2, 3, 10}, {OP_X, 4, 3, 3, 14},
    {OP_W, 3, 1, 4, 3}, {OP_U, 4, 0, 4, 5}, {OP_W, 3, 2, 4, 8}, {OP_W, 3, 3, 4, 13},
    {OP_X, 5, 0, 5, 2}, {OP_X, 5, 1, 5, 6}, {OP_X, 5, 2, 5, 10}, {OP_X, 5, 3, 5, 14},
    {OP_W, 3, 4, 6, 3}, {OP_W, 4, 0, 6, 8}, {OP_W, 4, 1, 6, 13},
    {OP_X, 6, 0, 7, 2}, {OP_X, 6, 1, 7, 6}, {OP_X, 6, 2, 7, 10}, {OP_X, 6, 3, 7, 14},
    {OP_W, 4, 2, 8, 3}, {OP_W, 4, 3, 8, 8}, {OP_W, 4, 4, 8, 13},
    {OP_X, 7, 0, 9, 2}, {OP_X, 7, 1, 9, 6}, {OP_X, 7, 2, 9, 10}, {OP_X, 7, 3, 9, 14},
    {OP_R, 0, 0, 10, 3}, {OP_R, 1, 0, 10, 8}, {OP_R, 2, 0, 10, 13},
    {OP_X, 8, 0, 11, 2}, {OP_X, 8, 1, 11, 6}, {OP_X, 8, 2, 11, 10}, {OP_X, 8, 3, 11, 14},
    {OP_R, 3, 0, 12, 3}, {OP_R, 4, 0, 12, 8}, {OP_U, 5, 0, 12, 13},
    {OP_X, 9, 0, 13, 2}, {OP_X, 9, 1, 13, 6}, {OP_X, 9, 2, 13, 10}, {OP_X, 9, 3, 13, 14},
    {OP_Q, 0, 0, 15, 3}, {OP_Q, 0, 1, 15, 8}, {OP_Q, 0, 2, 15, 13}, {OP_Q, 0, 3, 16, 3}, {OP_Q, 0, 4, 16, 8},
    {OP_T, 0, 0, 17, 1}, {OP_T, 1, 0, 17, 2}, {OP_Q, 1, 0, 17, 3}, {OP_T, 2, 0, 17, 5}, {OP_T, 3, 0, 17, 6}, {OP_T, 4, 0, 17, 7},
    {OP_Q, 1, 1, 17, 8}, {OP_Q, 1, 2, 17, 13},
    {OP_S, 0, 0, 18, 1}, {OP_Q, 1, 3, 18, 3}, {OP_S, 1, 0, 18, 6}, {OP_Q, 1, 4, 18, 8}, {OP_S, 2, 0, 18, 11}, {OP_S, 3, 0, 19, 2}, {OP_S, 4, 0, 19, 7},
};
constexpr int P1N = sizeof(P1SEQ) / sizeof(P1SEQ[0]);
constexpr int p1_wait(int s) {
    int issued = 0, need = 0;
    for (int i = 0; i < P1N; ++i) {
        if (P1SEQ[i].ks < 2 * s - 1 || P1SEQ[i].ks < 0) issued = i + 1;
        if ((P1SEQ[i].kind == OP_X && P1SEQ[i].a == s) || (P1SEQ[i].kind == OP_W && P1SEQ[i].a <= (s >> 1))) need = need > i + 1 ? need : i + 1;
    }
    return issued - need;
}
constexpr bool p1_ok() { for (int s = 0; s < 10; ++s) if (p1_wait(s) < 0 || p1_wait(s) > 63) return false; return true; }
static_assert(p1_ok(), "P1a schedule: an op is waited for before it is issued");

#define STAMP(i) do {} while (0)
#define STAMPRT(i) do {} while (0)

template <int I> struct IC { static constexpr int v = I; };
template <int B, int E, class F> DI void sfor(F&& f) { if constexpr (B < E) { f(IC<B>{}); sfor<B + 1, E>(f); } }
#define OPAQUE_S(p) asm volatile("" : "+s"(p))

DI f32x4 mfma(s16x8 a, s16x8 b, f32x4 c) { return __builtin_amdgcn_mfma_f32_16x16x32_bf16(a, b, c, 0, 0, 0); }
DI s16x4 trread(unsigned addr) { return __builtin_bit_cast(s16x4, __builtin_amdgcn_ds_read_tr16_b64_v4i16((LAS s16x4*)addr)); }
DI s16x8 cat(s16x4 a, s16x4 b) { return __builtin_shufflevector(a, b, 0, 1, 2, 3, 4, 5, 6, 7); }
DI unsigned pk2(float lo, float hi) {
    typedef __bf16 bf2 __attribute__((ext_vector_type(2)));
    typedef float f2 __attribute__((ext_vector_type(2)));
    f2 v = {lo, hi};
    return __builtin_bit_cast(unsigned, __builtin_convertvector(v, bf2));
}
DI s16x8 pack8(f32x4 a, f32x4 b) {
    u32x4 r = {pk2(a[0], a[1]), pk2(a[2], a[3]), pk2(b[0], b[1]), pk2(b[2], b[3])};
    return __builtin_bit_cast(s16x8, r);
}
DI u32x2 pack4(f32x4 a) { u32x2 r = {pk2(a[0], a[1]), pk2(a[2], a[3])}; return r; }
DI float bflo(unsigned u) { return __uint_as_float(u << 16); }
DI float bfhi(unsigned u) { return __uint_as_float(u & 0xffff0000u); }
DI float phi(float t) { return (fmaxf(t, 0.f) + 1e-6f) + __builtin_amdgcn_fmed3f(__builtin_amdgcn_exp2f(t * 1.44269504088896f), 0.f, 1.f); }
DI float add_xor16(float v) { const unsigned u = __float_as_uint(v); auto r = __builtin_amdgcn_permlane16_swap(u, u, false, false); return __uint_as_float(r[0]) + __uint_as_float(r[1]); }
DI float add_xor32(float v) { const unsigned u = __float_as_uint(v); auto r = __builtin_amdgcn_permlane32_swap(u, u, false, false); return __uint_as_float(r[0]) + __uint_as_float(r[1]); }
constexpr int wq_pos(int grp) { return grp < 3 ? 16384 + 5120 * grp : grp < 7 ? 32768 + 5120 * (grp - 3) : 5120 * (grp - 7); }

template <int LOFF> DI void glds16(unsigned voff, const void* sbase, unsigned lds) {
    asm volatile("s_add_u32 m0, %2, %3\n\ts_nop 0\n\tglobal_load_lds_dwordx4 %0, %1" ::"v"(voff), "s"(sbase), "s"(lds), "i"(LOFF) : "memory", "scc");
}
template <int LOFF, int GOFF> DI void glds16i(unsigned voff, const void* sbase, unsigned lds) {
    asm volatile("s_add_u32 m0, %2, %3\n\ts_nop 0\n\tglobal_load_lds_dwordx4 %0, %1 offset:%4" ::"v"(voff), "s"(sbase), "s"(lds), "i"(LOFF - GOFF), "i"(GOFF) : "memory", "scc");
}
template <int LOFF> DI void glds16c(unsigned voff, const void* sbase, unsigned lds) {
    asm volatile("s_add_u32 m0, %2, %3\n\ts_nop 0\n\tglobal_load_lds_dwordx4 %0, %1 sc1" ::"v"(voff), "s"(sbase), "s"(lds), "i"(LOFF) : "memory", "scc");
}
DI void glds4(unsigned voff, const void* sbase, unsigned lds) {
    asm volatile("s_mov_b32 m0, %2\n\ts_nop 0\n\tglobal_load_lds_dword %0, %1" ::"v"(voff), "s"(sbase), "s"(lds) : "memory");
}
#define WAITV(n) asm volatile("s_waitcnt vmcnt(%0)" ::"i"(n) : "memory")
#define BARX() do { __builtin_amdgcn_s_barrier(); asm volatile("" ::: "memory"); } while (0)
#define BAR() do { asm volatile("s_waitcnt lgkmcnt(0)" ::: "memory"); __builtin_amdgcn_s_barrier(); asm volatile("" ::: "memory"); } while (0)

DI void ld16_sc1(s16x8& d, unsigned voff, const void* sbase) {
    asm volatile("global_load_dwordx4 %0, %1, %2 sc1" : "=v"(d) : "v"(voff), "s"(sbase) : "memory");
}
DI void ld16a_sc1(s16x8& d, unsigned voff, const void* sbase) {
    asm volatile("global_load_dwordx4 %0, %1, %2 sc1" : "=a"(d) : "v"(voff), "s"(sbase) : "memory");
}
DI void ld8_sc1(u64& d, unsigned voff, const void* sbase) { asm volatile("global_load_dwordx2 %0, %1, %2 sc1" : "=v"(d) : "v"(voff), "s"(sbase) : "memory"); }
DI void st16_sc1(unsigned voff, void* sbase, u32x4 v) {
    asm volatile("global_store_dwordx4 %0, %1, %2 sc1\n\ts_nop 1" ::"v"(voff), "v"(v), "s"(sbase) : "memory");
}
DI void st8_sc1(unsigned voff, void* sbase, u32x2 v) {
    asm volatile("global_store_dwordx2 %0, %1, %2 sc1" ::"v"(voff), "v"(v), "s"(sbase) : "memory");
}
DI void drain_bar() { WAITV(0); BAR(); }
DI void flags_store(char* ws, int arr, int line, int slot, u64 tag) {
    __hip_atomic_store((GAS u64*)(ws + WS_FLAG + (size_t)arr * 32768 + (size_t)line * 128) + slot, tag, RLX_AGENT);
}
template <int NV> DI void await(char* ws, int arr, int line, u64 tag, int w, int lane) {
    if (w == 0) {
        GAS u64* fl = (GAS u64*)(ws + WS_FLAG + (size_t)arr * 32768 + (size_t)line * 128) + (lane & 15);
        for (unsigned spins = 0; spins < (1u << 20); ++spins) {
            u64 f = __hip_atomic_load(fl, RLX_AGENT);
            if (__all(f == tag)) break;
        }
    }
    WAITV(NV);
    BAR();
}

__global__ void __launch_bounds__(256, 1)
fused_block(const char* __restrict__ x, const char* __restrict__ wqkv, const char* __restrict__ wo, const char* __restrict__ w1,
            const char* __restrict__ w2, char* __restrict__ out, char* __restrict__ ws) {
    __shared__ __attribute__((aligned(16))) char smem[LDS_BYTES];
    const unsigned lb = (unsigned)(uintptr_t)(LAS char*)smem;
    const int tid = threadIdx.x, lane = tid & 63;
    const int w = __builtin_amdgcn_readfirstlane(tid >> 6);
    const int b = blockIdx.x, x8 = b & 7, rr = b >> 3;
    const int i1 = 4 * (x8 >> 1) + (rr >> 3), j1 = 8 * (x8 & 1) + (rr & 7);
    const int i2 = i1, j2 = j1;
    const int half = (rr >> 2) & 1, c8 = 4 * (x8 & 1) + (rr & 3), jw = 8 * half + c8;
    const int g = lane >> 4, n = lane & 15, q4 = n >> 2, p4 = lane & 3;
    const s16x8 ones = {0x3F80, 0x3F80, 0x3F80, 0x3F80, 0x3F80, 0x3F80, 0x3F80, 0x3F80};
    const f32x4 z4 = {0, 0, 0, 0};
    STAMP(0); STAMPRT(62);

    s16x8 X[20][2];
    f32x4 acc[5][2], sq[2], rs[2], csA, csB;
    unsigned voffW[5], voffX[4];
    const char* xb = x + (size_t)i1 * 128 * 1280;
    const unsigned ldsXw = lb + L_B + w * 4096, ldsWw = lb + L_A + w * 5120;
#pragma unroll
    for (int u = 0; u < 4; ++u)
        voffX[u] = (32 * w + 8 * u + (lane >> 3)) * 1280 + 16 * ((lane & 7) ^ ((lane >> 4) + 4 * (u & 1)));
#define ISSUE_X(s, u) glds16i<((s) % 3) * 16384 + (u) * 1024, (s) * 128>(voffX[u], xb, ldsXw)
#define ISSUE_XS(s) do { ISSUE_X(s, 0); ISSUE_X(s, 1); ISSUE_X(s, 2); ISSUE_X(s, 3); } while (0)
#pragma unroll
    for (int u = 0; u < 5; ++u) {
        int q = 64 * u + lane, row = q / 10, s = q - row * 10;
        row ^= ((row >> 3) & 1) << 2;
        voffW[u] = row * 3840 + (s < 5 ? (640 + 40 * j1) * 2 + 16 * s : (1280 + 40 * j1) * 2 + 16 * (s - 5));
    }
    const char* wkb = wqkv + (size_t)w * (32 * 3840);
    const char* wrun = wkb;
#define ISSUE_W(t, u) do { glds16<(t) * 20480 + (u) * 1024>(voffW[u], wrun, ldsWw); if ((u) == 4) { wrun += 128 * 3840; OPAQUE_S(wrun); } } while (0)
    unsigned voffQ[5];
    auto make_voffQ = [&] {
        int l2 = lane; asm volatile("" : "+v"(l2));
#pragma unroll
        for (int u = 0; u < 5; ++u) {
            int q = 64 * u + l2, row = q / 5, s = q - row * 5;
            row ^= ((row >> 3) & 1) << 2;
            voffQ[u] = row * 3840 + 80 * j1 + 16 * s;
        }
        asm volatile("" : "+v"(voffQ[0]), "+v"(voffQ[1]), "+v"(voffQ[2]), "+v"(voffQ[3]), "+v"(voffQ[4]));
    };
    auto issueQ = [&](auto A, auto U) {
        constexpr int a = A.v, u = U.v;
        const int grp = a == 0 ? (w < 2 ? w : 2) : a == 1 ? 3 + w : 7 + (w < 2 ? w : 2);
        const int pos = a == 0 ? 16384 + 5120 * grp : a == 1 ? 32768 + 5120 * w : 5120 * (grp - 7);
        glds16<u * 1024>(voffQ[u], wqkv + (size_t)grp * (64 * 3840), lb + L_B + pos);
    };
    const unsigned raR = lb + L_A + (4 * g + q4) * 128 + 16 * ((2 * w + (p4 >> 1)) ^ ((((4 * g + q4) >> 1) & 3) << 1)) + 8 * (p4 & 1);
    s16x8 Rf[5];
    auto runit = [&](auto R, unsigned& isw1, unsigned& ksr, unsigned& cl) {
        constexpr int r = R.v;
        const unsigned ub = (unsigned)b;
        unsigned v;
        if constexpr (r < 2) { isw1 = 1; v = ub + 256 * r; }
        else if constexpr (r == 2) { isw1 = ub < 28; v = isw1 ? ub + 512 : ub - 28; }
        else if constexpr (r == 3) { isw1 = 0; v = ub + 228; }
        else { isw1 = 0; v = ub < 56 ? ub + 484 : ub + 228; }
        ksr = isw1 ? (v * 2428u) >> 16 : (v * 6554u) >> 16;
        cl = v - ksr * (isw1 ? 27u : 10u);
    };
    auto p1issue = [&](auto N) {
        constexpr P1Op op = P1SEQ[N.v];
        if constexpr (op.kind == OP_X) ISSUE_X(op.a, op.b);
        else if constexpr (op.kind == OP_W) ISSUE_W(op.a, op.b);
        else if constexpr (op.kind == OP_R) {
            unsigned isw1, ksr, cl;
            runit(IC<op.a>{}, isw1, ksr, cl);
            const unsigned pitch = isw1 ? 3456u : 1280u;
            const unsigned rrow = 8 * w + (lane >> 3);
            glds16<op.a * 4096>(rrow * pitch + 16 * ((lane & 7) ^ (((rrow >> 1) & 3) << 1)), (isw1 ? w1 : w2) + (ksr * 32 * pitch + cl * 128), lb + L_A + w * 1024);
        } else if constexpr (op.kind == OP_U) {
            const unsigned wv = rr * 4 + w;
            if constexpr (op.a == 0 || op.a == 2) {
                const unsigned l = lane < 20 ? lane : 0, rw = (l * 52u) >> 8;
                glds4((4 * wv + rw) * 1280 + (l - 5 * rw + (op.a == 2 ? 5 : 0)) * 128, x + (size_t)(x8 >> 1) * 512 * 1280, lb + L_JUNK);
            } else if constexpr (op.a == 1 || op.a == 3) {
                const unsigned li = wv * 25 + (lane < 25 ? lane : 0), rw = (li * 6554u) >> 16, rem = li - rw * 10;
                glds4((rw + (op.a == 3 ? 320 : 0)) * 3840 + 1280 + (rem >= 5 ? 640 : 0) + (x8 & 1) * 640 + rem * 128, wqkv, lb + L_JUNK);
            } else if constexpr (op.a == 4) {
                const unsigned li = wv * 25 + (lane < 25 ? lane : 0), rw = (li * 13108u) >> 16, rem = li - rw * 5;
                glds4(rw * 3840 + (x8 & 1) * 640 + rem * 128, wqkv, lb + L_JUNK);
            } else if constexpr (op.a == 6) {
                const int T0_ = j2 < 12 ? 7 * j2 : 84 + 6 * (j2 - 12), Tsg_ = j2 < 12 ? ((j2 & 1) ? T0_ : T0_ + 6) : T0_;
                const int tq = lane & 15, ar = lane >> 4, m8 = lane >> 3, l8 = lane & 7;
                unsigned o_;
                if (w == 0) o_ = (unsigned)WS_FLAG + ar * 32768 + (ar == 0 ? tq * 16 + j1 : ar == 1 ? i1 * 16 + tq : i2 * 16 + tq) * 128;
                else if (w == 1) o_ = (unsigned)WS_O + ((8 * i1 + m8) * 20 + 16 + (j1 >> 2)) * 1024 + l8 * 128;
                else if (w == 2) o_ = lane < 32 ? (unsigned)WS_H + ((8 * i2 + 4 * half + m8) * 20 + ((5 * c8) >> 1) + ((c8 & 1) ? 0 : 2)) * 1024 + l8 * 128
                                                : (unsigned)WS_STAT2 + ((8 * i2 + 4 * half) * 16 * 64) + (lane - 32) * 128;
                else o_ = (unsigned)WS_H1 + ((8 * i2 + m8) * 54 + (Tsg_ >> 1)) * 1024 + l8 * 128;
                glds4(o_, ws, lb + L_JUNK);
            } else {
                const unsigned li = wv * 25 + (lane < 25 ? lane : 0), rw = (li * 13108u) >> 16, rem = li - rw * 5;
                glds4(rw * 1280 + (x8 & 1) * 640 + rem * 128, wo, lb + L_JUNK);
            }
        } else if constexpr (op.kind == OP_Q) issueQ(IC<op.a>{}, IC<op.b>{});
        else if constexpr (op.kind == OP_T) {
            Rf[op.a] = cat(trread(raR + op.a * 4096), trread(raR + op.a * 4096 + 2048));
        } else if constexpr (op.kind == OP_S) {
            unsigned isw1, ksr, cl;
            runit(IC<op.a>{}, isw1, ksr, cl);
            const unsigned T = 4 * cl + w, c8r = (T * 52u) >> 8;
            const unsigned dst = isw1 ? (unsigned)WS_W1R + (T * 20 + ksr) * 1024 : (unsigned)WS_W2R + ((c8r * 54 + ksr) * 5 + (T - 5 * c8r)) * 1024;
            st16_sc1((unsigned)(lane * 16), ws + dst, __builtin_bit_cast(u32x4, Rf[op.a]));
        } else if constexpr (op.a == 0) {
            u64 pc;
            asm volatile("s_getpc_b64 %0" : "=s"(pc));
            pc &= ~(u64)127;
            const unsigned cl_ = (rr * 4 + w) * 3 + ((lane & 3) < 3 ? (lane & 3) : 0);
            glds4((cl_ < CODE_LINES ? cl_ : CODE_LINES - 1) * 128, (const void*)pc, lb + L_JUNK);
        } else asm volatile("s_mov_b32 m0, %2\n\ts_nop 0\n\tglobal_load_lds_dword %0, %1 sc1" ::"v"((lane & 1) * 4), "s"(ws), "s"(lb + L_EPOCH) : "memory");
    };
    sfor<0, P1N>([&](auto N) { if constexpr (P1SEQ[N.v].ks < 0) p1issue(N); });
    unsigned rda[5];
    auto make_rda = [&] {
        int l2 = lane; asm volatile("" : "+v"(l2));
        const int g2 = l2 >> 4, n2 = l2 & 15;
        const unsigned red = lb + L_A + 20480 + w * 3360, trash = lb + L_TRASH + l2 * 16;
        rda[0] = red + (n2 * 20 + 4 * g2) * 4;
        rda[1] = (n2 == 0 || (n2 >= 8 && n2 < 12)) ? red + ((n2 == 0 ? 20 : 8 + n2) * 20 + 4 * g2) * 4 : trash;
        rda[2] = (n2 == 0 || n2 >= 12) ? red + (420 + (n2 == 0 ? 20 : 4 + n2) * 20 + 4 * g2) * 4 : trash;
        rda[3] = g2 < 2 ? red + (420 * g2 + n2 * 20 + 16) * 4 : trash;
        rda[4] = (g2 < 2 && (n2 == 0 || (g2 == 0 ? (n2 >= 8 && n2 < 12) : n2 >= 12))) ? red + (420 * g2 + (n2 == 0 ? 20 : (g2 == 0 ? 8 + n2 : 4 + n2)) * 20 + 16) * 4 : trash;
        asm volatile("" : "+v"(rda[0]), "+v"(rda[1]), "+v"(rda[2]), "+v"(rda[3]), "+v"(rda[4]));
        if (tid == 0) { *(LAS u64*)(lb + L_ZERO) = 0; *(LAS u64*)(lb + L_ZERO + 8) = 0; }
    };
    const int ro1 = 8 * g + q4 + 4 * (g & 1), ro2 = 8 * g + q4 + 4 * (1 - (g & 1));
    {
        unsigned vW1[2], vW2[2], vM1[2], vM2[2], vC1[2], vC2[2];
        const int mo = (p4 == 0 ? 32 : p4 == 1 ? 72 : p4 == 2 ? 112 : 152);
#pragma unroll
        for (int h = 0; h < 2; ++h) {
            vW1[h] = lb + L_A + h * 51200 + ro1 * 160 + 8 * p4; vW2[h] = lb + L_A + h * 51200 + ro2 * 160 + 8 * p4;
            vM1[h] = lb + L_A + h * 51200 + ro1 * 160 + mo; vM2[h] = lb + L_A + h * 51200 + ro2 * 160 + mo;
            vC1[h] = vW1[h] + 40 * w; vC2[h] = vW2[h] + 40 * w;
            asm volatile("" : "+v"(vW1[h]), "+v"(vW2[h]), "+v"(vM1[h]), "+v"(vM2[h]), "+v"(vC1[h]), "+v"(vC2[h]));
        }
        unsigned vD1 = vM1[0] + w * 5120, vD2 = vM2[0] + w * 5120;
        unsigned vX[2];
#pragma unroll
        for (int kl = 0; kl < 2; ++kl) vX[kl] = lb + L_B + w * 4096 + n * 128 + 16 * ((4 * kl + g) ^ (n >> 1));
#pragma unroll
        for (int t = 0; t < 5; ++t) acc[t][0] = acc[t][1] = z4;
        sq[0] = sq[1] = rs[0] = rs[1] = csA = csB = z4;
        s16x4 Wl[2][7], Wh[2][7];
        auto side = [&](auto KS, auto I) {
            constexpr int ks = KS.v, i = I.v, r = ks & 1, s = ks >> 1, kl = ks & 1, h = ks >= 10;
            constexpr unsigned ko = (ks - 10 * h) * 5120;
            if constexpr (i < 2) X[ks][i] = *(volatile LAS s16x8*)(vX[kl] + (s % 3) * 16384 + i * 2048);
            else if constexpr (i < 10) { constexpr int t = (i - 2) >> 1; if constexpr (i & 1) Wh[r][t] = trread(vW2[h] + ko + 40 * t); else Wl[r][t] = trread(vW1[h] + ko + 40 * t); }
            else if constexpr (i < 12) { if constexpr (i & 1) Wh[r][4] = trread(vM2[h] + ko); else Wl[r][4] = trread(vM1[h] + ko); }
            else if constexpr (i < 14) { if constexpr (i & 1) Wh[r][5] = trread(vC2[h] + ko); else Wl[r][5] = trread(vC1[h] + ko); }
            else {
                constexpr int u = ks >> 2;
                if constexpr (i & 1) { Wh[r][6] = trread(vD2 + (u % 3) * 20480); if constexpr (u == 2) { vD2 += 61440; asm volatile("" : "+v"(vD2)); } }
                else { Wl[r][6] = trread(vD1 + (u % 3) * 20480); if constexpr (u == 2) { vD1 += 61440; asm volatile("" : "+v"(vD1)); } }
            }
        };
        STAMP(48);
        WAITV(p1_wait(0));
        BARX();
        STAMP(49);
        sfor<0, 14>([&](auto I) { side(IC<0>{}, I); });
        SB();
        sfor<0, 20>([&](auto KS) {
            constexpr int ks = KS.v;
            if constexpr ((ks & 1) && ks < 19) {
                constexpr int s = (ks + 1) >> 1;
                WAITV(p1_wait(s));
                if constexpr (s >= 8) BAR();
                else if constexpr ((s & 1) == 0) BARX();
                SB();
            }
            if constexpr (ks == 4) STAMP(50);
            if constexpr (ks == 8) STAMP(51);
            if constexpr (ks == 12) STAMP(52);
            if constexpr (ks == 16) STAMP(53);
            constexpr int r = ks & 1;
            constexpr int nm = ((ks & 3) == 3) ? 16 : 15;
            constexpr int nside = ks < 19 ? (((ks + 1) & 3) == 3 ? 16 : 14) : 0;
            sfor<0, 16>([&](auto I) {
                constexpr int i = I.v;
                if constexpr (i < nm) {
                    if constexpr (i < 10) { constexpr int t = i >> 1, m = i & 1; acc[t][m] = mfma(X[ks][m], cat(Wl[r][t], Wh[r][t]), acc[t][m]); }
                    else if constexpr (i == 10) csA = mfma(ones, cat(Wl[r][5], Wh[r][5]), csA);
                    else if constexpr (i < 13) sq[i - 11] = mfma(X[ks][i - 11], X[ks][i - 11], sq[i - 11]);
                    else if constexpr (i < 15) rs[i - 13] = mfma(X[ks][i - 13], ones, rs[i - 13]);
                    else csB = mfma(ones, cat(Wl[r][6], Wh[r][6]), csB);
                }
                if constexpr (i < nside) side(IC<ks + 1>{}, I);
                sfor<0, P1N>([&](auto N) { if constexpr (P1SEQ[N.v].ks == ks && P1SEQ[N.v].slot == i) p1issue(N); });
                if constexpr (ks == 3 && i == 12) make_rda();
                if constexpr (ks == 5 && i == 12) make_voffQ();
                SB();
            });
        });
    }
    asm volatile("" : "+v"(csA), "+v"(csB));
    const u64 tag = *(volatile LAS u64*)(lb + L_EPOCH) + 1;
    STAMP(1);

    if (g == 0) { ((LAS float*)(lb + L_CSKV))[16 * w + n] = csA[0]; ((LAS float*)(lb + L_CS4))[16 * w + n] = csB[0]; }
    f32x4 lnA[2], lnB[2];
#pragma unroll
    for (int m = 0; m < 2; ++m) {
        f32x4 mean4, rstd4;
#pragma unroll
        for (int r = 0; r < 4; ++r) {
            const float s2 = __uint_as_float(__builtin_amdgcn_ds_bpermute(4 * (20 * g + r), __float_as_uint(sq[m][r])));
            const float mean = rs[m][r] * (1.f / 640.f);
            const float rstd = __builtin_amdgcn_rsqf(fmaxf(s2 * (1.f / 640.f) - mean * mean, 0.f) + 1e-5f);
            mean4[r] = mean; rstd4[r] = rstd;
            lnA[m][r] = rstd; lnB[m][r] = -mean * rstd;
        }
        *(LAS f32x4*)(lb + L_MEAN + (32 * w + 16 * m + 4 * g) * 4) = mean4;
        *(LAS f32x4*)(lb + L_RSTD + (32 * w + 16 * m + 4 * g) * 4) = rstd4;
    }
    BAR();
    STAMP(31);
    {
        float cs[5];
#pragma unroll
        for (int t = 0; t < 4; ++t) cs[t] = ((LAS float*)(lb + L_CSKV))[16 * t + n];
        {
            LAS float* c4 = (LAS float*)(lb + L_CS4);
            cs[4] = (c4[n] + c4[16 + n]) + (c4[32 + n] + c4[48 + n]);
        }
        s16x8 F[4], MA, MB;
        sfor<0, 5>([&](auto TT) {
            constexpr int t = TT.v;
            issueQ(IC<2>{}, TT);
            f32x4 v[2], vb_[2];
#pragma unroll
            for (int m = 0; m < 2; ++m)
#pragma unroll
                for (int r = 0; r < 4; ++r) {
                    const float y = lnA[m][r] * acc[t][m][r] + lnB[m][r] * cs[t];
                    if (t < 2) v[m][r] = phi(y);
                    else if (t < 4) v[m][r] = y;
                    else { v[m][r] = n < 8 ? phi(y) : y; vb_[m][r] = n < 8 ? 1.f : y; }
                }
            if constexpr (t < 4) F[t] = pack8(v[0], v[1]);
            else { MA = pack8(v[0], v[1]); MB = pack8(vb_[0], vb_[1]); }
            SB();
        });
        STAMP(33);
        f32x4 P1 = mfma(F[0], F[2], z4), P2 = mfma(F[0], MB, z4), P3 = mfma(MA, F[2], z4), P4 = mfma(MA, MB, z4);
        f32x4 P5 = mfma(F[1], F[3], z4), P6 = mfma(F[1], MB, z4), P7 = mfma(MA, F[3], z4);
        *(LAS f32x4*)(rda[0]) = P1;
        *(LAS f32x4*)(rda[0] + 1680) = P5;
        *(LAS f32x4*)(rda[1]) = P2;
        *(LAS f32x4*)(rda[2]) = P6;
        *(LAS f32x4*)(rda[3]) = (g == 0 ? P3 : P7);
        *(LAS f32x4*)(rda[4]) = P4;
    }
    STAMP(34);
    BAR();
    STAMP(35);
    if (tid < 210) {
        LAS char* red = (LAS char*)(lb + L_A + 20480 + tid * 16);
        f32x4 s = (*(LAS f32x4*)(red) + *(LAS f32x4*)(red + 3360)) + (*(LAS f32x4*)(red + 6720) + *(LAS f32x4*)(red + 10080));
        st16_sc1((unsigned)(tid * 16), ws + WS_KVP + (size_t)(j1 * 16 + i1) * 3360, __builtin_bit_cast(u32x4, s));
    }
    STAMP(36);
    drain_bar();
    if (tid < 16) flags_store(ws, 0, tid * 16 + j1, i1, tag);
    if (tid == 16) __hip_atomic_store((GAS u64*)(ws + WS_GFLAG) + b, tag, RLX_AGENT);
    STAMP(2);

    unsigned voff160[5];
    {
        int lane2 = lane; asm volatile("" : "+v"(lane2));
#pragma unroll
        for (int u = 0; u < 5; ++u) { int q = 64 * u + lane2, row = q / 10, s = q - row * 10; voff160[u] = row * 1280 + 160 * c8 + 16 * s; }
    }
    s16x8 kp[16];
    u64 gf[4];
    f32x4 aq[3][2];
    unsigned oa[12];
    const char* worun = wo + (size_t)w * (32 * 1280);
    {
        const unsigned vQ1 = lb + L_B + ro1 * 80 + 8 * p4, vQ2 = lb + L_B + ro2 * 80 + 8 * p4;
        const int to = (p4 == 0 ? 32 : p4 == 1 ? 72 : p4 == 2 ? 32 : 72);
        const unsigned vT1 = lb + L_B + ro1 * 80 + to, vT2 = lb + L_B + ro2 * 80 + to;
        const int to2 = (p4 & 1) ? 72 : 32;
        const unsigned vC1 = w < 2 ? vQ1 + 40 * w : vT1, vC2 = w < 2 ? vQ2 + 40 * w : vT2;
        (void)to2;
        f32x4 cq = z4;
#pragma unroll
        for (int t = 0; t < 3; ++t) aq[t][0] = aq[t][1] = z4;
        s16x4 Wl[2][4], Wh[2][4];
        auto side = [&](auto KS, auto I) {
            constexpr int ks = KS.v, i = I.v, r = ks & 1, t = i >> 1;
            constexpr unsigned ko = wq_pos(ks >> 1) + (ks & 1) * 2560;
            if constexpr (t < 2) { if constexpr (i & 1) Wh[r][t] = trread(vQ2 + ko + 40 * t); else Wl[r][t] = trread(vQ1 + ko + 40 * t); }
            else if constexpr (t == 2) { if constexpr (i & 1) Wh[r][2] = trread(vT2 + ko); else Wl[r][2] = trread(vT1 + ko); }
            else { if constexpr (i & 1) Wh[r][3] = trread(vC2 + ko); else Wl[r][3] = trread(vC1 + ko); }
        };
        sfor<0, 8>([&](auto I) { side(IC<0>{}, I); });
        SB();
        const char* kb = ws + WS_KVP + (size_t)j1 * 16 * 3360;
        const unsigned kvo = (tid < 210 ? tid : 209) * 16;
        {
            const unsigned kt = lb + L_KVT, zz = lb + L_ZERO;
            auto ka = [&](int head, int e, int c) { return kt + (head * 420 + e * 20 + 4 * c) * 2; };
            oa[0] = ka(0, n, g);  oa[1] = g == 0 ? ka(0, n, 4) : zz;
            oa[2] = ka(1, n, g);  oa[3] = g == 1 ? ka(1, n, 4) : zz;
            oa[4] = ka(0, 20, g); oa[5] = g == 0 ? ka(0, 20, 4) : zz;
            oa[6] = ka(1, 20, g); oa[7] = g == 1 ? ka(1, 20, 4) : zz;
            oa[8] = n < 4 ? ka(0, 16 + n, g) : zz;                oa[9] = (n < 4 && g == 0) ? ka(0, 16 + n, 4) : zz;
            oa[10] = (n >= 4 && n < 8) ? ka(1, 12 + n, g) : zz;   oa[11] = (n >= 4 && n < 8 && g == 1) ? ka(1, 12 + n, 4) : zz;
            asm volatile("" : "+v"(oa[0]), "+v"(oa[1]), "+v"(oa[2]), "+v"(oa[3]), "+v"(oa[4]), "+v"(oa[5]), "+v"(oa[6]), "+v"(oa[7]),
                         "+v"(oa[8]), "+v"(oa[9]), "+v"(oa[10]), "+v"(oa[11]));
        }
        sfor<0, 20>([&](auto KS) {
            constexpr int ks = KS.v, r = ks & 1;
            if constexpr (ks == 12) { STAMP(3); await<63>(ws, 0, i1 * 16 + j1, tag, w, lane); STAMP(4); SB(); }
            sfor<0, 8>([&](auto I) {
                constexpr int i = I.v;
                if constexpr (i < 6) { constexpr int t = i >> 1, m = i & 1; aq[t][m] = mfma(cat(Wl[r][t], Wh[r][t]), X[ks][m], aq[t][m]); }
                else if constexpr (i == 6) cq = mfma(ones, cat(Wl[r][3], Wh[r][3]), cq);
                if constexpr (ks < 19) side(IC<ks + 1>{}, I);
                if constexpr ((i == 2 || i == 6) && ks < 9) {
                    constexpr int pc_ = 2 * ks + (i == 6), tt = pc_ / 5, u = pc_ % 5;
                    glds16<tt * 20480 + u * 1024>(voff160[u], worun, ldsWw);
                    if constexpr (u == 4) { worun += 128 * 1280; OPAQUE_S(worun); }
                }
                if constexpr (ks == 18 && i < 4) ld8_sc1(gf[i], (unsigned)(lane * 8 + i * 512), ws + WS_GFLAG);
                if constexpr (ks >= 12 && ks < 18 && (i == 1 || i == 4 || i == 7) && 3 * (ks - 12) + i / 3 < 16) {
                    constexpr int pi = 3 * (ks - 12) + i / 3;
                    ld16_sc1(kp[pi], kvo + pi * 3360, kb);
                }
                SB();
            });
        });
        STAMP(37);
        { const unsigned hi = pk2(cq[0], cq[0]); const float lo = cq[0] - bflo(hi);
          if (g == 0 && w < 3) *(LAS u32x2*)(lb + L_CSQ + w * 128 + n * 8) = u32x2{hi, pk2(lo, lo)}; }
    }

    bool gok;
    {
        asm volatile("s_waitcnt vmcnt(0)" : "+v"(kp[0]), "+v"(kp[1]), "+v"(kp[2]), "+v"(kp[3]), "+v"(kp[4]), "+v"(kp[5]), "+v"(kp[6]), "+v"(kp[7]),
                     "+v"(kp[8]), "+v"(kp[9]), "+v"(kp[10]), "+v"(kp[11]), "+v"(kp[12]), "+v"(kp[13]), "+v"(kp[14]), "+v"(kp[15]),
                     "+v"(gf[0]), "+v"(gf[1]), "+v"(gf[2]), "+v"(gf[3])::"memory");
        gok = __all(gf[0] == tag && gf[1] == tag && gf[2] == tag && gf[3] == tag);
        f32x4 s0 = __builtin_bit_cast(f32x4, kp[0]), s1 = __builtin_bit_cast(f32x4, kp[1]);
#pragma unroll
        for (int i = 2; i < 16; i += 2) { s0 += __builtin_bit_cast(f32x4, kp[i]); s1 += __builtin_bit_cast(f32x4, kp[i + 1]); }
        if (tid < 210) *(LAS u32x2*)(lb + L_KVT + tid * 8) = pack4(s0 + s1);
    }
    STAMP(38);
    BAR();
    STAMP(39);
    if (w < 2) sfor<0, 5>([&](auto U) { glds16<U.v * 1024>(voff160[U.v], x + (size_t)(8 * i2 + 4 * half + 2 * w) * 16 * 1280, lb + L_XRES + w * 5120); });
    {
        auto rd2 = [&](unsigned a0, unsigned a1) { const u32x2 lo = *(LAS u32x2*)a0, hi = *(LAS u32x2*)a1; return __builtin_bit_cast(s16x8, u32x4{lo[0], lo[1], hi[0], hi[1]}); };
        s16x8 Fa[2], Fb[2];
        {
            const unsigned csa = g == 0 ? lb + L_CSQ + n * 8 : lb + L_ZERO;
            s16x8 cf[3];
#pragma unroll
            for (int t = 0; t < 3; ++t) { const u32x2 c = *(LAS u32x2*)(csa + (g == 0 ? t * 128 : 0)); cf[t] = __builtin_bit_cast(s16x8, u32x4{c[0], c[1], 0u, 0u}); }
#pragma unroll
            for (int m = 0; m < 2; ++m) {
                const float nm = -((LAS float*)(lb + L_MEAN))[32 * w + 16 * m + n], rstd = ((LAS float*)(lb + L_RSTD))[32 * w + 16 * m + n];
                const unsigned hi = pk2(nm, nm);
                const unsigned pr = g == 0 ? ((hi & 0xffffu) | (pk2(nm - bflo(hi), 0.f) << 16)) : 0u;
                const s16x8 hm = __builtin_bit_cast(s16x8, u32x4{pr, pr, 0u, 0u});
                f32x4 v[3];
#pragma unroll
                for (int t = 0; t < 3; ++t) {
                    const f32x4 y = mfma(cf[t], hm, aq[t][m]) * rstd;
#pragma unroll
                    for (int r = 0; r < 4; ++r) v[t][r] = phi(y[r]);
                }
                const s16x8 q2 = pack8(v[0], v[2]), q3 = pack8(v[1], v[2]);
                u32x4 u2 = __builtin_bit_cast(u32x4, q2), u3 = __builtin_bit_cast(u32x4, q3);
                if (g >= 2) { u2[2] = u2[3] = 0u; u3[2] = u3[3] = 0u; }
                Fa[m] = __builtin_bit_cast(s16x8, u2); Fb[m] = __builtin_bit_cast(s16x8, u3);
            }
        }
        const s16x8 Ama = rd2(oa[0], oa[1]), Amb = rd2(oa[2], oa[3]), Aza = rd2(oa[4], oa[5]), Azb = rd2(oa[6], oa[7]), Ata = rd2(oa[8], oa[9]), Atb = rd2(oa[10], oa[11]);
#pragma unroll
        for (int m = 0; m < 2; ++m) {
            f32x4 Da = mfma(Ama, Fa[m], z4), Db = mfma(Amb, Fb[m], z4);
            f32x4 Dt = mfma(Atb, Fb[m], mfma(Ata, Fa[m], z4));
            f32x4 Dza = mfma(Aza, Fa[m], z4), Dzb = mfma(Azb, Fb[m], z4);
            const float ra = __builtin_amdgcn_rcpf(Dza[0] + 1e-6f), rb = __builtin_amdgcn_rcpf(Dzb[0] + 1e-6f);
            const int mt = 8 * i1 + 2 * w + m;
            s16x8 of = pack8(Da * ra, Db * rb);
            st16_sc1((unsigned)((mt * 20 + j1) * 1024 + lane * 16), ws + WS_O, __builtin_bit_cast(u32x4, of));
            u32x2 tl = pack4(Dt * (g == 0 ? ra : rb));
            const int t = j1 >> 1;
            if (g < 2) st8_sc1((unsigned)((mt * 20 + 16 + (t >> 1)) * 1024 + (n + 16 * (2 * (j1 & 1) + g)) * 16 + (t & 1) * 8), ws + WS_O, tl);
        }
    }
    STAMP(40);
    drain_bar();
    if (tid < 16) flags_store(ws, 1, i1 * 16 + tid, j1, tag);
    STAMP(5);

    const int T0 = j2 < 12 ? 7 * j2 : 84 + 6 * (j2 - 12);
    const int odd = (j2 < 12) ? (j2 & 1) : 0;
    const int Tp0 = T0 + odd;
    const int Tsg = j2 < 12 ? (odd ? T0 : T0 + 6) : Tp0;
    const unsigned vlan16 = lane * 16;
    const int ksb = w < 2 ? 14 * w : 28 + 13 * (w - 2);
    const int nks = w < 2 ? 14 : 13;
    s16x8 V[14][5];
    const char* w2run = ws + WS_W2R + (size_t)(c8 * 54 + ksb) * 5120;
    const int step13 = nks == 14 ? 5120 : 0;
#define ISSUE_V(it, t) do { if constexpr ((it) < 8) ld16a_sc1(V[it][t], vlan16 + (t) * 1024, w2run); else ld16_sc1(V[it][t], vlan16 + (t) * 1024, w2run); \
            if ((t) == 4) { w2run += ((it) == 12 ? step13 : 5120); OPAQUE_S(w2run); } } while (0)
    if (w == 3 && gok) {
#pragma unroll
        for (int k = 0; k < 5; ++k) {
            int li = k * 64 + lane; li = li < 280 ? li : 279; li += rr * 280; li = li < ((x8 & 1) ? 8320 : 8960) ? li : 8319;
            glds4((unsigned)(li * 128), ws + WS_W1R + (x8 & 1) * 56 * 20480, lb + L_JUNK);
        }
    }
    sfor<18, 25>([&](auto P) {
        constexpr int tt = P.v / 5, u = P.v % 5;
        glds16<tt * 20480 + u * 1024>(voff160[u], worun, ldsWw);
        if constexpr (u == 4) { worun += 128 * 1280; OPAQUE_S(worun); }
    });
    const int mtW = 8 * i2 + 4 * half + w;
    STAMP(45);
    await<0>(ws, 1, i2 * 16 + jw, tag, w, lane);
    STAMP(6);
    {
        s16x8 O[20];
        unsigned orun = (unsigned)(mtW * 20 * 1024 + lane * 16);
#define ISSUE_O(i) do { ld16_sc1(O[i], orun, ws + WS_O); orun += 1024u; asm volatile("" : "+v"(orun)); } while (0)
        sfor<0, 8>([&](auto I) { ISSUE_O(I.v); });
        unsigned vA = lb + L_A + (4 * g + q4) * 160 + 8 * p4, vA2 = vA + 8 * 6400;
        const unsigned vB = lb + L_A + (20 * g + 16 + q4) * 160 + 8 * p4;
        unsigned vB2 = vB + 2 * 25600;
        asm volatile("" : "+v"(vA), "+v"(vA2), "+v"(vB2));
        f32x4 a[5];
#pragma unroll
        for (int t = 0; t < 5; ++t) a[t] = z4;
        s16x4 Wl[3][5], Wh[3][5];
        auto side = [&](auto I, auto J) {
            constexpr int i = I.v, j = J.v, r = i % 3, t = j >> 1;
            if constexpr (i < 16) {
                const unsigned a_ = (i < 8 ? vA : vA2) + (i & 7) * 6400 + 32 * t;
                if constexpr (j & 1) Wh[r][t] = trread(a_ + 3200); else Wl[r][t] = trread(a_);
            } else {
                const unsigned a_ = (i < 18 ? vB : vB2) + (i & 1) * 25600 + 32 * t;
                if constexpr (j & 1) Wh[r][t] = trread(a_ + 12800); else Wl[r][t] = trread(a_);
            }
        };
        sfor<0, 2>([&](auto I) { sfor<0, 10>([&](auto J) { side(I, J); }); });
        SB();
        sfor<0, 20>([&](auto I) {
            constexpr int i = I.v;
            constexpr int ny = ((i + 7 < 19 ? i + 7 : 19) - i) + (i > 12 ? (i < 17 ? 2 * (i - 12) : 10) : 0) + (i == 18 ? 1 : i == 19 ? 2 : 0);
            asm volatile("s_waitcnt vmcnt(%1)" : "+v"(O[i]) : "i"(ny) : "memory");
            SB();
            sfor<0, 5>([&](auto T) {
                constexpr int t = T.v;
                a[t] = mfma(cat(Wl[i % 3][t], Wh[i % 3][t]), O[i], a[t]);
                if constexpr (i + 2 < 20) { side(IC<i + 2>{}, IC<2 * t>{}); side(IC<i + 2>{}, IC<2 * t + 1>{}); }
                if constexpr (t == 1 && i + 8 < 20) ISSUE_O(i + 8);
                if constexpr ((i == 17 || i == 18) && t == 1) {
                    int li = w * 68 + (i == 17 ? lane : 64 + (lane < 4 ? lane : 0)); li = li < 270 ? li : 269;
                    glds4((unsigned)((rr * 270 + li) * 128), ws + WS_W2R + (size_t)(x8 & 1) * 4 * 54 * 5120, lb + L_JUNK);
                }
                if constexpr (i >= 12 && i < 17 && (t == 1 || t == 3)) {
                    constexpr int bg = 2 * (i - 12) + (t == 3);
                    const int Tg = bg < 5 ? Tp0 + 5 : Tsg;
                    glds16c<(5 + bg / 5) * 20480 + (bg % 5) * 4096>(vlan16, ws + WS_W1R + (size_t)(Tg * 20 + (bg % 5) * 4 + w) * 1024, lb + w * 1024);
                }
                SB();
            });
        });
        STAMP(28);
        float su = 0.f, s2 = 0.f;
        u32x2 hpk[5];
#pragma unroll
        for (int t = 0; t < 5; ++t) {
            const u32x2 xr = *(LAS u32x2*)(lb + L_XRES + (16 * w + n) * 160 + 32 * t + 8 * g);
            const f32x4 hv = f32x4{bflo(xr[0]), bfhi(xr[0]), bflo(xr[1]), bfhi(xr[1])} + a[t];
            hpk[t] = pack4(hv);
            *(LAS u32x2*)(lb + L_XRES + (16 * w + n) * 160 + 32 * t + 8 * g) = hpk[t];
#pragma unroll
            for (int r = 0; r < 4; ++r) { su += hv[r]; s2 += hv[r] * hv[r]; }
        }
        const unsigned hb = (unsigned)((mtW * 20 + ((5 * c8) >> 1)) * 1024 + lane * 16);
        const int od = c8 & 1;
        st8_sc1(hb + (od ? 8 : 2048), ws + WS_H, od ? hpk[0] : hpk[4]);
        { const u32x2 p0 = od ? hpk[1] : hpk[0], p1 = od ? hpk[2] : hpk[1]; st16_sc1(hb + (od ? 1024 : 0), ws + WS_H, u32x4{p0[0], p0[1], p1[0], p1[1]}); }
        { const u32x2 p0 = od ? hpk[3] : hpk[2], p1 = od ? hpk[4] : hpk[3]; st16_sc1(hb + (od ? 2048 : 1024), ws + WS_H, u32x4{p0[0], p0[1], p1[0], p1[1]}); }
        su = add_xor32(add_xor16(su)); s2 = add_xor32(add_xor16(s2));
        if (g == 0) { u32x2 st = {__float_as_uint(su), __float_as_uint(s2)}; st8_sc1((unsigned)((mtW * 16 + n) * 64 + c8 * 8), ws + WS_STAT2, st); }
    }
    STAMP(42);
    drain_bar();
    if (tid < 16) flags_store(ws, 2, i2 * 16 + tid, jw, tag);
    if (b == 0 && tid == 0) __hip_atomic_store((GAS u64*)ws, tag, RLX_AGENT);
    STAMP(7);

    {
        const char* s1 = ws + WS_W1R + (size_t)(Tp0 * 20 + w) * 1024;
        const unsigned l1 = lb + L_A + w * 1024;
        sfor<0, 25>([&](auto U) { glds16c<U.v * 4096>(vlan16, s1, l1); s1 += 4096; OPAQUE_S(s1); });
    }
    STAMP(43);
    await<0>(ws, 2, i2 * 16 + j2, tag, w, lane);
    STAMP(8);
    {
        s16x8 H[20][2];
        const int mt0 = 8 * i2 + 2 * w;
        s16x8 stv[2];
#pragma unroll
        for (int m = 0; m < 2; ++m) ld16_sc1(stv[m], (unsigned)(((mt0 + m) * 16 + n) * 64 + g * 16), ws + WS_STAT2);
        unsigned hrun = (unsigned)(mt0 * 20 * 1024 + lane * 16);
#define ISSUE_H(ks) do { ld16_sc1(H[ks][0], hrun, ws + WS_H); ld16_sc1(H[ks][1], hrun + 20 * 1024, ws + WS_H); hrun += 1024; asm volatile("" : "+v"(hrun)); } while (0)
        unsigned vF0 = lb + vlan16, vF1 = lb + 61440 + vlan16, vF2 = lb + 122880 + vlan16;
        asm volatile("" : "+v"(vF0), "+v"(vF1), "+v"(vF2));
        auto wread = [&](auto TT, auto KS) -> s16x8 {
            constexpr int t = TT.v, ks = KS.v;
            return *(volatile LAS s16x8*)((t < 3 ? vF0 : t < 6 ? vF1 : vF2) + (t % 3) * 20480 + ks * 1024);
        };
        {
            f32x4 c0 = z4, c1 = z4;
            const unsigned ta = lb + vlan16 + w * 20480, tb = lb + vlan16 + (w < 3 ? w + 4 : w) * 20480;
            s16x8 Ca[4], Cb[4];
            sfor<0, 3>([&](auto K) { Ca[K.v] = *(volatile LAS s16x8*)(ta + K.v * 1024); Cb[K.v] = *(volatile LAS s16x8*)(tb + K.v * 1024); });
            SB();
            sfor<0, 20>([&](auto K) {
                constexpr int k = K.v;
                c0 = mfma(ones, Ca[k & 3], c0);
                if constexpr (k + 3 < 20) Ca[(k + 3) & 3] = *(volatile LAS s16x8*)(ta + (k + 3) * 1024);
                SB();
                c1 = mfma(ones, Cb[k & 3], c1);
                if constexpr (k + 3 < 20) Cb[(k + 3) & 3] = *(volatile LAS s16x8*)(tb + (k + 3) * 1024);
                if constexpr ((k & 1) == 0 && k < 16) ISSUE_H(k >> 1);
                SB();
            });
            auto split = [&](float c) { const unsigned hi = pk2(c, c); const float lo = c - bflo(hi); u32x2 r = {hi, pk2(lo, lo)}; return r; };
            if (g == 0) {
                *(LAS u32x2*)(lb + L_CS1 + w * 128 + n * 8) = split(c0[0]);
                if (w < 3) *(LAS u32x2*)(lb + L_CS1 + (w + 4) * 128 + n * 8) = split(c1[0]);
            }
            if (tid < 2) *(LAS u64*)(lb + L_CS1 + 896 + tid * 8) = 0;
        }
        STAMP(19);
        asm volatile("s_waitcnt vmcnt(16)" : "+v"(stv[0]), "+v"(stv[1])::"memory");
        s16x8 Hm[2];
#pragma unroll
        for (int m = 0; m < 2; ++m) {
            f32x4 s = __builtin_bit_cast(f32x4, stv[m]);
            const float nm = add_xor32(add_xor16(s[0] + s[2])) * (-1.f / 640.f);
            const unsigned hi = pk2(nm, nm);
            const unsigned pr = g == 0 ? ((hi & 0xffffu) | (pk2(nm - bflo(hi), 0.f) << 16)) : 0u;
            Hm[m] = __builtin_bit_cast(s16x8, u32x4{pr, pr, 0u, 0u});
        }
        s16x8 Wf1[3][4];
        sfor<0, 2>([&](auto KS) { sfor<0, 4>([&](auto TT) { Wf1[KS.v][TT.v] = wread(TT, KS); }); });
        __builtin_amdgcn_s_waitcnt(0xC87F);
        BARX();
        const unsigned csa = lb + L_CS1 + (g == 0 ? n * 8 : 896);
        auto csfrag = [&](int t) { const u32x2 c = *(LAS u32x2*)(csa + (g == 0 ? t * 128 : 0)); return __builtin_bit_cast(s16x8, u32x4{c[0], c[1], 0u, 0u}); };
        auto relu2 = [&](unsigned p) { typedef short s2_ __attribute__((ext_vector_type(2))); const s2_ z = {0, 0};
                                       return __builtin_bit_cast(unsigned, __builtin_elementwise_max(__builtin_bit_cast(s2_, p), z)); };
        auto fin8 = [&](f32x4 x0, f32x4 x1) { return u32x4{relu2(pk2(x0[0], x0[1])), relu2(pk2(x0[2], x0[3])), relu2(pk2(x1[0], x1[1])), relu2(pk2(x1[2], x1[3]))}; };
        f32x4 a1[4][2];
        sfor<0, 4>([&](auto TT) { const s16x8 cf = csfrag(TT.v); a1[TT.v][0] = mfma(cf, Hm[0], z4); a1[TT.v][1] = mfma(cf, Hm[1], z4); });
        {
            auto& Wf = Wf1;
            SB();
            sfor<0, 20>([&](auto KS) {
                constexpr int ks = KS.v;
                constexpr int ny = 2 * ((ks + 7 < 19 ? ks + 7 : 19) - ks) + (ks > 12 ? (ks < 17 ? 2 * (ks - 12) : 10) : 0);
                asm volatile("s_waitcnt vmcnt(%2)" : "+v"(H[ks][0]), "+v"(H[ks][1]) : "i"(ny) : "memory");
                SB();
                sfor<0, 8>([&](auto I) {
                    constexpr int i = I.v, t = i >> 1, m = i & 1;
                    a1[t][m] = mfma(Wf[ks % 3][t], H[ks][m], a1[t][m]);
                    if constexpr (ks + 2 < 20 && (i & 1) == 0) Wf[(ks + 2) % 3][t] = wread(IC<t>{}, IC<ks + 2>{});
                    if constexpr (i == 3 && ks + 8 < 20) ISSUE_H(ks + 8);
                    if constexpr (ks >= 12 && ks < 17 && (i == 1 || i == 5)) { constexpr int pv = 2 * (ks - 12) + (i == 5); ISSUE_V(pv / 5, pv % 5); }
                    SB();
                });
            });
        }
        STAMP(20);
        f32x4 a2[3][2];
        sfor<0, 3>([&](auto TT) { const s16x8 cf = csfrag(4 + TT.v); a2[TT.v][0] = mfma(cf, Hm[0], z4); a2[TT.v][1] = mfma(cf, Hm[1], z4); });
        {
            s16x8 Wf[3][3];
            sfor<0, 2>([&](auto KS) { sfor<0, 3>([&](auto TT) { Wf[KS.v][TT.v] = wread(IC<4 + TT.v>{}, KS); }); });
            u32x2 e0, e1;
            sfor<0, 20>([&](auto KS) {
                constexpr int ks = KS.v;
                sfor<0, 6>([&](auto I) {
                    constexpr int i = I.v, t = i >> 1, m = i & 1;
                    a2[t][m] = mfma(Wf[ks % 3][t], H[ks][m], a2[t][m]);
                    if constexpr (ks + 2 < 20 && (i & 1) == 0) Wf[(ks + 2) % 3][t] = wread(IC<4 + t>{}, IC<ks + 2>{});
                    if constexpr (i == 3 || (i == 5 && (ks & 1) == 0)) { constexpr int pv = 10 + (ks >> 1) * 3 + ((ks & 1) ? 2 : (i == 5 ? 1 : 0)); ISSUE_V(pv / 5, pv % 5); }
                    if constexpr (ks >= 1 && ks < 17 && ((ks - 1) & 3) < 3) {
                        constexpr int u = (ks - 1) >> 2, ph = (ks - 1) & 3, P = u >> 1, mm = u & 1;
                        if constexpr (ph == 0 && i == 1) e0 = u32x2{relu2(pk2(a1[2 * P][mm][0], a1[2 * P][mm][1])), relu2(pk2(a1[2 * P][mm][2], a1[2 * P][mm][3]))};
                        if constexpr (ph == 1 && i == 1) e1 = u32x2{relu2(pk2(a1[2 * P + 1][mm][0], a1[2 * P + 1][mm][1])), relu2(pk2(a1[2 * P + 1][mm][2], a1[2 * P + 1][mm][3]))};
                        if constexpr (ph == 2 && i == 1)
                            st16_sc1((unsigned)(((mt0 + mm) * 54 + (Tp0 >> 1) + P) * 1024 + lane * 16), ws + WS_H1, u32x4{e0[0], e0[1], e1[0], e1[1]});
                    }
                    SB();
                });
            });
        }
        STAMP(22);
#pragma unroll
        for (int m = 0; m < 2; ++m) {
            st16_sc1((unsigned)(((mt0 + m) * 54 + (Tp0 >> 1) + 2) * 1024 + lane * 16), ws + WS_H1, fin8(a2[0][m], a2[1][m]));
            if (j2 < 12) st8_sc1((unsigned)(((mt0 + m) * 54 + (Tsg >> 1)) * 1024 + lane * 16 + (Tsg & 1) * 8), ws + WS_H1,
                                 u32x2{relu2(pk2(a2[2][m][0], a2[2][m][1])), relu2(pk2(a2[2][m][2], a2[2][m][3]))});
        }
        STAMP(24);
        drain_bar();
        if (tid < 16) flags_store(ws, 3, i2 * 16 + tid, j2, tag);
        STAMP(9);

        sfor<40, 60>([&](auto U) { ISSUE_V(U.v / 5, U.v % 5); });
        STAMP(44);
        await<63>(ws, 3, i2 * 16 + jw, tag, w, lane);
        STAMP(10);
        unsigned arun = (unsigned)(((8 * i2 + 4 * half) * 54 + ksb) * 1024 + lane * 16);
        const unsigned astep13 = nks == 14 ? 1024u : 0u;
        s16x8 A[4][4];
        unsigned aoff[4];
#pragma unroll
        for (int m = 0; m < 4; ++m) aoff[m] = ((w + m) & 3) * 54 * 1024;
        s16x8 st2v;
        ld16_sc1(st2v, (unsigned)((mtW * 16 + n) * 64 + g * 16), ws + WS_STAT2);
#define ISSUE_A(it, m) do { ld16_sc1(A[(it) & 3][m], arun + aoff[m], ws + WS_H1); \
            if ((m) == 3) { arun += ((it) == 12 ? astep13 : 1024u); asm volatile("" : "+v"(arun)); } } while (0)
        sfor<0, 3>([&](auto IT) { sfor<0, 4>([&](auto M) { ISSUE_A(IT.v, M.v); }); });
        f32x4 a[4][5];
#pragma unroll
        for (int m = 0; m < 4; ++m)
#pragma unroll
            for (int t = 0; t < 5; ++t) a[m][t] = z4;
        sfor<0, 14>([&](auto IT) {
            constexpr int it = IT.v;
            constexpr int ny = [] {
                int seq[200] = {}, n_ = 0;
                for (int a_ = 0; a_ < 3; ++a_) for (int m = 0; m < 4; ++m) seq[n_++] = 1000 + a_;
                for (int j = 0; j < it; ++j)
                    for (int i = 0; i < 20; ++i) {
                        if ((i & 3) == 1 && i < 16 && j + 3 < 14) seq[n_++] = 1000 + j + 3;
                        if ((i == 3 || i == 11) && j < 5) seq[n_++] = 2000 + (60 + 2 * j + (i == 11)) / 5;
                    }
                int need = 0;
                for (int q = 0; q < n_; ++q) if (seq[q] == 1000 + it || seq[q] == 2000 + it) need = q + 1;
                return n_ - need;
            }();
            if constexpr (it == 1) STAMP(25);
            if constexpr (it == 5) STAMP(26);
            if constexpr (it == 9) STAMP(27);
            if constexpr (it < 8)
                asm volatile("s_waitcnt vmcnt(%9)" : "+v"(A[it & 3][0]), "+v"(A[it & 3][1]), "+v"(A[it & 3][2]), "+v"(A[it & 3][3]),
                             "+a"(V[it][0]), "+a"(V[it][1]), "+a"(V[it][2]), "+a"(V[it][3]), "+a"(V[it][4]) : "i"(ny) : "memory");
            else
                asm volatile("s_waitcnt vmcnt(%9)" : "+v"(A[it & 3][0]), "+v"(A[it & 3][1]), "+v"(A[it & 3][2]), "+v"(A[it & 3][3]),
                             "+v"(V[it][0]), "+v"(V[it][1]), "+v"(V[it][2]), "+v"(V[it][3]), "+v"(V[it][4]) : "i"(ny) : "memory");
            SB();
            if (it < 13 || nks == 14) {
                sfor<0, 20>([&](auto I) {
                    constexpr int i = I.v, t = i >> 2, m = i & 3;
                    a[m][t] = mfma(V[it][t], A[it & 3][m], a[m][t]);
                    if constexpr ((i & 3) == 1 && i < 16 && it + 3 < 14) ISSUE_A(it + 3, i >> 2);
                    if constexpr ((i == 3 || i == 11) && it < 5) { constexpr int pv = 60 + 2 * it + (i == 11); ISSUE_V(pv / 5, pv % 5); }
                    SB();
                });
            }
        });
        STAMP(11);
        asm volatile("" : "+v"(st2v));
        const f32x4 sv = __builtin_bit_cast(f32x4, st2v);
        const float smean = add_xor32(add_xor16(sv[0] + sv[2])) * (1.f / 640.f);
        const float rstd2 = __builtin_amdgcn_rsqf(fmaxf(add_xor32(add_xor16(sv[1] + sv[3])) * (1.f / 640.f) - smean * smean, 0.f) + 1e-5f);
#pragma unroll
        for (int m = 1; m < 4; ++m)
#pragma unroll
            for (int t = 0; t < 5; ++t) *(LAS f32x4*)(lb + ((((w + m) & 3) * 3 + (m - 1)) * 5 + t) * 1024 + lane * 16) = a[m][t];
        BAR();
        STAMP(46);
        f32x4 fin[5];
#pragma unroll
        for (int t = 0; t < 5; ++t) {
            const LAS char* p = (const LAS char*)(lb + (w * 15 + t) * 1024 + lane * 16);
            fin[t] = ((a[0][t] + *(const LAS f32x4*)p) + (*(const LAS f32x4*)(p + 5120) + *(const LAS f32x4*)(p + 10240))) * rstd2;
        }
        STAMP(47);
        LAS char* ot = (LAS char*)(lb + L_B + w * 2560);
#pragma unroll
        for (int t = 0; t < 5; ++t) {
            const u32x2 hp = *(LAS u32x2*)(lb + L_XRES + (16 * w + n) * 160 + 32 * t + 8 * g);
            const f32x4 ov = f32x4{bflo(hp[0]), bfhi(hp[0]), bflo(hp[1]), bfhi(hp[1])} + fin[t];
            *(LAS u32x2*)(ot + n * 160 + 32 * t + 8 * g) = pack4(ov);
        }
        asm volatile("s_waitcnt lgkmcnt(0)" ::: "memory");
        int lane3 = lane; asm volatile("" : "+v"(lane3));
#pragma unroll
        for (int u = 0; u < 3; ++u) {
            const int q = 64 * u + lane3, row = q / 10, sl = q - row * 10;
            if (q < 160) *(GAS u32x4*)(out + (size_t)(mtW * 16 + row) * 1280 + 160 * c8 + 16 * sl) = *(LAS u32x4*)(ot + q * 16);
        }
    }
    STAMP(12); STAMPRT(63);
}

extern "C" void kernel_launch(void* const* d_in, const int* in_sizes, int n_in, void* d_out, int out_size, void* d_ws, size_t ws_size,
                              hipStream_t stream) {
    (void)in_sizes; (void)n_in; (void)out_size; (void)ws_size;
    fused_block<<<256, 256, 0, stream>>>((const char*)d_in[0], (const char*)d_in[1], (const char*)d_in[2], (const char*)d_in[4],
                                         (const char*)d_in[5], (char*)d_out, (char*)d_ws);
}
```

```cpp
#include <hip/hip_runtime.h>
#include <stdint.h>
#pragma clang diagnostic ignored "-Wint-to-pointer-cast"

typedef short s16x4 __attribute__((ext_vector_type(4)));
typedef short s16x8 __attribute__((ext_vector_type(8)));
typedef float f32x4 __attribute__((ext_vector_type(4)));
typedef unsigned u32x4 __attribute__((ext_vector_type(4)));
typedef unsigned u32x2 __attribute__((ext_vector_type(2)));
typedef unsigned long long u64;
#define LAS __attribute__((address_space(3)))
#define GAS __attribute__((address_space(1)))
#define DI __device__ __forceinline__
#define RLX_AGENT __ATOMIC_RELAXED, __HIP_MEMORY_SCOPE_AGENT
#define SB() __builtin_amdgcn_sched_barrier(0)

constexpr size_t WS_FLAG = 4096;
constexpr size_t WS_GFLAG = 2048;
constexpr size_t WS_KVP = 262144;
constexpr size_t WS_STAT2 = 0x120000;
constexpr size_t WS_O = 2u << 20;
constexpr size_t WS_H = 5u << 20;
constexpr size_t WS_H1 = 8u << 20;
constexpr size_t WS_W1R = 16u << 20;
constexpr size_t WS_W2R = 19u << 20;

constexpr int L_A = 0;
constexpr int L_B = 102400;
constexpr int L_XRES = L_B + 40960;
constexpr int L_M = 155648;
constexpr int L_MEAN = L_M;
constexpr int L_RSTD = L_M + 512;
constexpr int L_CSKV = L_M + 1024;
constexpr int L_CS4 = L_M + 1280;
constexpr int L_KVT = L_M + 1536;
constexpr int L_CS1 = L_M + 3328;
constexpr int L_JUNK = L_M + 5632;
constexpr int L_ZERO = L_M + 5888;
constexpr int L_CSQ = L_M + 5904 + 16;
constexpr int L_EPOCH = L_M + 7424;
constexpr int L_TRASH = L_M + 6400;
constexpr int LDS_BYTES = 163840;
constexpr int CODE_LINES = 368;

enum { OP_X, OP_W, OP_R, OP_O, OP_Q, OP_T, OP_S, OP_U };
struct P1Op { int kind, a, b, ks, slot; };
constexpr P1Op P1SEQ[] = {
    {OP_O, 0, 0, -1, 0},
    {OP_U, 0, 0, -1, 0}, {OP_U, 1, 0, -1, 0},
    {OP_X, 0, 0, -1, 0}, {OP_X, 0, 1, -1, 0}, {OP_X, 0, 2, -1, 0}, {OP_X, 0, 3, -1, 0},
    {OP_W, 0, 0, -1, 0}, {OP_W, 0, 1, -1, 0}, {OP_W, 0, 2, -1, 0}, {OP_W, 0, 3, -1, 0}, {OP_W, 0, 4, -1, 0},
    {OP_U, 2, 0, -1, 0}, {OP_U, 3, 0, -1, 0},
    {OP_X, 1, 0, -1, 0}, {OP_X, 1, 1, -1, 0}, {OP_X, 1, 2, -1, 0}, {OP_X, 1, 3, -1, 0},
    {OP_O, 1, 0, -1, 0},
    {OP_W, 1, 0, -1, 0}, {OP_W, 1, 1, -1, 0}, {OP_W, 1, 2, -1, 0}, {OP_W, 1, 3, -1, 0}, {OP_W, 1, 4, -1, 0},
    {OP_X, 2, 0, -1, 0}, {OP_X, 2, 1, -1, 0}, {OP_X, 2, 2, -1, 0}, {OP_X, 2, 3, -1, 0},
    {OP_W, 2, 0, 0, 3}, {OP_W, 2, 1, 0, 8}, {OP_W, 2, 2, 0, 13},
    {OP_X, 3, 0, 1, 2}, {OP_X, 3, 1, 1, 6}, {OP_X, 3, 2, 1, 10}, {OP_X, 3, 3, 1, 14},
    {OP_W, 2, 3, 2, 3}, {OP_W, 2, 4, 2, 8}, {OP_W, 3, 0, 2, 13},
    {OP_X, 4, 0, 3, 2}, {OP_X, 4, 1, 3, 6}, {OP_X, 4, 2, 3, 10}, {OP_X, 4, 3, 3, 14},
    {OP_W, 3, 1, 4, 3}, {OP_U, 4, 0, 4, 5}, {OP_W, 3, 2, 4, 8}, {OP_W, 3, 3, 4, 13},
    {OP_X, 5, 0, 5, 2}, {OP_X, 5, 1, 5, 6}, {OP_X, 5, 2, 5, 10}, {OP_X, 5, 3, 5, 14},
    {OP_W, 3, 4, 6, 3}, {OP_W, 4, 0, 6, 8}, {OP_W, 4, 1, 6, 13},
    {OP_X, 6, 0, 7, 2}, {OP_X, 6, 1, 7, 6}, {OP_X, 6, 2, 7, 10}, {OP_X, 6, 3, 7, 14},
    {OP_W, 4, 2, 8, 3}, {OP_W, 4, 3, 8, 8}, {OP_W, 4, 4, 8, 13},
    {OP_X, 7, 0, 9, 2}, {OP_X, 7, 1, 9, 6}, {OP_X, 7, 2, 9, 10}, {OP_X, 7, 3, 9, 14},
    {OP_R, 0, 0, 10, 3}, {OP_R, 1, 0, 10, 8}, {OP_R, 2, 0, 10, 13},
    {OP_X, 8, 0, 11, 2}, {OP_X, 8, 1, 11, 6}, {OP_X, 8, 2, 11, 10}, {OP_X, 8, 3, 11, 14},
    {OP_R, 3, 0, 12, 3}, {OP_R, 4, 0, 12, 8}, {OP_U, 5, 0, 12, 13},
    {OP_X, 9, 0, 13, 2}, {OP_X, 9, 1, 13, 6}, {OP_X, 9, 2, 13, 10}, {OP_X, 9, 3, 13, 14},
    {OP_Q, 0, 0, 15, 3}, {OP_Q, 0, 1, 15, 8}, {OP_Q, 0, 2, 15, 13}, {OP_Q, 0, 3, 16, 3}, {OP_Q, 0, 4, 16, 8},
    {OP_T, 0, 0, 17, 1}, {OP_T, 1, 0, 17, 2}, {OP_Q, 1, 0, 17, 3}, {OP_T, 2, 0, 17, 5}, {OP_T, 3, 0, 17, 6}, {OP_T, 4, 0, 17, 7},
    {OP_Q, 1, 1, 17, 8}, {OP_Q, 1, 2, 17, 13},
    {OP_S, 0, 0, 18, 1}, {OP_Q, 1, 3, 18, 3}, {OP_S, 1, 0, 18, 6}, {OP_Q, 1, 4, 18, 8}, {OP_S, 2, 0, 18, 11}, {OP_S, 3, 0, 19, 2}, {OP_S, 4, 0, 19, 7},
};
constexpr int P1N = sizeof(P1SEQ) / sizeof(P1SEQ[0]);
constexpr int p1_wait(int s) {
    int issued = 0, need = 0;
    for (int i = 0; i < P1N; ++i) {
        if (P1SEQ[i].ks < 2 * s - 1 || P1SEQ[i].ks < 0) issued = i + 1;
        if ((P1SEQ[i].kind == OP_X && P1SEQ[i].a == s) || (P1SEQ[i].kind == OP_W && P1SEQ[i].a <= (s >> 1))) need = need > i + 1 ? need : i + 1;
    }
    return issued - need;
}
constexpr bool p1_ok() { for (int s = 0; s < 10; ++s) if (p1_wait(s) < 0 || p1_wait(s) > 63) return false; return true; }
static_assert(p1_ok(), "P1a schedule: an op is waited for before it is issued");

template <int I> struct IC { static constexpr int v = I; };
template <int B, int E, class F> DI void sfor(F&& f) { if constexpr (B < E) { f(IC<B>{}); sfor<B + 1, E>(f); } }
#define OPAQUE_S(p) asm volatile("" : "+s"(p))

DI f32x4 mfma(s16x8 a, s16x8 b, f32x4 c) { return __builtin_amdgcn_mfma_f32_16x16x32_bf16(a, b, c, 0, 0, 0); }
DI s16x4 trread(unsigned addr) { return __builtin_bit_cast(s16x4, __builtin_amdgcn_ds_read_tr16_b64_v4i16((LAS s16x4*)addr)); }
DI s16x8 cat(s16x4 a, s16x4 b) { return __builtin_shufflevector(a, b, 0, 1, 2, 3, 4, 5, 6, 7); }
DI unsigned pk2(float lo, float hi) {
    typedef __bf16 bf2 __attribute__((ext_vector_type(2)));
    typedef float f2 __attribute__((ext_vector_type(2)));
    f2 v = {lo, hi};
    return __builtin_bit_cast(unsigned, __builtin_convertvector(v, bf2));
}
DI s16x8 pack8(f32x4 a, f32x4 b) {
    u32x4 r = {pk2(a[0], a[1]), pk2(a[2], a[3]), pk2(b[0], b[1]), pk2(b[2], b[3])};
    return __builtin_bit_cast(s16x8, r);
}
DI u32x2 pack4(f32x4 a) { u32x2 r = {pk2(a[0], a[1]), pk2(a[2], a[3])}; return r; }
DI float bflo(unsigned u) { return __uint_as_float(u << 16); }
DI float bfhi(unsigned u) { return __uint_as_float(u & 0xffff0000u); }
DI float phi(float t) { return (fmaxf(t, 0.f) + 1e-6f) + __builtin_amdgcn_fmed3f(__builtin_amdgcn_exp2f(t * 1.44269504088896f), 0.f, 1.f); }
DI float add_xor16(float v) { const unsigned u = __float_as_uint(v); auto r = __builtin_amdgcn_permlane16_swap(u, u, false, false); return __uint_as_float(r[0]) + __uint_as_float(r[1]); }
DI float add_xor32(float v) { const unsigned u = __float_as_uint(v); auto r = __builtin_amdgcn_permlane32_swap(u, u, false, false); return __uint_as_float(r[0]) + __uint_as_float(r[1]); }
constexpr int wq_pos(int grp) { return grp < 3 ? 16384 + 5120 * grp : grp < 7 ? 32768 + 5120 * (grp - 3) : 5120 * (grp - 7); }

template <int LOFF> DI void glds16(unsigned voff, const void* sbase, unsigned lds) {
    asm volatile("s_add_u32 m0, %2, %3\n\ts_nop 0\n\tglobal_load_lds_dwordx4 %0, %1" ::"v"(voff), "s"(sbase), "s"(lds), "i"(LOFF) : "memory", "scc");
}
template <int LOFF, int GOFF> DI void glds16i(unsigned voff, const void* sbase, unsigned lds) {
    asm volatile("s_add_u32 m0, %2, %3\n\ts_nop 0\n\tglobal_load_lds_dwordx4 %0, %1 offset:%4" ::"v"(voff), "s"(sbase), "s"(lds), "i"(LOFF - GOFF), "i"(GOFF) : "memory", "scc");
}
template <int LOFF> DI void glds16c(unsigned voff, const void* sbase, unsigned lds) {
    asm volatile("s_add_u32 m0, %2, %3\n\ts_nop 0\n\tglobal_load_lds_dwordx4 %0, %1 sc1" ::"v"(voff), "s"(sbase), "s"(lds), "i"(LOFF) : "memory", "scc");
}
DI void glds4(unsigned voff, const void* sbase, unsigned lds) {
    asm volatile("s_mov_b32 m0, %2\n\ts_nop 0\n\tglobal_load_lds_dword %0, %1" ::"v"(voff), "s"(sbase), "s"(lds) : "memory");
}
#define WAITV(n) asm volatile("s_waitcnt vmcnt(%0)" ::"i"(n) : "memory")
#define BARX() do { __builtin_amdgcn_s_barrier(); asm volatile("" ::: "memory"); } while (0)
#define BAR() do { asm volatile("s_waitcnt lgkmcnt(0)" ::: "memory"); __builtin_amdgcn_s_barrier(); asm volatile("" ::: "memory"); } while (0)

DI void ld16_sc1(s16x8& d, unsigned voff, const void* sbase) {
    asm volatile("global_load_dwordx4 %0, %1, %2 sc1" : "=v"(d) : "v"(voff), "s"(sbase) : "memory");
}
DI void ld16a_sc1(s16x8& d, unsigned voff, const void* sbase) {
    asm volatile("global_load_dwordx4 %0, %1, %2 sc1" : "=a"(d) : "v"(voff), "s"(sbase) : "memory");
}
DI void ld8_sc1(u64& d, unsigned voff, const void* sbase) { asm volatile("global_load_dwordx2 %0, %1, %2 sc1" : "=v"(d) : "v"(voff), "s"(sbase) : "memory"); }
DI void st16_sc1(unsigned voff, void* sbase, u32x4 v) {
    asm volatile("global_store_dwordx4 %0, %1, %2 sc1\n\ts_nop 1" ::"v"(voff), "v"(v), "s"(sbase) : "memory");
}
DI void st8_sc1(unsigned voff, void* sbase, u32x2 v) {
    asm volatile("global_store_dwordx2 %0, %1, %2 sc1" ::"v"(voff), "v"(v), "s"(sbase) : "memory");
}
DI void drain_bar() { WAITV(0); BAR(); }
DI void flag_put(char* ws, int arr, int line, int slot, u64 tag) {
    __hip_atomic_store((GAS u64*)(ws + WS_FLAG + (size_t)arr * 32768 + (size_t)line * 128) + slot, tag, RLX_AGENT);
}
template <int NV> DI void await(char* ws, int arr, int line, u64 tag, int w, int lane) {
    if (w == 0) {
        GAS u64* fl = (GAS u64*)(ws + WS_FLAG + (size_t)arr * 32768 + (size_t)line * 128) + (lane & 15);
        for (unsigned spins = 0; spins < (1u << 20); ++spins) {
            u64 f = __hip_atomic_load(fl, RLX_AGENT);
            if (__all(f == tag)) break;
        }
    }
    WAITV(NV);
    BAR();
}

__global__ void __launch_bounds__(256, 1)
fused_block(const char* __restrict__ x, const char* __restrict__ wqkv, const char* __restrict__ wo, const char* __restrict__ w1,
            const char* __restrict__ w2, char* __restrict__ out, char* __restrict__ ws) {
    __shared__ __attribute__((aligned(16))) char smem[LDS_BYTES];
    const unsigned lb = (unsigned)(uintptr_t)(LAS char*)smem;
    const int tid = threadIdx.x, lane = tid & 63;
    const int w = __builtin_amdgcn_readfirstlane(tid >> 6);
    const int b = blockIdx.x, x8 = b & 7, rr = b >> 3;
    const int i1 = 4 * (x8 >> 1) + (rr >> 3), j1 = 8 * (x8 & 1) + (rr & 7);
    const int i2 = i1, j2 = j1;
    const int half = (rr >> 2) & 1, c8 = 4 * (x8 & 1) + (rr & 3), jw = 8 * half + c8;
    const int g = lane >> 4, n = lane & 15, q4 = n >> 2, p4 = lane & 3;
    const s16x8 ones = {0x3F80, 0x3F80, 0x3F80, 0x3F80, 0x3F80, 0x3F80, 0x3F80, 0x3F80};
    const f32x4 z4 = {0, 0, 0, 0};

    s16x8 X[20][2];
    f32x4 acc[5][2], sq[2], rs[2], csA, csB;
    unsigned voffW[5], voffX[4];
    const char* xb = x + (size_t)i1 * 128 * 1280;
    const unsigned ldsXw = lb + L_B + w * 4096, ldsWw = lb + L_A + w * 5120;
#pragma unroll
    for (int u = 0; u < 4; ++u)
        voffX[u] = (32 * w + 8 * u + (lane >> 3)) * 1280 + 16 * ((lane & 7) ^ ((lane >> 4) + 4 * (u & 1)));
#define ISSUE_X(s, u) glds16i<((s) % 3) * 16384 + (u) * 1024, (s) * 128>(voffX[u], xb, ldsXw)
#define ISSUE_XS(s) do { ISSUE_X(s, 0); ISSUE_X(s, 1); ISSUE_X(s, 2); ISSUE_X(s, 3); } while (0)
#pragma unroll
    for (int u = 0; u < 5; ++u) {
        int q = 64 * u + lane, row = q / 10, s = q - row * 10;
        row ^= ((row >> 3) & 1) << 2;
        voffW[u] = row * 3840 + (s < 5 ? (640 + 40 * j1) * 2 + 16 * s : (1280 + 40 * j1) * 2 + 16 * (s - 5));
    }
    const char* wkb = wqkv + (size_t)w * (32 * 3840);
    const char* wrun = wkb;
#define ISSUE_W(t, u) do { glds16<(t) * 20480 + (u) * 1024>(voffW[u], wrun, ldsWw); if ((u) == 4) { wrun += 128 * 3840; OPAQUE_S(wrun); } } while (0)
    unsigned voffQ[5];
    auto make_voffQ = [&] {
        int l2 = lane; asm volatile("" : "+v"(l2));
#pragma unroll
        for (int u = 0; u < 5; ++u) {
            int q = 64 * u + l2, row = q / 5, s = q - row * 5;
            row ^= ((row >> 3) & 1) << 2;
            voffQ[u] = row * 3840 + 80 * j1 + 16 * s;
        }
        asm volatile("" : "+v"(voffQ[0]), "+v"(voffQ[1]), "+v"(voffQ[2]), "+v"(voffQ[3]), "+v"(voffQ[4]));
    };
    auto issueQ = [&](auto A, auto U) {
        constexpr int a = A.v, u = U.v;
        const int grp = a == 0 ? (w < 2 ? w : 2) : a == 1 ? 3 + w : 7 + (w < 2 ? w : 2);
        const int pos = a == 0 ? 16384 + 5120 * grp : a == 1 ? 32768 + 5120 * w : 5120 * (grp - 7);
        glds16<u * 1024>(voffQ[u], wqkv + (size_t)grp * (64 * 3840), lb + L_B + pos);
    };
    const unsigned raR = lb + L_A + (4 * g + q4) * 128 + 16 * ((2 * w + (p4 >> 1)) ^ ((((4 * g + q4) >> 1) & 3) << 1)) + 8 * (p4 & 1);
    s16x8 Rf[5];
    auto runit = [&](auto R, unsigned& isw1, unsigned& ksr, unsigned& cl) {
        constexpr int r = R.v;
        const unsigned ub = (unsigned)b;
        unsigned v;
        if constexpr (r < 2) { isw1 = 1; v = ub + 256 * r; }
        else if constexpr (r == 2) { isw1 = ub < 28; v = isw1 ? ub + 512 : ub - 28; }
        else if constexpr (r == 3) { isw1 = 0; v = ub + 228; }
        else { isw1 = 0; v = ub < 56 ? ub + 484 : ub + 228; }
        ksr = isw1 ? (v * 2428u) >> 16 : (v * 6554u) >> 16;
        cl = v - ksr * (isw1 ? 27u : 10u);
    };
    auto p1issue = [&](auto N) {
        constexpr P1Op op = P1SEQ[N.v];
        if constexpr (op.kind == OP_X) ISSUE_X(op.a, op.b);
        else if constexpr (op.kind == OP_W) ISSUE_W(op.a, op.b);
        else if constexpr (op.kind == OP_R) {
            unsigned isw1, ksr, cl;
            runit(IC<op.a>{}, isw1, ksr, cl);
            const unsigned pitch = isw1 ? 3456u : 1280u;
            const unsigned rrow = 8 * w + (lane >> 3);
            glds16<op.a * 4096>(rrow * pitch + 16 * ((lane & 7) ^ (((rrow >> 1) & 3) << 1)), (isw1 ? w1 : w2) + (ksr * 32 * pitch + cl * 128), lb + L_A + w * 1024);
        } else if constexpr (op.kind == OP_U) {
            const unsigned wv = rr * 4 + w;
            if constexpr (op.a == 0 || op.a == 2) {
                const unsigned l = lane < 20 ? lane : 0, rw = (l * 52u) >> 8;
                glds4((4 * wv + rw) * 1280 + (l - 5 * rw + (op.a == 2 ? 5 : 0)) * 128, x + (size_t)(x8 >> 1) * 512 * 1280, lb + L_JUNK);
            } else if constexpr (op.a == 1 || op.a == 3) {
                const unsigned li = wv * 25 + (lane < 25 ? lane : 0), rw = (li * 6554u) >> 16, rem = li - rw * 10;
                glds4((rw + (op.a == 3 ? 320 : 0)) * 3840 + 1280 + (rem >= 5 ? 640 : 0) + (x8 & 1) * 640 + rem * 128, wqkv, lb + L_JUNK);
            } else if constexpr (op.a == 4) {
                const unsigned li = wv * 25 + (lane < 25 ? lane : 0), rw = (li * 13108u) >> 16, rem = li - rw * 5;
                glds4(rw * 3840 + (x8 & 1) * 640 + rem * 128, wqkv, lb + L_JUNK);
            } else {
                const unsigned li = wv * 25 + (lane < 25 ? lane : 0), rw = (li * 13108u) >> 16, rem = li - rw * 5;
                glds4(rw * 1280 + (x8 & 1) * 640 + rem * 128, wo, lb + L_JUNK);
            }
        } else if constexpr (op.kind == OP_Q) issueQ(IC<op.a>{}, IC<op.b>{});
        else if constexpr (op.kind == OP_T) {
            Rf[op.a] = cat(trread(raR + op.a * 4096), trread(raR + op.a * 4096 + 2048));
        } else if constexpr (op.kind == OP_S) {
            unsigned isw1, ksr, cl;
            runit(IC<op.a>{}, isw1, ksr, cl);
            const unsigned T = 4 * cl + w, c8r = (T * 52u) >> 8;
            const unsigned dst = isw1 ? (unsigned)WS_W1R + (T * 20 + ksr) * 1024 : (unsigned)WS_W2R + ((c8r * 54 + ksr) * 5 + (T - 5 * c8r)) * 1024;
            st16_sc1((unsigned)(lane * 16), ws + dst, __builtin_bit_cast(u32x4, Rf[op.a]));
        } else if constexpr (op.a == 0) {
            u64 pc;
            asm volatile("s_getpc_b64 %0" : "=s"(pc));
            pc &= ~(u64)127;
            const unsigned cl_ = (rr * 4 + w) * 3 + ((lane & 3) < 3 ? (lane & 3) : 0);
            glds4((cl_ < CODE_LINES ? cl_ : CODE_LINES - 1) * 128, (const void*)pc, lb + L_JUNK);
        } else asm volatile("s_mov_b32 m0, %2\n\ts_nop 0\n\tglobal_load_lds_dword %0, %1 sc1" ::"v"((lane & 1) * 4), "s"(ws), "s"(lb + L_EPOCH) : "memory");
    };
    sfor<0, P1N>([&](auto N) { if constexpr (P1SEQ[N.v].ks < 0) p1issue(N); });
    unsigned rda[5];
    auto make_rda = [&] {
        int l2 = lane; asm volatile("" : "+v"(l2));
        const int g2 = l2 >> 4, n2 = l2 & 15;
        const unsigned red = lb + L_A + 20480 + w * 3360, trash = lb + L_TRASH + l2 * 16;
        rda[0] = red + (n2 * 20 + 4 * g2) * 4;
        rda[1] = (n2 == 0 || (n2 >= 8 && n2 < 12)) ? red + ((n2 == 0 ? 20 : 8 + n2) * 20 + 4 * g2) * 4 : trash;
        rda[2] = (n2 == 0 || n2 >= 12) ? red + (420 + (n2 == 0 ? 20 : 4 + n2) * 20 + 4 * g2) * 4 : trash;
        rda[3] = g2 < 2 ? red + (420 * g2 + n2 * 20 + 16) * 4 : trash;
        rda[4] = (g2 < 2 && (n2 == 0 || (g2 == 0 ? (n2 >= 8 && n2 < 12) : n2 >= 12))) ? red + (420 * g2 + (n2 == 0 ? 20 : (g2 == 0 ? 8 + n2 : 4 + n2)) * 20 + 16) * 4 : trash;
        asm volatile("" : "+v"(rda[0]), "+v"(rda[1]), "+v"(rda[2]), "+v"(rda[3]), "+v"(rda[4]));
        if (tid == 0) { *(LAS u64*)(lb + L_ZERO) = 0; *(LAS u64*)(lb + L_ZERO + 8) = 0; }
    };
    const int ro1 = 8 * g + q4 + 4 * (g & 1), ro2 = 8 * g + q4 + 4 * (1 - (g & 1));
    {
        unsigned vW1[2], vW2[2], vM1[2], vM2[2], vC1[2], vC2[2];
        const int mo = (p4 == 0 ? 32 : p4 == 1 ? 72 : p4 == 2 ? 112 : 152);
#pragma unroll
        for (int h = 0; h < 2; ++h) {
            vW1[h] = lb + L_A + h * 51200 + ro1 * 160 + 8 * p4; vW2[h] = lb + L_A + h * 51200 + ro2 * 160 + 8 * p4;
            vM1[h] = lb + L_A + h * 51200 + ro1 * 160 + mo; vM2[h] = lb + L_A + h * 51200 + ro2 * 160 + mo;
            vC1[h] = vW1[h] + 40 * w; vC2[h] = vW2[h] + 40 * w;
            asm volatile("" : "+v"(vW1[h]), "+v"(vW2[h]), "+v"(vM1[h]), "+v"(vM2[h]), "+v"(vC1[h]), "+v"(vC2[h]));
        }
        unsigned vD1 = vM1[0] + w * 5120, vD2 = vM2[0] + w * 5120;
        unsigned vX[2];
#pragma unroll
        for (int kl = 0; kl < 2; ++kl) vX[kl] = lb + L_B + w * 4096 + n * 128 + 16 * ((4 * kl + g) ^ (n >> 1));
#pragma unroll
        for (int t = 0; t < 5; ++t) acc[t][0] = acc[t][1] = z4;
        sq[0] = sq[1] = rs[0] = rs[1] = csA = csB = z4;
        s16x4 Wl[2][7], Wh[2][7];
        auto side = [&](auto KS, auto I) {
            constexpr int ks = KS.v, i = I.v, r = ks & 1, s = ks >> 1, kl = ks & 1, h = ks >= 10;
            constexpr unsigned ko = (ks - 10 * h) * 5120;
            if constexpr (i < 2) X[ks][i] = *(volatile LAS s16x8*)(vX[kl] + (s % 3) * 16384 + i * 2048);
            else if constexpr (i < 10) { constexpr int t = (i - 2) >> 1; if constexpr (i & 1) Wh[r][t] = trread(vW2[h] + ko + 40 * t); else Wl[r][t] = trread(vW1[h] + ko + 40 * t); }
            else if constexpr (i < 12) { if constexpr (i & 1) Wh[r][4] = trread(vM2[h] + ko); else Wl[r][4] = trread(vM1[h] + ko); }
            else if constexpr (i < 14) { if constexpr (i & 1) Wh[r][5] = trread(vC2[h] + ko); else Wl[r][5] = trread(vC1[h] + ko); }
            else {
                constexpr int u = ks >> 2;
                if constexpr (i & 1) { Wh[r][6] = trread(vD2 + (u % 3) * 20480); if constexpr (u == 2) { vD2 += 61440; asm volatile("" : "+v"(vD2)); } }
                else { Wl[r][6] = trread(vD1 + (u % 3) * 20480); if constexpr (u == 2) { vD1 += 61440; asm volatile("" : "+v"(vD1)); } }
            }
        };
        WAITV(p1_wait(0));
        BARX();
        sfor<0, 14>([&](auto I) { side(IC<0>{}, I); });
        SB();
        sfor<0, 20>([&](auto KS) {
            constexpr int ks = KS.v;
            if constexpr ((ks & 1) && ks < 19) {
                constexpr int s = (ks + 1) >> 1;
                WAITV(p1_wait(s));
                if constexpr (s >= 8) BAR();
                else if constexpr ((s & 1) == 0) BARX();
                SB();
            }
            constexpr int r = ks & 1;
            constexpr int nm = ((ks & 3) == 3) ? 16 : 15;
            constexpr int nside = ks < 19 ? (((ks + 1) & 3) == 3 ? 16 : 14) : 0;
            sfor<0, 16>([&](auto I) {
                constexpr int i = I.v;
                if constexpr (i < nm) {
                    if constexpr (i < 10) { constexpr int t = i >> 1, m = i & 1; acc[t][m] = mfma(X[ks][m], cat(Wl[r][t], Wh[r][t]), acc[t][m]); }
                    else if constexpr (i == 10) csA = mfma(ones, cat(Wl[r][5], Wh[r][5]), csA);
                    else if constexpr (i < 13) sq[i - 11] = mfma(X[ks][i - 11], X[ks][i - 11], sq[i - 11]);
                    else if constexpr (i < 15) rs[i - 13] = mfma(X[ks][i - 13], ones, rs[i - 13]);
                    else csB = mfma(ones, cat(Wl[r][6], Wh[r][6]), csB);
                }
                if constexpr (i < nside) side(IC<ks + 1>{}, I);
                sfor<0, P1N>([&](auto N) { if constexpr (P1SEQ[N.v].ks == ks && P1SEQ[N.v].slot == i) p1issue(N); });
                if constexpr (ks == 3 && i == 12) make_rda();
                if constexpr (ks == 5 && i == 12) make_voffQ();
                SB();
            });
        });
    }
    asm volatile("" : "+v"(csA), "+v"(csB));
    const u64 tag = *(volatile LAS u64*)(lb + L_EPOCH) + 1;
    if (g == 0) { ((LAS float*)(lb + L_CSKV))[16 * w + n] = csA[0]; ((LAS float*)(lb + L_CS4))[16 * w + n] = csB[0]; }
    f32x4 lnA[2], lnB[2];
#pragma unroll
    for (int m = 0; m < 2; ++m) {
        f32x4 mean4, rstd4;
#pragma unroll
        for (int r = 0; r < 4; ++r) {
            const float s2 = __uint_as_float(__builtin_amdgcn_ds_bpermute(4 * (20 * g + r), __float_as_uint(sq[m][r])));
            const float mean = rs[m][r] * (1.f / 640.f);
            const float rstd = __builtin_amdgcn_rsqf(fmaxf(s2 * (1.f / 640.f) - mean * mean, 0.f) + 1e-5f);
            mean4[r] = mean; rstd4[r] = rstd;
            lnA[m][r] = rstd; lnB[m][r] = -mean * rstd;
        }
        *(LAS f32x4*)(lb + L_MEAN + (32 * w + 16 * m + 4 * g) * 4) = mean4;
        *(LAS f32x4*)(lb + L_RSTD + (32 * w + 16 * m + 4 * g) * 4) = rstd4;
    }
    BAR();
    {
        float cs[5];
#pragma unroll
        for (int t = 0; t < 4; ++t) cs[t] = ((LAS float*)(lb + L_CSKV))[16 * t + n];
        {
            LAS float* c4 = (LAS float*)(lb + L_CS4);
            cs[4] = (c4[n] + c4[16 + n]) + (c4[32 + n] + c4[48 + n]);
        }
        s16x8 F[4], MA, MB;
        sfor<0, 5>([&](auto TT) {
            constexpr int t = TT.v;
            issueQ(IC<2>{}, TT);
            f32x4 v[2], vb_[2];
#pragma unroll
            for (int m = 0; m < 2; ++m)
#pragma unroll
                for (int r = 0; r < 4; ++r) {
                    const float y = lnA[m][r] * acc[t][m][r] + lnB[m][r] * cs[t];
                    if (t < 2) v[m][r] = phi(y);
                    else if (t < 4) v[m][r] = y;
                    else { v[m][r] = n < 8 ? phi(y) : y; vb_[m][r] = n < 8 ? 1.f : y; }
                }
            if constexpr (t < 4) F[t] = pack8(v[0], v[1]);
            else { MA = pack8(v[0], v[1]); MB = pack8(vb_[0], vb_[1]); }
            SB();
        });
        f32x4 P1 = mfma(F[0], F[2], z4), P2 = mfma(F[0], MB, z4), P3 = mfma(MA, F[2], z4), P4 = mfma(MA, MB, z4);
        f32x4 P5 = mfma(F[1], F[3], z4), P6 = mfma(F[1], MB, z4), P7 = mfma(MA, F[3], z4);
        *(LAS f32x4*)(rda[0]) = P1;
        *(LAS f32x4*)(rda[0] + 1680) = P5;
        *(LAS f32x4*)(rda[1]) = P2;
        *(LAS f32x4*)(rda[2]) = P6;
        *(LAS f32x4*)(rda[3]) = (g == 0 ? P3 : P7);
        *(LAS f32x4*)(rda[4]) = P4;
    }
    BAR();
    if (tid < 210) {
        LAS char* red = (LAS char*)(lb + L_A + 20480 + tid * 16);
        f32x4 s = (*(LAS f32x4*)(red) + *(LAS f32x4*)(red + 3360)) + (*(LAS f32x4*)(red + 6720) + *(LAS f32x4*)(red + 10080));
        st16_sc1((unsigned)(tid * 16), ws + WS_KVP + (size_t)(j1 * 16 + i1) * 3360, __builtin_bit_cast(u32x4, s));
    }
    drain_bar();
    if (tid < 16) flag_put(ws, 0, tid * 16 + j1, i1, tag);
    if (tid == 16) __hip_atomic_store((GAS u64*)(ws + WS_GFLAG) + b, tag, RLX_AGENT);
    unsigned voff160[5];
    {
        int lane2 = lane; asm volatile("" : "+v"(lane2));
#pragma unroll
        for (int u = 0; u < 5; ++u) { int q = 64 * u + lane2, row = q / 10, s = q - row * 10; voff160[u] = row * 1280 + 160 * c8 + 16 * s; }
    }
    s16x8 kp[16];
    u64 gf[4];
    f32x4 aq[3][2];
    unsigned oa[12];
    const char* worun = wo + (size_t)w * (32 * 1280);
    {
        const unsigned vQ1 = lb + L_B + ro1 * 80 + 8 * p4, vQ2 = lb + L_B + ro2 * 80 + 8 * p4;
        const int to = (p4 == 0 ? 32 : p4 == 1 ? 72 : p4 == 2 ? 32 : 72);
        const unsigned vT1 = lb + L_B + ro1 * 80 + to, vT2 = lb + L_B + ro2 * 80 + to;
        const int to2 = (p4 & 1) ? 72 : 32;
        const unsigned vC1 = w < 2 ? vQ1 + 40 * w : vT1, vC2 = w < 2 ? vQ2 + 40 * w : vT2;
        (void)to2;
        f32x4 cq = z4;
#pragma unroll
        for (int t = 0; t < 3; ++t) aq[t][0] = aq[t][1] = z4;
        s16x4 Wl[2][4], Wh[2][4];
        auto side = [&](auto KS, auto I) {
            constexpr int ks = KS.v, i = I.v, r = ks & 1, t = i >> 1;
            constexpr unsigned ko = wq_pos(ks >> 1) + (ks & 1) * 2560;
            if constexpr (t < 2) { if constexpr (i & 1) Wh[r][t] = trread(vQ2 + ko + 40 * t); else Wl[r][t] = trread(vQ1 + ko + 40 * t); }
            else if constexpr (t == 2) { if constexpr (i & 1) Wh[r][2] = trread(vT2 + ko); else Wl[r][2] = trread(vT1 + ko); }
            else { if constexpr (i & 1) Wh[r][3] = trread(vC2 + ko); else Wl[r][3] = trread(vC1 + ko); }
        };
        sfor<0, 8>([&](auto I) { side(IC<0>{}, I); });
        SB();
        const char* kb = ws + WS_KVP + (size_t)j1 * 16 * 3360;
        const unsigned kvo = (tid < 210 ? tid : 209) * 16;
        {
            const unsigned kt = lb + L_KVT, zz = lb + L_ZERO;
            auto ka = [&](int head, int e, int c) { return kt + (head * 420 + e * 20 + 4 * c) * 2; };
            oa[0] = ka(0, n, g);  oa[1] = g == 0 ? ka(0, n, 4) : zz;
            oa[2] = ka(1, n, g);  oa[3] = g == 1 ? ka(1, n, 4) : zz;
            oa[4] = ka(0, 20, g); oa[5] = g == 0 ? ka(0, 20, 4) : zz;
            oa[6] = ka(1, 20, g); oa[7] = g == 1 ? ka(1, 20, 4) : zz;
            oa[8] = n < 4 ? ka(0, 16 + n, g) : zz;                oa[9] = (n < 4 && g == 0) ? ka(0, 16 + n, 4) : zz;
            oa[10] = (n >= 4 && n < 8) ? ka(1, 12 + n, g) : zz;   oa[11] = (n >= 4 && n < 8 && g == 1) ? ka(1, 12 + n, 4) : zz;
            asm volatile("" : "+v"(oa[0]), "+v"(oa[1]), "+v"(oa[2]), "+v"(oa[3]), "+v"(oa[4]), "+v"(oa[5]), "+v"(oa[6]), "+v"(oa[7]),
                         "+v"(oa[8]), "+v"(oa[9]), "+v"(oa[10]), "+v"(oa[11]));
        }
        sfor<0, 20>([&](auto KS) {
            constexpr int ks = KS.v, r = ks & 1;
            if constexpr (ks == 12) { await<63>(ws, 0, i1 * 16 + j1, tag, w, lane); SB(); }
            sfor<0, 8>([&](auto I) {
                constexpr int i = I.v;
                if constexpr (i < 6) { constexpr int t = i >> 1, m = i & 1; aq[t][m] = mfma(cat(Wl[r][t], Wh[r][t]), X[ks][m], aq[t][m]); }
                else if constexpr (i == 6) cq = mfma(ones, cat(Wl[r][3], Wh[r][3]), cq);
                if constexpr (ks < 19) side(IC<ks + 1>{}, I);
                if constexpr ((i == 2 || i == 6) && ks < 9) {
                    constexpr int pc_ = 2 * ks + (i == 6), tt = pc_ / 5, u = pc_ % 5;
                    glds16<tt * 20480 + u * 1024>(voff160[u], worun, ldsWw);
                    if constexpr (u == 4) { worun += 128 * 1280; OPAQUE_S(worun); }
                }
                if constexpr (ks == 18 && i < 4) ld8_sc1(gf[i], (unsigned)(lane * 8 + i * 512), ws + WS_GFLAG);
                if constexpr (ks >= 12 && ks < 18 && (i == 1 || i == 4 || i == 7) && 3 * (ks - 12) + i / 3 < 16) {
                    constexpr int pi = 3 * (ks - 12) + i / 3;
                    ld16_sc1(kp[pi], kvo + pi * 3360, kb);
                }
                SB();
            });
        });
        { const unsigned hi = pk2(cq[0], cq[0]); const float lo = cq[0] - bflo(hi);
          if (g == 0 && w < 3) *(LAS u32x2*)(lb + L_CSQ + w * 128 + n * 8) = u32x2{hi, pk2(lo, lo)}; }
    }

    bool gok;
    {
        asm volatile("s_waitcnt vmcnt(0)" : "+v"(kp[0]), "+v"(kp[1]), "+v"(kp[2]), "+v"(kp[3]), "+v"(kp[4]), "+v"(kp[5]), "+v"(kp[6]), "+v"(kp[7]),
                     "+v"(kp[8]), "+v"(kp[9]), "+v"(kp[10]), "+v"(kp[11]), "+v"(kp[12]), "+v"(kp[13]), "+v"(kp[14]), "+v"(kp[15]),
                     "+v"(gf[0]), "+v"(gf[1]), "+v"(gf[2]), "+v"(gf[3])::"memory");
        gok = __all(gf[0] == tag && gf[1] == tag && gf[2] == tag && gf[3] == tag);
        f32x4 s0 = __builtin_bit_cast(f32x4, kp[0]), s1 = __builtin_bit_cast(f32x4, kp[1]);
#pragma unroll
        for (int i = 2; i < 16; i += 2) { s0 += __builtin_bit_cast(f32x4, kp[i]); s1 += __builtin_bit_cast(f32x4, kp[i + 1]); }
        if (tid < 210) *(LAS u32x2*)(lb + L_KVT + tid * 8) = pack4(s0 + s1);
    }
    BAR();
    if (w < 2) sfor<0, 5>([&](auto U) { glds16<U.v * 1024>(voff160[U.v], x + (size_t)(8 * i2 + 4 * half + 2 * w) * 16 * 1280, lb + L_XRES + w * 5120); });
    {
        auto rd2 = [&](unsigned a0, unsigned a1) { const u32x2 lo = *(LAS u32x2*)a0, hi = *(LAS u32x2*)a1; return __builtin_bit_cast(s16x8, u32x4{lo[0], lo[1], hi[0], hi[1]}); };
        s16x8 Fa[2], Fb[2];
        {
            const unsigned csa = g == 0 ? lb + L_CSQ + n * 8 : lb + L_ZERO;
            s16x8 cf[3];
#pragma unroll
            for (int t = 0; t < 3; ++t) { const u32x2 c = *(LAS u32x2*)(csa + (g == 0 ? t * 128 : 0)); cf[t] = __builtin_bit_cast(s16x8, u32x4{c[0], c[1], 0u, 0u}); }
#pragma unroll
            for (int m = 0; m < 2; ++m) {
                const float nm = -((LAS float*)(lb + L_MEAN))[32 * w + 16 * m + n], rstd = ((LAS float*)(lb + L_RSTD))[32 * w + 16 * m + n];
                const unsigned hi = pk2(nm, nm);
                const unsigned pr = g == 0 ? ((hi & 0xffffu) | (pk2(nm - bflo(hi), 0.f) << 16)) : 0u;
                const s16x8 hm = __builtin_bit_cast(s16x8, u32x4{pr, pr, 0u, 0u});
                f32x4 v[3];
#pragma unroll
                for (int t = 0; t < 3; ++t) {
                    const f32x4 y = mfma(cf[t], hm, aq[t][m]) * rstd;
#pragma unroll
                    for (int r = 0; r < 4; ++r) v[t][r] = phi(y[r]);
                }
                const s16x8 q2 = pack8(v[0], v[2]), q3 = pack8(v[1], v[2]);
                u32x4 u2 = __builtin_bit_cast(u32x4, q2), u3 = __builtin_bit_cast(u32x4, q3);
                if (g >= 2) { u2[2] = u2[3] = 0u; u3[2] = u3[3] = 0u; }
                Fa[m] = __builtin_bit_cast(s16x8, u2); Fb[m] = __builtin_bit_cast(s16x8, u3);
            }
        }
        const s16x8 Ama = rd2(oa[0], oa[1]), Amb = rd2(oa[2], oa[3]), Aza = rd2(oa[4], oa[5]), Azb = rd2(oa[6], oa[7]), Ata = rd2(oa[8], oa[9]), Atb = rd2(oa[10], oa[11]);
#pragma unroll
        for (int m = 0; m < 2; ++m) {
            f32x4 Da = mfma(Ama, Fa[m], z4), Db = mfma(Amb, Fb[m], z4);
            f32x4 Dt = mfma(Atb, Fb[m], mfma(Ata, Fa[m], z4));
            f32x4 Dza = mfma(Aza, Fa[m], z4), Dzb = mfma(Azb, Fb[m], z4);
            const float ra = __builtin_amdgcn_rcpf(Dza[0] + 1e-6f), rb = __builtin_amdgcn_rcpf(Dzb[0] + 1e-6f);
            const int mt = 8 * i1 + 2 * w + m;
            s16x8 of = pack8(Da * ra, Db * rb);
            st16_sc1((unsigned)((mt * 20 + j1) * 1024 + lane * 16), ws + WS_O, __builtin_bit_cast(u32x4, of));
            u32x2 tl = pack4(Dt * (g == 0 ? ra : rb));
            const int t = j1 >> 1;
            if (g < 2) st8_sc1((unsigned)((mt * 20 + 16 + (t >> 1)) * 1024 + (n + 16 * (2 * (j1 & 1) + g)) * 16 + (t & 1) * 8), ws + WS_O, tl);
        }
    }
    drain_bar();
    if (tid < 16) flag_put(ws, 1, i1 * 16 + tid, j1, tag);
    const int T0 = j2 < 12 ? 7 * j2 : 84 + 6 * (j2 - 12);
    const int odd = (j2 < 12) ? (j2 & 1) : 0;
    const int Tp0 = T0 + odd;
    const int Tsg = j2 < 12 ? (odd ? T0 : T0 + 6) : Tp0;
    const unsigned vlan16 = lane * 16;
    const int ksb = w < 2 ? 14 * w : 28 + 13 * (w - 2);
    const int nks = w < 2 ? 14 : 13;
    s16x8 V[14][5];
    const char* w2run = ws + WS_W2R + (size_t)(c8 * 54 + ksb) * 5120;
    const int step13 = nks == 14 ? 5120 : 0;
#define ISSUE_V(it, t) do { if constexpr ((it) < 8) ld16a_sc1(V[it][t], vlan16 + (t) * 1024, w2run); else ld16_sc1(V[it][t], vlan16 + (t) * 1024, w2run); \
            if ((t) == 4) { w2run += ((it) == 12 ? step13 : 5120); OPAQUE_S(w2run); } } while (0)
    if (w == 3 && gok) {
#pragma unroll
        for (int k = 0; k < 5; ++k) {
            int li = k * 64 + lane; li = li < 280 ? li : 279; li += rr * 280; li = li < ((x8 & 1) ? 8320 : 8960) ? li : 8319;
            glds4((unsigned)(li * 128), ws + WS_W1R + (x8 & 1) * 56 * 20480, lb + L_JUNK);
        }
    }
    sfor<18, 25>([&](auto P) {
        constexpr int tt = P.v / 5, u = P.v % 5;
        glds16<tt * 20480 + u * 1024>(voff160[u], worun, ldsWw);
        if constexpr (u == 4) { worun += 128 * 1280; OPAQUE_S(worun); }
    });
    const int mtW = 8 * i2 + 4 * half + w;
    await<0>(ws, 1, i2 * 16 + jw, tag, w, lane);
    {
        s16x8 O[20];
        unsigned orun = (unsigned)(mtW * 20 * 1024 + lane * 16);
#define ISSUE_O(i) do { ld16_sc1(O[i], orun, ws + WS_O); orun += 1024u; asm volatile("" : "+v"(orun)); } while (0)
        sfor<0, 8>([&](auto I) { ISSUE_O(I.v); });
        unsigned vA = lb + L_A + (4 * g + q4) * 160 + 8 * p4, vA2 = vA + 8 * 6400;
        const unsigned vB = lb + L_A + (20 * g + 16 + q4) * 160 + 8 * p4;
        unsigned vB2 = vB + 2 * 25600;
        asm volatile("" : "+v"(vA), "+v"(vA2), "+v"(vB2));
        f32x4 a[5];
#pragma unroll
        for (int t = 0; t < 5; ++t) a[t] = z4;
        s16x4 Wl[3][5], Wh[3][5];
        auto side = [&](auto I, auto J) {
            constexpr int i = I.v, j = J.v, r = i % 3, t = j >> 1;
            if constexpr (i < 16) {
                const unsigned a_ = (i < 8 ? vA : vA2) + (i & 7) * 6400 + 32 * t;
                if constexpr (j & 1) Wh[r][t] = trread(a_ + 3200); else Wl[r][t] = trread(a_);
            } else {
                const unsigned a_ = (i < 18 ? vB : vB2) + (i & 1) * 25600 + 32 * t;
                if constexpr (j & 1) Wh[r][t] = trread(a_ + 12800); else Wl[r][t] = trread(a_);
            }
        };
        sfor<0, 2>([&](auto I) { sfor<0, 10>([&](auto J) { side(I, J); }); });
        SB();
        sfor<0, 20>([&](auto I) {
            constexpr int i = I.v;
            constexpr int ny = ((i + 7 < 19 ? i + 7 : 19) - i) + (i > 12 ? (i < 17 ? 2 * (i - 12) : 10) : 0) + (i == 18 ? 1 : i == 19 ? 2 : 0);
            asm volatile("s_waitcnt vmcnt(%1)" : "+v"(O[i]) : "i"(ny) : "memory");
            SB();
            sfor<0, 5>([&](auto T) {
                constexpr int t = T.v;
                a[t] = mfma(cat(Wl[i % 3][t], Wh[i % 3][t]), O[i], a[t]);
                if constexpr (i + 2 < 20) { side(IC<i + 2>{}, IC<2 * t>{}); side(IC<i + 2>{}, IC<2 * t + 1>{}); }
                if constexpr (t == 1 && i + 8 < 20) ISSUE_O(i + 8);
                if constexpr ((i == 17 || i == 18) && t == 1) {
                    int li = w * 68 + (i == 17 ? lane : 64 + (lane < 4 ? lane : 0)); li = li < 270 ? li : 269;
                    glds4((unsigned)((rr * 270 + li) * 128), ws + WS_W2R + (size_t)(x8 & 1) * 4 * 54 * 5120, lb + L_JUNK);
                }
                if constexpr (i >= 12 && i < 17 && (t == 1 || t == 3)) {
                    constexpr int bg = 2 * (i - 12) + (t == 3);
                    const int Tg = bg < 5 ? Tp0 + 5 : Tsg;
                    glds16c<(5 + bg / 5) * 20480 + (bg % 5) * 4096>(vlan16, ws + WS_W1R + (size_t)(Tg * 20 + (bg % 5) * 4 + w) * 1024, lb + w * 1024);
                }
                SB();
            });
        });
        float su = 0.f, s2 = 0.f;
        u32x2 hpk[5];
#pragma unroll
        for (int t = 0; t < 5; ++t) {
            const u32x2 xr = *(LAS u32x2*)(lb + L_XRES + (16 * w + n) * 160 + 32 * t + 8 * g);
            const f32x4 hv = f32x4{bflo(xr[0]), bfhi(xr[0]), bflo(xr[1]), bfhi(xr[1])} + a[t];
            hpk[t] = pack4(hv);
            *(LAS u32x2*)(lb + L_XRES + (16 * w + n) * 160 + 32 * t + 8 * g) = hpk[t];
#pragma unroll
            for (int r = 0; r < 4; ++r) { su += hv[r]; s2 += hv[r] * hv[r]; }
        }
        const unsigned hb = (unsigned)((mtW * 20 + ((5 * c8) >> 1)) * 1024 + lane * 16);
        const int od = c8 & 1;
        st8_sc1(hb + (od ? 8 : 2048), ws + WS_H, od ? hpk[0] : hpk[4]);
        { const u32x2 p0 = od ? hpk[1] : hpk[0], p1 = od ? hpk[2] : hpk[1]; st16_sc1(hb + (od ? 1024 : 0), ws + WS_H, u32x4{p0[0], p0[1], p1[0], p1[1]}); }
        { const u32x2 p0 = od ? hpk[3] : hpk[2], p1 = od ? hpk[4] : hpk[3]; st16_sc1(hb + (od ? 2048 : 1024), ws + WS_H, u32x4{p0[0], p0[1], p1[0], p1[1]}); }
        su = add_xor32(add_xor16(su)); s2 = add_xor32(add_xor16(s2));
        if (g == 0) { u32x2 st = {__float_as_uint(su), __float_as_uint(s2)}; st8_sc1((unsigned)((mtW * 16 + n) * 64 + c8 * 8), ws + WS_STAT2, st); }
    }
    drain_bar();
    if (tid < 16) flag_put(ws, 2, i2 * 16 + tid, jw, tag);
    if (b == 0 && tid == 0) __hip_atomic_store((GAS u64*)ws, tag, RLX_AGENT);
    {
        const char* s1 = ws + WS_W1R + (size_t)(Tp0 * 20 + w) * 1024;
        const unsigned l1 = lb + L_A + w * 1024;
        sfor<0, 25>([&](auto U) { glds16c<U.v * 4096>(vlan16, s1, l1); s1 += 4096; OPAQUE_S(s1); });
    }
    await<0>(ws, 2, i2 * 16 + j2, tag, w, lane);
    {
        s16x8 H[20][2];
        const int mt0 = 8 * i2 + 2 * w;
        s16x8 stv[2];
#pragma unroll
        for (int m = 0; m < 2; ++m) ld16_sc1(stv[m], (unsigned)(((mt0 + m) * 16 + n) * 64 + g * 16), ws + WS_STAT2);
        unsigned hrun = (unsigned)(mt0 * 20 * 1024 + lane * 16);
#define ISSUE_H(ks) do { ld16_sc1(H[ks][0], hrun, ws + WS_H); ld16_sc1(H[ks][1], hrun + 20 * 1024, ws + WS_H); hrun += 1024; asm volatile("" : "+v"(hrun)); } while (0)
        unsigned vF0 = lb + vlan16, vF1 = lb + 61440 + vlan16, vF2 = lb + 122880 + vlan16;
        asm volatile("" : "+v"(vF0), "+v"(vF1), "+v"(vF2));
        auto wread = [&](auto TT, auto KS) -> s16x8 {
            constexpr int t = TT.v, ks = KS.v;
            return *(volatile LAS s16x8*)((t < 3 ? vF0 : t < 6 ? vF1 : vF2) + (t % 3) * 20480 + ks * 1024);
        };
        {
            f32x4 c0 = z4, c1 = z4;
            const unsigned ta = lb + vlan16 + w * 20480, tb = lb + vlan16 + (w < 3 ? w + 4 : w) * 20480;
            s16x8 Ca[4], Cb[4];
            sfor<0, 3>([&](auto K) { Ca[K.v] = *(volatile LAS s16x8*)(ta + K.v * 1024); Cb[K.v] = *(volatile LAS s16x8*)(tb + K.v * 1024); });
            SB();
            sfor<0, 20>([&](auto K) {
                constexpr int k = K.v;
                c0 = mfma(ones, Ca[k & 3], c0);
                if constexpr (k + 3 < 20) Ca[(k + 3) & 3] = *(volatile LAS s16x8*)(ta + (k + 3) * 1024);
                SB();
                c1 = mfma(ones, Cb[k & 3], c1);
                if constexpr (k + 3 < 20) Cb[(k + 3) & 3] = *(volatile LAS s16x8*)(tb + (k + 3) * 1024);
                if constexpr ((k & 1) == 0 && k < 16) ISSUE_H(k >> 1);
                SB();
            });
            auto split = [&](float c) { const unsigned hi = pk2(c, c); const float lo = c - bflo(hi); u32x2 r = {hi, pk2(lo, lo)}; return r; };
            if (g == 0) {
                *(LAS u32x2*)(lb + L_CS1 + w * 128 + n * 8) = split(c0[0]);
                if (w < 3) *(LAS u32x2*)(lb + L_CS1 + (w + 4) * 128 + n * 8) = split(c1[0]);
            }
            if (tid < 2) *(LAS u64*)(lb + L_CS1 + 896 + tid * 8) = 0;
        }
        asm volatile("s_waitcnt vmcnt(16)" : "+v"(stv[0]), "+v"(stv[1])::"memory");
        s16x8 Hm[2];
#pragma unroll
        for (int m = 0; m < 2; ++m) {
            f32x4 s = __builtin_bit_cast(f32x4, stv[m]);
            const float nm = add_xor32(add_xor16(s[0] + s[2])) * (-1.f / 640.f);
            const unsigned hi = pk2(nm, nm);
            const unsigned pr = g == 0 ? ((hi & 0xffffu) | (pk2(nm - bflo(hi), 0.f) << 16)) : 0u;
            Hm[m] = __builtin_bit_cast(s16x8, u32x4{pr, pr, 0u, 0u});
        }
        s16x8 Wf1[3][4];
        sfor<0, 2>([&](auto KS) { sfor<0, 4>([&](auto TT) { Wf1[KS.v][TT.v] = wread(TT, KS); }); });
        __builtin_amdgcn_s_waitcnt(0xC87F);
        BARX();
        const unsigned csa = lb + L_CS1 + (g == 0 ? n * 8 : 896);
        auto csfrag = [&](int t) { const u32x2 c = *(LAS u32x2*)(csa + (g == 0 ? t * 128 : 0)); return __builtin_bit_cast(s16x8, u32x4{c[0], c[1], 0u, 0u}); };
        auto relu2 = [&](unsigned p) { typedef short s2_ __attribute__((ext_vector_type(2))); const s2_ z = {0, 0};
                                       return __builtin_bit_cast(unsigned, __builtin_elementwise_max(__builtin_bit_cast(s2_, p), z)); };
        auto fin8 = [&](f32x4 x0, f32x4 x1) { return u32x4{relu2(pk2(x0[0], x0[1])), relu2(pk2(x0[2], x0[3])), relu2(pk2(x1[0], x1[1])), relu2(pk2(x1[2], x1[3]))}; };
        f32x4 a1[4][2];
        sfor<0, 4>([&](auto TT) { const s16x8 cf = csfrag(TT.v); a1[TT.v][0] = mfma(cf, Hm[0], z4); a1[TT.v][1] = mfma(cf, Hm[1], z4); });
        {
            auto& Wf = Wf1;
            SB();
            sfor<0, 20>([&](auto KS) {
                constexpr int ks = KS.v;
                constexpr int ny = 2 * ((ks + 7 < 19 ? ks + 7 : 19) - ks) + (ks > 12 ? (ks < 17 ? 2 * (ks - 12) : 10) : 0);
                asm volatile("s_waitcnt vmcnt(%2)" : "+v"(H[ks][0]), "+v"(H[ks][1]) : "i"(ny) : "memory");
                SB();
                sfor<0, 8>([&](auto I) {
                    constexpr int i = I.v, t = i >> 1, m = i & 1;
                    a1[t][m] = mfma(Wf[ks % 3][t], H[ks][m], a1[t][m]);
                    if constexpr (ks + 2 < 20 && (i & 1) == 0) Wf[(ks + 2) % 3][t] = wread(IC<t>{}, IC<ks + 2>{});
                    if constexpr (i == 3 && ks + 8 < 20) ISSUE_H(ks + 8);
                    if constexpr (ks >= 12 && ks < 17 && (i == 1 || i == 5)) { constexpr int pv = 2 * (ks - 12) + (i == 5); ISSUE_V(pv / 5, pv % 5); }
                    SB();
                });
            });
        }
        f32x4 a2[3][2];
        sfor<0, 3>([&](auto TT) { const s16x8 cf = csfrag(4 + TT.v); a2[TT.v][0] = mfma(cf, Hm[0], z4); a2[TT.v][1] = mfma(cf, Hm[1], z4); });
        {
            s16x8 Wf[3][3];
            sfor<0, 2>([&](auto KS) { sfor<0, 3>([&](auto TT) { Wf[KS.v][TT.v] = wread(IC<4 + TT.v>{}, KS); }); });
            u32x2 e0, e1;
            sfor<0, 20>([&](auto KS) {
                constexpr int ks = KS.v;
                sfor<0, 6>([&](auto I) {
                    constexpr int i = I.v, t = i >> 1, m = i & 1;
                    a2[t][m] = mfma(Wf[ks % 3][t], H[ks][m], a2[t][m]);
                    if constexpr (ks + 2 < 20 && (i & 1) == 0) Wf[(ks + 2) % 3][t] = wread(IC<4 + t>{}, IC<ks + 2>{});
                    if constexpr (i == 3 || (i == 5 && (ks & 1) == 0)) { constexpr int pv = 10 + (ks >> 1) * 3 + ((ks & 1) ? 2 : (i == 5 ? 1 : 0)); ISSUE_V(pv / 5, pv % 5); }
                    if constexpr (ks >= 1 && ks < 17 && ((ks - 1) & 3) < 3) {
                        constexpr int u = (ks - 1) >> 2, ph = (ks - 1) & 3, P = u >> 1, mm = u & 1;
                        if constexpr (ph == 0 && i == 1) e0 = u32x2{relu2(pk2(a1[2 * P][mm][0], a1[2 * P][mm][1])), relu2(pk2(a1[2 * P][mm][2], a1[2 * P][mm][3]))};
                        if constexpr (ph == 1 && i == 1) e1 = u32x2{relu2(pk2(a1[2 * P + 1][mm][0], a1[2 * P + 1][mm][1])), relu2(pk2(a1[2 * P + 1][mm][2], a1[2 * P + 1][mm][3]))};
                        if constexpr (ph == 2 && i == 1)
                            st16_sc1((unsigned)(((mt0 + mm) * 54 + (Tp0 >> 1) + P) * 1024 + lane * 16), ws + WS_H1, u32x4{e0[0], e0[1], e1[0], e1[1]});
                    }
                    SB();
                });
            });
        }
#pragma unroll
        for (int m = 0; m < 2; ++m) {
            st16_sc1((unsigned)(((mt0 + m) * 54 + (Tp0 >> 1) + 2) * 1024 + lane * 16), ws + WS_H1, fin8(a2[0][m], a2[1][m]));
            if (j2 < 12) st8_sc1((unsigned)(((mt0 + m) * 54 + (Tsg >> 1)) * 1024 + lane * 16 + (Tsg & 1) * 8), ws + WS_H1,
                                 u32x2{relu2(pk2(a2[2][m][0], a2[2][m][1])), relu2(pk2(a2[2][m][2], a2[2][m][3]))});
        }
        drain_bar();
        if (tid < 16) flag_put(ws, 3, i2 * 16 + tid, j2, tag);
        sfor<40, 60>([&](auto U) { ISSUE_V(U.v / 5, U.v % 5); });
        await<63>(ws, 3, i2 * 16 + jw, tag, w, lane);
        unsigned arun = (unsigned)(((8 * i2 + 4 * half) * 54 + ksb) * 1024 + lane * 16);
        const unsigned astep13 = nks == 14 ? 1024u : 0u;
        s16x8 A[4][4];
        unsigned aoff[4];
#pragma unroll
        for (int m = 0; m < 4; ++m) aoff[m] = ((w + m) & 3) * 54 * 1024;
        s16x8 st2v;
        ld16_sc1(st2v, (unsigned)((mtW * 16 + n) * 64 + g * 16), ws + WS_STAT2);
#define ISSUE_A(it, m) do { ld16_sc1(A[(it) & 3][m], arun + aoff[m], ws + WS_H1); \
            if ((m) == 3) { arun += ((it) == 12 ? astep13 : 1024u); asm volatile("" : "+v"(arun)); } } while (0)
        sfor<0, 3>([&](auto IT) { sfor<0, 4>([&](auto M) { ISSUE_A(IT.v, M.v); }); });
        f32x4 a[4][5];
#pragma unroll
        for (int m = 0; m < 4; ++m)
#pragma unroll
            for (int t = 0; t < 5; ++t) a[m][t] = z4;
        sfor<0, 14>([&](auto IT) {
            constexpr int it = IT.v;
            constexpr int ny = [] {
                int seq[200] = {}, n_ = 0;
                for (int a_ = 0; a_ < 3; ++a_) for (int m = 0; m < 4; ++m) seq[n_++] = 1000 + a_;
                for (int j = 0; j < it; ++j)
                    for (int i = 0; i < 20; ++i) {
                        if ((i & 3) == 1 && i < 16 && j + 3 < 14) seq[n_++] = 1000 + j + 3;
                        if ((i == 3 || i == 11) && j < 5) seq[n_++] = 2000 + (60 + 2 * j + (i == 11)) / 5;
                    }
                int need = 0;
                for (int q = 0; q < n_; ++q) if (seq[q] == 1000 + it || seq[q] == 2000 + it) need = q + 1;
                return n_ - need;
            }();
            if constexpr (it < 8)
                asm volatile("s_waitcnt vmcnt(%9)" : "+v"(A[it & 3][0]), "+v"(A[it & 3][1]), "+v"(A[it & 3][2]), "+v"(A[it & 3][3]),
                             "+a"(V[it][0]), "+a"(V[it][1]), "+a"(V[it][2]), "+a"(V[it][3]), "+a"(V[it][4]) : "i"(ny) : "memory");
            else
                asm volatile("s_waitcnt vmcnt(%9)" : "+v"(A[it & 3][0]), "+v"(A[it & 3][1]), "+v"(A[it & 3][2]), "+v"(A[it & 3][3]),
                             "+v"(V[it][0]), "+v"(V[it][1]), "+v"(V[it][2]), "+v"(V[it][3]), "+v"(V[it][4]) : "i"(ny) : "memory");
            SB();
            if (it < 13 || nks == 14) {
                sfor<0, 20>([&](auto I) {
                    constexpr int i = I.v, t = i >> 2, m = i & 3;
                    a[m][t] = mfma(V[it][t], A[it & 3][m], a[m][t]);
                    if constexpr ((i & 3) == 1 && i < 16 && it + 3 < 14) ISSUE_A(it + 3, i >> 2);
                    if constexpr ((i == 3 || i == 11) && it < 5) { constexpr int pv = 60 + 2 * it + (i == 11); ISSUE_V(pv / 5, pv % 5); }
                    SB();
                });
            }
        });
        asm volatile("" : "+v"(st2v));
        const f32x4 sv = __builtin_bit_cast(f32x4, st2v);
        const float smean = add_xor32(add_xor16(sv[0] + sv[2])) * (1.f / 640.f);
        const float rstd2 = __builtin_amdgcn_rsqf(fmaxf(add_xor32(add_xor16(sv[1] + sv[3])) * (1.f / 640.f) - smean * smean, 0.f) + 1e-5f);
#pragma unroll
        for (int m = 1; m < 4; ++m)
#pragma unroll
            for (int t = 0; t < 5; ++t) *(LAS f32x4*)(lb + ((((w + m) & 3) * 3 + (m - 1)) * 5 + t) * 1024 + lane * 16) = a[m][t];
        BAR();
        f32x4 fin[5];
#pragma unroll
        for (int t = 0; t < 5; ++t) {
            const LAS char* p = (const LAS char*)(lb + (w * 15 + t) * 1024 + lane * 16);
            fin[t] = ((a[0][t] + *(const LAS f32x4*)p) + (*(const LAS f32x4*)(p + 5120) + *(const LAS f32x4*)(p + 10240))) * rstd2;
        }
        LAS char* ot = (LAS char*)(lb + L_B + w * 2560);
#pragma unroll
        for (int t = 0; t < 5; ++t) {
            const u32x2 hp = *(LAS u32x2*)(lb + L_XRES + (16 * w + n) * 160 + 32 * t + 8 * g);
            const f32x4 ov = f32x4{bflo(hp[0]), bfhi(hp[0]), bflo(hp[1]), bfhi(hp[1])} + fin[t];
            *(LAS u32x2*)(ot + n * 160 + 32 * t + 8 * g) = pack4(ov);
        }
        asm volatile("s_waitcnt lgkmcnt(0)" ::: "memory");
        int lane3 = lane; asm volatile("" : "+v"(lane3));
#pragma unroll
        for (int u = 0; u < 3; ++u) {
            const int q = 64 * u + lane3, row = q / 10, sl = q - row * 10;
            if (q < 160) *(GAS u32x4*)(out + (size_t)(mtW * 16 + row) * 1280 + 160 * c8 + 16 * sl) = *(LAS u32x4*)(ot + q * 16);
        }
    }
}

extern "C" void kernel_launch(void* const* d_in, const int* in_sizes, int n_in, void* d_out, int out_size, void* d_ws, size_t ws_size,
                              hipStream_t stream) {
    (void)in_sizes; (void)n_in; (void)out_size; (void)ws_size;
    fused_block<<<256, 256, 0, stream>>>((const char*)d_in[0], (const char*)d_in[1], (const char*)d_in[2], (const char*)d_in[4],
                                         (const char*)d_in[5], (char*)d_out, (char*)d_ws);
}
```
